```python
import torch

def _softmax(x, axis=-1):
    return torch.softmax(x, dim=axis)
def _gelu(x):
    return torch.nn.functional.gelu(x, approximate="tanh")
def _silu(x):
    return torch.nn.functional.silu(x)
def _sigmoid(x):
    return torch.sigmoid(x)
def _layernorm(x, axis=-1, eps=1e-5):
    m = x.mean(dim=axis, keepdim=True)
    v = x.var(dim=axis, keepdim=True, unbiased=False)
    return (x - m) / torch.sqrt(v + eps)
def _scatter_add(tgt, idx, src, axis=0):
    return tgt.clone().index_add_(axis, idx, src)
def _segment_sum(src, seg, n):
    out = torch.zeros((n,) + src.shape[1:], dtype=src.dtype)
    return out.index_add_(0, seg, src)
def _pad(x, pw):
    flat = tuple(v for pair in reversed(pw) for v in pair)
    return torch.nn.functional.pad(x, flat)

kernel_name = 'standalone__mha_block_cached__sampling'

def setup_inputs():

    _w = (lambda a: (lambda t: t.to(torch.bfloat16) if t.is_floating_point() else t)(torch.as_tensor(a)))
    B, Sq, D = 16, 8, 1024
    H, Sk, Dh = 32, 2048, 128
    return {
        "x": _w((lambda *s: torch.randn(*s, dtype=torch.bfloat16) * 0.8)(B, Sq, D)),
        "k_cache": _w((lambda *s: torch.randn(*s, dtype=torch.bfloat16) * 0.8)(B, H, Sk, Dh)),
        "v_cache": _w((lambda *s: torch.randn(*s, dtype=torch.bfloat16) * 0.05)(B, H, Sk, Dh)),
        "wq": _w((lambda *s: torch.randn(*s, dtype=torch.bfloat16) * 0.8)(D, H * Dh) * (D ** -0.5)),
        "wo": _w((lambda *s: torch.randn(*s, dtype=torch.bfloat16) * 0.05)(H * Dh, D) * ((H * Dh) ** -0.5)),
    }

def reference(x, k_cache, v_cache, wq, wo):
    B, Sq, D = x.shape
    H, Dh = 32, 128
    q = (lambda x, *d: x.permute(*d))((x @ wq).reshape(B, Sq, H, Dh), 0, 2, 1, 3)
    scores = torch.einsum("bhqd,bhkd->bhqk", q, k_cache) * (Dh ** -0.5)
    p = _softmax(scores, axis=-1)
    o = torch.einsum("bhqk,bhkd->bhqd", p, v_cache)
    return (lambda x, *d: x.permute(*d))(o, 0, 2, 1, 3).reshape(B, Sq, H * Dh) @ wo
```

```cpp
#include <hip/hip_runtime.h>
#include <hip/hip_bf16.h>

#define GAS __attribute__((address_space(1)))
#define LAS __attribute__((address_space(3)))
#define RLX_AGENT __ATOMIC_RELAXED, __HIP_MEMORY_SCOPE_AGENT
typedef unsigned v4u __attribute__((ext_vector_type(4)));
typedef unsigned v2u __attribute__((ext_vector_type(2)));
typedef float f32x4 __attribute__((ext_vector_type(4)));
typedef __bf16 bf16x8 __attribute__((ext_vector_type(8)));
typedef __bf16 bf16x2 __attribute__((ext_vector_type(2)));
typedef float f32x2 __attribute__((ext_vector_type(2)));
typedef GAS unsigned long long gu64;

namespace {

constexpr int NB = 16, SQ = 8, DM = 1024, NH = 32, SK = 2048, DH = 128;
constexpr int NWG = 256, NTHR = 512;
constexpr int NT = 16;
constexpr float CEXP = 0.08838834764831845f * 1.4426950408889634f;
constexpr int AUX_NT = 2, AUX_SC1 = 16;
constexpr int RSRC_W3 = 0x00020000;
constexpr unsigned SENT = 0xFFFFFFFFu;
constexpr unsigned kSpinCap = 1u << 20;

constexpr int WS_EPOCH = 0;
constexpr int WS_FLAG = 4096;
constexpr int WS_O = 65536;
constexpr int WS_BYTES = WS_O + 8 * 128 * 1024;

constexpr int L_WAVE = 16384;
constexpr int L_QS = 8 * L_WAVE;
constexpr int QS_STRIDE = 272;
constexpr int L_BYTES = L_QS + 16 * QS_STRIDE;
constexpr int QP_STRIDE = 528;
constexpr int L_ML = 8 * QP_STRIDE;
constexpr int L_RED = 8192;

__device__ __forceinline__ f32x4 mfma16(v4u a, v4u b, f32x4 c) {
  return __builtin_amdgcn_mfma_f32_16x16x32_bf16(__builtin_bit_cast(bf16x8, a), __builtin_bit_cast(bf16x8, b), c, 0, 0, 0);
}
__device__ __forceinline__ unsigned pack_lo(unsigned a, unsigned b) { return __builtin_amdgcn_perm(b, a, 0x05040100u); }
__device__ __forceinline__ unsigned pack_hi(unsigned a, unsigned b) { return __builtin_amdgcn_perm(b, a, 0x07060302u); }
__device__ __forceinline__ unsigned cvt_pk(float lo, float hi) {
  f32x2 v = {lo, hi};
  return __builtin_bit_cast(unsigned, __builtin_convertvector(v, bf16x2));
}
template <int E> __device__ __forceinline__ v4u tr_frag(const v4u (&R)[8]) {
  v4u f;
#pragma unroll
  for (int jj = 0; jj < 4; ++jj)
    f[jj] = (E & 1) ? pack_hi(R[2 * jj][E >> 1], R[2 * jj + 1][E >> 1]) : pack_lo(R[2 * jj][E >> 1], R[2 * jj + 1][E >> 1]);
  return f;
}
__device__ __forceinline__ float xg_max(float x) {
  auto s = __builtin_amdgcn_permlane16_swap(__float_as_uint(x), __float_as_uint(x), false, false);
  x = fmaxf(__uint_as_float(s[0]), __uint_as_float(s[1]));
  auto t = __builtin_amdgcn_permlane32_swap(__float_as_uint(x), __float_as_uint(x), false, false);
  return fmaxf(__uint_as_float(t[0]), __uint_as_float(t[1]));
}
__device__ __forceinline__ float xg_sum(float x) {
  auto s = __builtin_amdgcn_permlane16_swap(__float_as_uint(x), __float_as_uint(x), false, false);
  x = __uint_as_float(s[0]) + __uint_as_float(s[1]);
  auto t = __builtin_amdgcn_permlane32_swap(__float_as_uint(x), __float_as_uint(x), false, false);
  return __uint_as_float(t[0]) + __uint_as_float(t[1]);
}
#define SB() __builtin_amdgcn_sched_barrier(0)

__global__ void __launch_bounds__(NTHR, 2)
mha_fused(const unsigned short* __restrict__ x, const unsigned short* __restrict__ kc, const unsigned short* __restrict__ vc,
          const unsigned short* __restrict__ wq, const unsigned short* __restrict__ wo, unsigned short* __restrict__ out,
          unsigned char* __restrict__ ws) {
  extern __shared__ __attribute__((aligned(16))) unsigned char smem[];
  LAS unsigned char* L = (LAS unsigned char*)smem;
  const int tid = threadIdx.x, lane = tid & 63, w = __builtin_amdgcn_readfirstlane(tid >> 6);
  const int r = lane & 15, g = lane >> 4;
  const int wg = blockIdx.x, idx = wg >> 3;
  const int h = 4 * (wg & 7) + (idx & 3), bp = idx >> 2;
  const int beta = w >> 2, ws4 = w & 3;
  const int bh = (2 * bp + beta) * NH + h;
  const int rot = (bh * 5) & (NT - 1);
  LAS unsigned char* LW = L + w * L_WAVE;

  const __amdgpu_buffer_rsrc_t rk = __builtin_amdgcn_make_buffer_rsrc((void*)(kc + (size_t)bh * SK * DH), 0, SK * DH * 2, RSRC_W3);
  const __amdgpu_buffer_rsrc_t rv = __builtin_amdgcn_make_buffer_rsrc((void*)(vc + (size_t)bh * SK * DH), 0, SK * DH * 2, RSRC_W3);
  const __amdgpu_buffer_rsrc_t rws = __builtin_amdgcn_make_buffer_rsrc((void*)ws, 0, WS_BYTES, RSRC_W3);
  const __amdgpu_buffer_rsrc_t rwq = __builtin_amdgcn_make_buffer_rsrc((void*)wq, 0, DM * NH * DH * 2, RSRC_W3);
  const __amdgpu_buffer_rsrc_t rwo = __builtin_amdgcn_make_buffer_rsrc((void*)wo, 0, NH * DH * DM * 2, RSRC_W3);
  const __amdgpu_buffer_rsrc_t rx = __builtin_amdgcn_make_buffer_rsrc((void*)x, 0, NB * SQ * DM * 2, RSRC_W3);

  const v2u epoch0 = __builtin_amdgcn_raw_buffer_load_b64(rws, 0, WS_EPOCH + bp * 256, AUX_SC1);
  const int o_slot = WS_O + (bp * 128 + 4 * h) * 1024;
  if (w < 4) __builtin_amdgcn_raw_buffer_store_b128(v4u{SENT, SENT, SENT, SENT}, rws, lane * 16, o_slot + w * 1024, AUX_SC1);
  asm volatile("" ::: "memory");
  SB();

  const int koff = (4 * (r & 3) + (r >> 2)) * 256 + g * 16;
  const int voff = lane * 16;
  v4u ring[3][8];
#define TILE_OFF(t) (((((t) + rot) & (NT - 1)) * 4 + ws4) * 8192)
#define LOAD_K(slot, t) { const int so_ = TILE_OFF(t); _Pragma("unroll") for (int i_ = 0; i_ < 8; ++i_) \
    ring[slot][i_] = __builtin_amdgcn_raw_buffer_load_b128(rk, koff, so_ + (i_ & 3) * 64 + (i_ >> 2) * 4096, AUX_NT); }
#define LOAD_V(slot, t) { const int so_ = TILE_OFF(t); _Pragma("unroll") for (int i_ = 0; i_ < 8; ++i_) \
    ring[slot][i_] = __builtin_amdgcn_raw_buffer_load_b128(rv, voff, so_ + i_ * 1024, AUX_NT); }
#define LOAD_ITEM(n) { if ((n) < 2 * NT) { if ((n) & 1) LOAD_V((n) % 3, (n) >> 1) else LOAD_K((n) % 3, (n) >> 1) } }

  {
    v4u xf[4];
    const int xoff = ((16 * bp + r) * DM + 128 * w + 8 * g) * 2;
#pragma unroll
    for (int s = 0; s < 4; ++s) xf[s] = __builtin_amdgcn_raw_buffer_load_b128(rx, xoff + s * 64, 0, 0);
    const int wqoff = (8 * g) * (NH * DH * 2) + r * 16;
    const int wqs = (128 * w) * (NH * DH * 2) + h * (DH * 2);
    v4u wa[3][8];
#define LOAD_WQ(slot, s) { _Pragma("unroll") for (int j_ = 0; j_ < 8; ++j_) \
      wa[slot][j_] = __builtin_amdgcn_raw_buffer_load_b128(rwq, wqoff, wqs + (32 * (s) + j_) * (NH * DH * 2), 0); }
    f32x4 qa[8];
#pragma unroll
    for (int e = 0; e < 8; ++e) qa[e] = f32x4{0.f, 0.f, 0.f, 0.f};
#define MMA_WQ(slot, s) { \
      qa[0] = mfma16(tr_frag<0>(wa[slot]), xf[s], qa[0]); qa[1] = mfma16(tr_frag<1>(wa[slot]), xf[s], qa[1]); \
      qa[2] = mfma16(tr_frag<2>(wa[slot]), xf[s], qa[2]); qa[3] = mfma16(tr_frag<3>(wa[slot]), xf[s], qa[3]); \
      qa[4] = mfma16(tr_frag<4>(wa[slot]), xf[s], qa[4]); qa[5] = mfma16(tr_frag<5>(wa[slot]), xf[s], qa[5]); \
      qa[6] = mfma16(tr_frag<6>(wa[slot]), xf[s], qa[6]); qa[7] = mfma16(tr_frag<7>(wa[slot]), xf[s], qa[7]); }
    LOAD_WQ(0, 0); SB();
    LOAD_ITEM(0); SB();
    LOAD_WQ(1, 1); SB();
    LOAD_ITEM(1); SB();
    LOAD_WQ(2, 2); SB();
    MMA_WQ(0, 0); SB();
    LOAD_WQ(0, 3); SB();
    MMA_WQ(1, 1); SB();
    LOAD_ITEM(2); SB();
    MMA_WQ(2, 2); SB();
    MMA_WQ(0, 3); SB();
#pragma unroll
    for (int rho = 0; rho < 4; ++rho) {
      LAS f32x4* p = (LAS f32x4*)(LW + r * QP_STRIDE + (32 * g + 8 * rho) * 4);
      p[0] = f32x4{qa[0][rho], qa[1][rho], qa[2][rho], qa[3][rho]};
      p[1] = f32x4{qa[4][rho], qa[5][rho], qa[6][rho], qa[7][rho]};
    }
  }
  const unsigned e_lo = __builtin_amdgcn_readfirstlane(epoch0[0]), e_hi = __builtin_amdgcn_readfirstlane(epoch0[1]);
  const unsigned long long tag64 = (((unsigned long long)e_hi << 32) | e_lo) + 1ull;
  const unsigned tag_lo = (unsigned)tag64, tag_hi = (unsigned)(tag64 >> 32);
  asm volatile("s_waitcnt vmcnt(8)" ::: "memory");
  __syncthreads();
  if (tid == 0) __builtin_amdgcn_raw_buffer_store_b64(v2u{tag_lo, tag_hi}, rws, 0, WS_FLAG + bp * 256 + h * 8, AUX_SC1);
  {
    const int row = tid >> 5, n4 = (tid & 31) * 4;
    f32x4 s = {0.f, 0.f, 0.f, 0.f};
#pragma unroll
    for (int ww = 0; ww < 8; ++ww) s += *(LAS f32x4*)(L + ww * L_WAVE + row * QP_STRIDE + n4 * 4);
    *(LAS v2u*)(L + L_QS + row * QS_STRIDE + n4 * 2) = v2u{cvt_pk(s[0], s[1]), cvt_pk(s[2], s[3])};
  }
  __syncthreads();
  v4u qf[4];
#pragma unroll
  for (int i = 0; i < 4; ++i) qf[i] = *(LAS v4u*)(L + L_QS + (8 * beta + (r & 7)) * QS_STRIDE + (32 * i + 8 * g) * 2);

  unsigned wh[8][8];
  const int wooff = (8 * g) * (DM * 2) + r * 4;
  const int wos = (512 * w) * (DM * 2) + h * 64;
#define LOAD_WO(slot, ks) { _Pragma("unroll") for (int j_ = 0; j_ < 8; ++j_) \
    wh[slot][j_] = __builtin_amdgcn_raw_buffer_load_b32(rwo, wooff + (j_ & 1) * (DM * 2), wos + (32 * (ks) + (j_ & ~1)) * (DM * 2), 0); }
#define FRAG_WO(slot) { _Pragma("unroll") for (int jj_ = 0; jj_ < 4; ++jj_) { \
    const unsigned a_ = wh[slot][2 * jj_], b_ = wh[slot][2 * jj_ + 1]; wh[slot][2 * jj_] = pack_lo(a_, b_); wh[slot][2 * jj_ + 1] = pack_hi(a_, b_); } }
#define PARK_WO(slot, ks) { \
    *(LAS v4u*)(LW + (ks) * 2048 + lane * 16) = v4u{wh[slot][0], wh[slot][2], wh[slot][4], wh[slot][6]}; \
    *(LAS v4u*)(LW + (ks) * 2048 + 1024 + lane * 16) = v4u{wh[slot][1], wh[slot][3], wh[slot][5], wh[slot][7]}; }

  f32x4 oacc[8];
#pragma unroll
  for (int e = 0; e < 8; ++e) oacc[e] = f32x4{0.f, 0.f, 0.f, 0.f};
  float m = -INFINITY, lsum = 0.f;
  v2u flags_v = {0u, 0u};

#pragma unroll
  for (int t = 0; t < NT; ++t) {
    const int ks = (2 * t) % 3, vs = (2 * t + 1) % 3;
    f32x4 s0 = {0.f, 0.f, 0.f, 0.f}, s1 = {0.f, 0.f, 0.f, 0.f};
#pragma unroll
    for (int i = 0; i < 4; ++i) { s0 = mfma16(ring[ks][i], qf[i], s0); s1 = mfma16(ring[ks][4 + i], qf[i], s1); }
    SB();
    LOAD_ITEM(2 * t + 3);
    if (t < 4) { LOAD_WO(2 * t, 2 * t); LOAD_WO(2 * t + 1, 2 * t + 1); }
    if (t >= 5 && t < 9) { LOAD_WO(2 * (t - 5), 2 * (t - 5) + 8); LOAD_WO(2 * (t - 5) + 1, 2 * (t - 5) + 9); }
    if (t == NT - 2) flags_v = __builtin_amdgcn_raw_buffer_load_b64(rws, (voff >> 1) & 0xF8, WS_FLAG + bp * 256, AUX_SC1);
    SB();
    float mx = fmaxf(fmaxf(fmaxf(s0[0], s0[1]), fmaxf(s0[2], s0[3])), fmaxf(fmaxf(s1[0], s1[1]), fmaxf(s1[2], s1[3])));
    mx = xg_max(mx);
    const float mn = fmaxf(m, mx);
    const float alpha = __builtin_amdgcn_exp2f((m - mn) * CEXP);
    const float mc = mn * CEXP;
    m = mn;
    float p[8];
#pragma unroll
    for (int j = 0; j < 4; ++j) {
      p[j] = __builtin_amdgcn_exp2f(__builtin_fmaf(s0[j], CEXP, -mc));
      p[4 + j] = __builtin_amdgcn_exp2f(__builtin_fmaf(s1[j], CEXP, -mc));
    }
    lsum = lsum * alpha + (((p[0] + p[1]) + (p[2] + p[3])) + ((p[4] + p[5]) + (p[6] + p[7])));
    const v4u pf = {cvt_pk(p[0], p[1]), cvt_pk(p[2], p[3]), cvt_pk(p[4], p[5]), cvt_pk(p[6], p[7])};
#pragma unroll
    for (int e = 0; e < 8; ++e) oacc[e] *= alpha;
    oacc[0] = mfma16(tr_frag<0>(ring[vs]), pf, oacc[0]); oacc[1] = mfma16(tr_frag<1>(ring[vs]), pf, oacc[1]);
    oacc[2] = mfma16(tr_frag<2>(ring[vs]), pf, oacc[2]); oacc[3] = mfma16(tr_frag<3>(ring[vs]), pf, oacc[3]);
    oacc[4] = mfma16(tr_frag<4>(ring[vs]), pf, oacc[4]); oacc[5] = mfma16(tr_frag<5>(ring[vs]), pf, oacc[5]);
    oacc[6] = mfma16(tr_frag<6>(ring[vs]), pf, oacc[6]); oacc[7] = mfma16(tr_frag<7>(ring[vs]), pf, oacc[7]);
    SB();
    LOAD_ITEM(2 * t + 4);
    SB();
    if (t >= 1 && t < 5) { FRAG_WO(2 * (t - 1)); FRAG_WO(2 * (t - 1) + 1); PARK_WO(2 * (t - 1), 2 * (t - 1)); PARK_WO(2 * (t - 1) + 1, 2 * (t - 1) + 1); }
    if (t >= 6 && t < 10) { FRAG_WO(2 * (t - 6)); FRAG_WO(2 * (t - 6) + 1); }
    SB();
  }

  const int lane2 = __builtin_amdgcn_mbcnt_hi(~0u, __builtin_amdgcn_mbcnt_lo(~0u, 0u));
  const int r2 = lane2 & 15, g2 = lane2 >> 4;
  v4u wl[8][2];
#pragma unroll
  for (int ks = 0; ks < 8; ++ks) {
    wl[ks][0] = *(LAS v4u*)(LW + ks * 2048 + lane2 * 16);
    wl[ks][1] = *(LAS v4u*)(LW + ks * 2048 + 1024 + lane2 * 16);
  }
  lsum = xg_sum(lsum);
  asm volatile("s_waitcnt lgkmcnt(0)" ::: "memory");
  SB();
  if (r2 < 8) {
#pragma unroll
    for (int rho = 0; rho < 4; ++rho) {
      LAS f32x4* p = (LAS f32x4*)(LW + r2 * QP_STRIDE + (32 * g2 + 8 * rho) * 4);
      p[0] = f32x4{oacc[0][rho], oacc[1][rho], oacc[2][rho], oacc[3][rho]};
      p[1] = f32x4{oacc[4][rho], oacc[5][rho], oacc[6][rho], oacc[7][rho]};
    }
    if (g2 == 0) *(LAS f32x2*)(LW + L_ML + r2 * 8) = f32x2{m, lsum};
  }
  __syncthreads();
  if (w < 4) {
    const int bt = r2 >> 3, q = r2 & 7;
    float mw[4], lw[4];
#pragma unroll
    for (int j = 0; j < 4; ++j) { const f32x2 ml = *(LAS f32x2*)(L + (4 * bt + j) * L_WAVE + L_ML + q * 8); mw[j] = ml[0]; lw[j] = ml[1]; }
    const float M = fmaxf(fmaxf(mw[0], mw[1]), fmaxf(mw[2], mw[3]));
    float Ls = 0.f;
    f32x4 a0 = {0.f, 0.f, 0.f, 0.f}, a1 = {0.f, 0.f, 0.f, 0.f};
#pragma unroll
    for (int j = 0; j < 4; ++j) {
      const float wt = __builtin_amdgcn_exp2f((mw[j] - M) * CEXP);
      Ls += wt * lw[j];
      const LAS f32x4* p = (const LAS f32x4*)(L + (4 * bt + j) * L_WAVE + q * QP_STRIDE + (32 * w + 8 * g2) * 4);
      a0 += p[0] * wt; a1 += p[1] * wt;
    }
    const float inv = 1.0f / Ls;
    v4u ov = {cvt_pk(a0[0] * inv, a0[1] * inv), cvt_pk(a0[2] * inv, a0[3] * inv), cvt_pk(a1[0] * inv, a1[1] * inv), cvt_pk(a1[2] * inv, a1[3] * inv)};
#pragma unroll
    for (int j = 0; j < 4; ++j) ov[j] = (ov[j] == SENT) ? 0x7FC07FC0u : ov[j];
    __builtin_amdgcn_raw_buffer_store_b128(ov, rws, lane2 * 16, o_slot + w * 1024, AUX_SC1);
  }
  {
    unsigned spins = 0;
    while (!__all(flags_v[0] == tag_lo && flags_v[1] == tag_hi)) {
      __builtin_amdgcn_s_sleep(2);
      if (++spins > kSpinCap) break;
      asm volatile("" ::: "memory");
      flags_v = __builtin_amdgcn_raw_buffer_load_b64(rws, (voff >> 1) & 0xF8, WS_FLAG + bp * 256, AUX_SC1);
    }
    asm volatile("" ::: "memory");
    if (h == 0 && w == 0 && lane2 == 0) __builtin_amdgcn_raw_buffer_store_b64(v2u{tag_lo, tag_hi}, rws, 0, WS_EPOCH + bp * 256, AUX_SC1);
  }
  f32x4 c0 = {0.f, 0.f, 0.f, 0.f}, c1 = {0.f, 0.f, 0.f, 0.f};
  {
    const int goff = WS_O + (bp * 128 + 16 * w) * 1024;
    v4u ob[16];
    unsigned spins = 0;
    for (;;) {
      asm volatile("" ::: "memory");
#pragma unroll
      for (int ks = 0; ks < 16; ++ks) ob[ks] = __builtin_amdgcn_raw_buffer_load_b128(rws, voff, goff + ks * 1024, AUX_SC1);
      bool ok = true;
#pragma unroll
      for (int ks = 0; ks < 16; ++ks) ok &= (ob[ks][0] != SENT) & (ob[ks][1] != SENT) & (ob[ks][2] != SENT) & (ob[ks][3] != SENT);
      if (__all(ok)) break;
      if (++spins > kSpinCap) break;
      __builtin_amdgcn_s_sleep(8);
    }
#pragma unroll
    for (int ks = 0; ks < 8; ++ks) { c0 = mfma16(wl[ks][0], ob[ks], c0); c1 = mfma16(wl[ks][1], ob[ks], c1); }
#pragma unroll
    for (int ks = 0; ks < 8; ++ks) {
      c0 = mfma16(v4u{wh[ks][0], wh[ks][2], wh[ks][4], wh[ks][6]}, ob[8 + ks], c0);
      c1 = mfma16(v4u{wh[ks][1], wh[ks][3], wh[ks][5], wh[ks][7]}, ob[8 + ks], c1);
    }
  }
  *(LAS f32x4*)(LW + L_RED + lane2 * 32) = f32x4{c0[0], c1[0], c0[1], c1[1]};
  *(LAS f32x4*)(LW + L_RED + lane2 * 32 + 16) = f32x4{c0[2], c1[2], c0[3], c1[3]};
  __syncthreads();
  if (w < 2) {
    const int ln = (w * 64 + lane2) >> 1, half = lane2 & 1;
    f32x4 s = {0.f, 0.f, 0.f, 0.f};
#pragma unroll
    for (int ww = 0; ww < 8; ++ww) s += *(LAS f32x4*)(L + ww * L_WAVE + L_RED + ln * 32 + half * 16);
    const int row = 16 * bp + (ln & 15), col = 32 * h + 8 * (ln >> 4) + 4 * half;
    *(GAS v2u*)(out + (size_t)row * DM + col) = v2u{cvt_pk(s[0], s[1]), cvt_pk(s[2], s[3])};
  }
}

}

extern "C" void kernel_launch(void* const* d_in, const int* in_sizes, int n_in, void* d_out, int out_size, void* d_ws,
                              size_t ws_size, hipStream_t stream) {
  (void)in_sizes; (void)n_in; (void)out_size; (void)ws_size;
  (void)hipFuncSetAttribute((const void*)mha_fused, hipFuncAttributeMaxDynamicSharedMemorySize, L_BYTES);
  hipLaunchKernelGGL(mha_fused, dim3(NWG), dim3(NTHR), L_BYTES, stream, (const unsigned short*)d_in[0], (const unsigned short*)d_in[1],
                     (const unsigned short*)d_in[2], (const unsigned short*)d_in[3], (const unsigned short*)d_in[4],
                     (unsigned short*)d_out, (unsigned char*)d_ws);
}
```

```cpp
#include <hip/hip_runtime.h>
#include <hip/hip_bf16.h>

#define GAS __attribute__((address_space(1)))
#define LAS __attribute__((address_space(3)))
typedef unsigned v4u __attribute__((ext_vector_type(4)));
typedef unsigned v2u __attribute__((ext_vector_type(2)));
typedef float f32x4 __attribute__((ext_vector_type(4)));
typedef __bf16 bf16x8 __attribute__((ext_vector_type(8)));
typedef __bf16 bf16x2 __attribute__((ext_vector_type(2)));
typedef float f32x2 __attribute__((ext_vector_type(2)));

#define AUX_WQ 2
#define AUX_WO 2
#define AUX_X 2
#define PRO_ORDER 0
#define NO_WO 0

namespace {

constexpr int NB = 16, SQ = 8, DM = 1024, NH = 32, SK = 2048, DH = 128;
constexpr int NWG = 256, NTHR = 512;
constexpr int NT = 16;
constexpr float CEXP = 0.08838834764831845f * 1.4426950408889634f;
constexpr int AUX_NT = 2, AUX_SC1 = 16;
constexpr int RSRC_W3 = 0x00020000;
constexpr unsigned SENT = 0xFFFFFFFFu;
constexpr unsigned kSpinCap = 1u << 16;

constexpr int WS_EPOCH = 0;
constexpr int WS_FLAG = 4096;
constexpr int WS_O = 65536;
constexpr int WS_BYTES = WS_O + 8 * 128 * 1024;

constexpr int L_WAVE = 16384;
constexpr int L_QS = 8 * L_WAVE;
constexpr int QS_STRIDE = 272;
constexpr int L_BYTES = L_QS + 16 * QS_STRIDE;
constexpr int QP_STRIDE = 528;
constexpr int L_ML = 16 * QP_STRIDE;
constexpr int L_RED = 12288;

__device__ __forceinline__ f32x4 mfma16(v4u a, v4u b, f32x4 c) {
  return __builtin_amdgcn_mfma_f32_16x16x32_bf16(__builtin_bit_cast(bf16x8, a), __builtin_bit_cast(bf16x8, b), c, 0, 0, 0);
}
__device__ __forceinline__ unsigned pack_lo(unsigned a, unsigned b) { return __builtin_amdgcn_perm(b, a, 0x05040100u); }
__device__ __forceinline__ unsigned pack_hi(unsigned a, unsigned b) { return __builtin_amdgcn_perm(b, a, 0x07060302u); }
__device__ __forceinline__ unsigned cvt_pk(float lo, float hi) {
  f32x2 v = {lo, hi};
  return __builtin_bit_cast(unsigned, __builtin_convertvector(v, bf16x2));
}
template <int E> __device__ __forceinline__ v4u tr_frag(const v4u (&R)[8]) {
  v4u f;
#pragma unroll
  for (int jj = 0; jj < 4; ++jj)
    f[jj] = (E & 1) ? pack_hi(R[2 * jj][E >> 1], R[2 * jj + 1][E >> 1]) : pack_lo(R[2 * jj][E >> 1], R[2 * jj + 1][E >> 1]);
  return f;
}
__device__ __forceinline__ float ror8(float x) {
  return __builtin_bit_cast(float, __builtin_amdgcn_mov_dpp(__builtin_bit_cast(int, x), 0x128, 0xf, 0xf, true));
}
__device__ __forceinline__ float bsel(unsigned mask, float a, float b) {
  return __uint_as_float((__float_as_uint(a) & mask) | (__float_as_uint(b) & ~mask));
}
__device__ __forceinline__ float xg_max(float x) {
  auto s = __builtin_amdgcn_permlane16_swap(__float_as_uint(x), __float_as_uint(x), false, false);
  x = fmaxf(__uint_as_float(s[0]), __uint_as_float(s[1]));
  auto t = __builtin_amdgcn_permlane32_swap(__float_as_uint(x), __float_as_uint(x), false, false);
  return fmaxf(__uint_as_float(t[0]), __uint_as_float(t[1]));
}
__device__ __forceinline__ float xg_sum(float x) {
  auto s = __builtin_amdgcn_permlane16_swap(__float_as_uint(x), __float_as_uint(x), false, false);
  x = __uint_as_float(s[0]) + __uint_as_float(s[1]);
  auto t = __builtin_amdgcn_permlane32_swap(__float_as_uint(x), __float_as_uint(x), false, false);
  return __uint_as_float(t[0]) + __uint_as_float(t[1]);
}
#define SB() __builtin_amdgcn_sched_barrier(0)
#define STAMP(i)

constexpr int last_issued(int c) { return (c < 2 ? c + 5 : c < 4 ? 6 : c + 3) > 2 * NT - 1 ? 2 * NT - 1 : (c < 2 ? c + 5 : c < 4 ? 6 : c + 3); }
constexpr int wo_before(int t) { return t <= 2 ? 0 : t >= 14 ? 16 : ((t - 2) * 4) / 3; }

__global__ void __launch_bounds__(NTHR, 2)
mha_fused(const unsigned short* __restrict__ x, const unsigned short* __restrict__ kc, const unsigned short* __restrict__ vc,
          const unsigned short* __restrict__ wq, const unsigned short* __restrict__ wo, unsigned short* __restrict__ out,
          unsigned char* __restrict__ ws) {
  extern __shared__ __attribute__((aligned(16))) unsigned char smem[];
  LAS unsigned char* L = (LAS unsigned char*)smem;
  const int tid = threadIdx.x, lane = tid & 63, w = __builtin_amdgcn_readfirstlane(tid >> 6);
  const int r = lane & 15, g = lane >> 4;
  const int wg = blockIdx.x, idx = wg >> 3;
  const int h = 4 * (wg & 7) + (idx & 3), bp = idx >> 2;
  const int beta = w >> 2, ws4 = w & 3;
  const int bh = (2 * bp + beta) * NH + h;
  const int rot = (bh * 5) & (NT - 1);
  LAS unsigned char* LW = L + w * L_WAVE;
  STAMP(0);

  const __amdgpu_buffer_rsrc_t rk = __builtin_amdgcn_make_buffer_rsrc((void*)(kc + (size_t)bh * SK * DH), 0, SK * DH * 2, RSRC_W3);
  const __amdgpu_buffer_rsrc_t rv = __builtin_amdgcn_make_buffer_rsrc((void*)(vc + (size_t)bh * SK * DH), 0, SK * DH * 2, RSRC_W3);
  const __amdgpu_buffer_rsrc_t rws = __builtin_amdgcn_make_buffer_rsrc((void*)ws, 0, WS_BYTES, RSRC_W3);
  const __amdgpu_buffer_rsrc_t rwq = __builtin_amdgcn_make_buffer_rsrc((void*)wq, 0, DM * NH * DH * 2, RSRC_W3);
  const __amdgpu_buffer_rsrc_t rwo = __builtin_amdgcn_make_buffer_rsrc((void*)wo, 0, NH * DH * DM * 2, RSRC_W3);
  const __amdgpu_buffer_rsrc_t rx = __builtin_amdgcn_make_buffer_rsrc((void*)x, 0, NB * SQ * DM * 2, RSRC_W3);

  const v2u epoch0 = __builtin_amdgcn_raw_buffer_load_b64(rws, 0, WS_EPOCH + bp * 256, AUX_SC1);
  const int o_slot = WS_O + (bp * 128 + 4 * h) * 1024;
  __builtin_amdgcn_raw_buffer_store_b64(v2u{SENT, SENT}, rws, lane * 8, o_slot + w * 512, AUX_SC1);
  asm volatile("" ::: "memory");
  SB();

  const int koff = (4 * ((r >> 1) & 1) + (r >> 2)) * 256 + (2 * g + (r & 1)) * 16;
  const int voff = lane * 16;
  v4u item[2 * NT][8];
#define TILE_OFF(t) (((((t) + rot) & (NT - 1)) * 4 + ws4) * 8192)
#define LOAD_ITEM(n) { const int so_ = TILE_OFF((n) >> 1); _Pragma("unroll") for (int i_ = 0; i_ < 8; ++i_) \
    item[n][i_] = ((n) & 1) ? __builtin_amdgcn_raw_buffer_load_b128(rv, voff, so_ + i_ * 1024, AUX_NT) \
                            : __builtin_amdgcn_raw_buffer_load_b128(rk, koff, so_ + (i_ >> 1) * 2048 + (i_ & 1) * 128, AUX_NT); }

  {
    v4u xf[4];
    const int xoff = ((16 * bp + r) * DM + 128 * w + 8 * g) * 2;
    const int wqoff = (8 * g) * (NH * DH * 2) + r * 16;
    const int wqs = (128 * w) * (NH * DH * 2) + h * (DH * 2);
    v4u wa[4][8];
#define LOAD_WQ(s) { _Pragma("unroll") for (int j_ = 0; j_ < 8; ++j_) \
      wa[s][j_] = __builtin_amdgcn_raw_buffer_load_b128(rwq, wqoff, wqs + (32 * (s) + j_) * (NH * DH * 2), AUX_WQ); }
    f32x4 qa[8];
#pragma unroll
    for (int e = 0; e < 8; ++e) qa[e] = f32x4{0.f, 0.f, 0.f, 0.f};
#define MMA_WQ(s) { \
      qa[0] = mfma16(tr_frag<0>(wa[s]), xf[s], qa[0]); qa[1] = mfma16(tr_frag<1>(wa[s]), xf[s], qa[1]); \
      qa[2] = mfma16(tr_frag<2>(wa[s]), xf[s], qa[2]); qa[3] = mfma16(tr_frag<3>(wa[s]), xf[s], qa[3]); \
      qa[4] = mfma16(tr_frag<4>(wa[s]), xf[s], qa[4]); qa[5] = mfma16(tr_frag<5>(wa[s]), xf[s], qa[5]); \
      qa[6] = mfma16(tr_frag<6>(wa[s]), xf[s], qa[6]); qa[7] = mfma16(tr_frag<7>(wa[s]), xf[s], qa[7]); }
#define LOAD_X() { _Pragma("unroll") for (int s_ = 0; s_ < 4; ++s_) xf[s_] = __builtin_amdgcn_raw_buffer_load_b128(rx, xoff + s_ * 64, 0, AUX_X); }
#define BAR() __builtin_amdgcn_s_barrier()
    LOAD_ITEM(0); SB();
    LOAD_X(); LOAD_WQ(0); SB(); LOAD_WQ(1); SB(); LOAD_WQ(2); SB(); LOAD_WQ(3); SB();
    BAR(); SB();
    MMA_WQ(0); SB();
    LOAD_ITEM(1); SB();
    MMA_WQ(1); SB();
    LOAD_ITEM(2); SB();
    MMA_WQ(2); SB();
    LOAD_ITEM(3); SB();
    MMA_WQ(3); SB();
    LOAD_ITEM(4); SB();
#pragma unroll
    for (int rho = 0; rho < 4; ++rho) {
      LAS f32x4* p = (LAS f32x4*)(LW + r * QP_STRIDE + (32 * g + 8 * rho) * 4);
      p[0] = f32x4{qa[0][rho], qa[1][rho], qa[2][rho], qa[3][rho]};
      p[1] = f32x4{qa[4][rho], qa[5][rho], qa[6][rho], qa[7][rho]};
    }
  }
  const unsigned e_lo = __builtin_amdgcn_readfirstlane(epoch0[0]), e_hi = __builtin_amdgcn_readfirstlane(epoch0[1]);
  const unsigned long long tag64 = (((unsigned long long)e_hi << 32) | e_lo) + 1ull;
  const unsigned tag_lo = (unsigned)tag64, tag_hi = (unsigned)(tag64 >> 32);
  asm volatile("s_waitcnt vmcnt(40)" ::: "memory");
  __syncthreads();
  if (tid == 0) __builtin_amdgcn_raw_buffer_store_b64(v2u{tag_lo, tag_hi}, rws, 0, WS_FLAG + bp * 256 + h * 8, AUX_SC1);
  {
    const int row = tid >> 5, n4 = (tid & 31) * 4;
    f32x4 s = {0.f, 0.f, 0.f, 0.f};
#pragma unroll
    for (int ww = 0; ww < 8; ++ww) s += *(LAS f32x4*)(L + ww * L_WAVE + row * QP_STRIDE + n4 * 4);
    *(LAS v2u*)(L + L_QS + row * QS_STRIDE + n4 * 2) = v2u{cvt_pk(s[0], s[1]), cvt_pk(s[2], s[3])};
  }
  __syncthreads();
  v4u qf[2];
#pragma unroll
  for (int hf = 0; hf < 2; ++hf) qf[hf] = *(LAS v4u*)(L + L_QS + (8 * beta + (r & 7)) * QS_STRIDE + (8 * hf + 2 * g + (r >> 3)) * 16);
  unsigned hmask = (r >> 3) ? 0xFFFFFFFFu : 0u;
  asm volatile("" : "+v"(hmask));
  const unsigned pmask = hmask ^ 0x0000FFFFu;

  STAMP(1);
  unsigned wh[16][8];
  const int wooff = (8 * g) * (DM * 2) + r * 4;
  const int wos = (512 * w) * (DM * 2) + h * 64;
#define LOAD_WO(ks) { _Pragma("unroll") for (int j_ = 0; j_ < 8; ++j_) \
    wh[ks][j_] = NO_WO ? 0u : __builtin_amdgcn_raw_buffer_load_b32(rwo, wooff, wos + (32 * (ks) + j_) * (DM * 2), AUX_WO); }
#define FRAG_WO(ks) { _Pragma("unroll") for (int jj_ = 0; jj_ < 4; ++jj_) { \
    const unsigned a_ = wh[ks][2 * jj_], b_ = wh[ks][2 * jj_ + 1]; wh[ks][2 * jj_] = pack_lo(a_, b_); wh[ks][2 * jj_ + 1] = pack_hi(a_, b_); } }
#define PARK_WO(ks) { \
    *(LAS v4u*)(LW + (ks) * 2048 + voff) = v4u{wh[ks][0], wh[ks][2], wh[ks][4], wh[ks][6]}; \
    *(LAS v4u*)(LW + (ks) * 2048 + 1024 + voff) = v4u{wh[ks][1], wh[ks][3], wh[ks][5], wh[ks][7]}; }

  f32x4 oacc[8];
#pragma unroll
  for (int e = 0; e < 8; ++e) oacc[e] = f32x4{0.f, 0.f, 0.f, 0.f};
  float m = -INFINITY, lsum = 0.f;
  v2u flags_v = {0u, 0u};

#pragma unroll
  for (int t = 0; t < NT; ++t) {
    f32x4 sa[4];
#pragma unroll
    for (int u = 0; u < 4; ++u) sa[u] = mfma16(item[2 * t][2 * u], qf[0], f32x4{0.f, 0.f, 0.f, 0.f});
#pragma unroll
    for (int u = 0; u < 4; ++u) sa[u] = mfma16(item[2 * t][2 * u + 1], qf[1], sa[u]);
    SB();
#pragma unroll
    for (int ks = wo_before(t); ks < wo_before(t + 1); ++ks) LOAD_WO(ks);
    if (t == NT - 2) flags_v = __builtin_amdgcn_raw_buffer_load_b64(rws, (voff >> 1) & 0xF8, WS_FLAG + bp * 256, AUX_SC1);
#pragma unroll
    for (int n = last_issued(2 * t - 1) + 1; n <= last_issued(2 * t); ++n) LOAD_ITEM(n);
    SB();
    if (t >= 2) {
#pragma unroll
      for (int ks = wo_before(t - 2); ks < wo_before(t - 1); ++ks) { FRAG_WO(ks); if (ks < 8) PARK_WO(ks); }
    }
    float s[4];
#pragma unroll
    for (int u = 0; u < 4; ++u) {
      const float xx = bsel(hmask, sa[u][3], sa[u][0]), yy = bsel(hmask, sa[u][1], sa[u][2]);
      s[u] = xx + ror8(yy);
    }
    const float mx = xg_max(fmaxf(fmaxf(s[0], s[1]), fmaxf(s[2], s[3])));
    const float mn = fmaxf(m, mx);
    const float alpha = __builtin_amdgcn_exp2f((m - mn) * CEXP);
    const float mc = mn * CEXP;
    m = mn;
    float p[4];
#pragma unroll
    for (int u = 0; u < 4; ++u) p[u] = __builtin_amdgcn_exp2f(__builtin_fmaf(s[u], CEXP, -mc));
    lsum = lsum * alpha + ((p[0] + p[1]) + (p[2] + p[3]));
    asm volatile("" : "+v"(lsum));
    const v4u pf = {cvt_pk(p[0], p[0]) & pmask, cvt_pk(p[1], p[1]) & pmask, cvt_pk(p[2], p[2]) & pmask, cvt_pk(p[3], p[3]) & pmask};
#pragma unroll
    for (int e = 0; e < 8; ++e) oacc[e] *= alpha;
    oacc[0] = mfma16(tr_frag<0>(item[2 * t + 1]), pf, oacc[0]); oacc[1] = mfma16(tr_frag<1>(item[2 * t + 1]), pf, oacc[1]);
    oacc[2] = mfma16(tr_frag<2>(item[2 * t + 1]), pf, oacc[2]); oacc[3] = mfma16(tr_frag<3>(item[2 * t + 1]), pf, oacc[3]);
    oacc[4] = mfma16(tr_frag<4>(item[2 * t + 1]), pf, oacc[4]); oacc[5] = mfma16(tr_frag<5>(item[2 * t + 1]), pf, oacc[5]);
    oacc[6] = mfma16(tr_frag<6>(item[2 * t + 1]), pf, oacc[6]); oacc[7] = mfma16(tr_frag<7>(item[2 * t + 1]), pf, oacc[7]);
    SB();
#pragma unroll
    for (int n = last_issued(2 * t) + 1; n <= last_issued(2 * t + 1); ++n) LOAD_ITEM(n);
    SB();
  }

  STAMP(2);
  const int lane2 = __builtin_amdgcn_mbcnt_hi(~0u, __builtin_amdgcn_mbcnt_lo(~0u, 0u));
  const int r2 = lane2 & 15, g2 = lane2 >> 4;
  v4u wl[8][2];
#pragma unroll
  for (int ks = 0; ks < 8; ++ks) {
    wl[ks][0] = *(LAS v4u*)(LW + ks * 2048 + lane2 * 16);
    wl[ks][1] = *(LAS v4u*)(LW + ks * 2048 + 1024 + lane2 * 16);
  }
  lsum = xg_sum(lsum);
  asm volatile("s_waitcnt lgkmcnt(0)" ::: "memory");
  SB();
#pragma unroll
  for (int rho = 0; rho < 4; ++rho) {
    LAS f32x4* p = (LAS f32x4*)(LW + r2 * QP_STRIDE + (32 * g2 + 8 * rho) * 4);
    p[0] = f32x4{oacc[0][rho], oacc[1][rho], oacc[2][rho], oacc[3][rho]};
    p[1] = f32x4{oacc[4][rho], oacc[5][rho], oacc[6][rho], oacc[7][rho]};
  }
  if (g2 == 0) *(LAS f32x2*)(LW + L_ML + r2 * 8) = f32x2{m, lsum};
  __syncthreads();
  STAMP(3);
  {
    const int fl = 32 * (w & 1) + (lane2 >> 1), c = fl & 15;
    const int d0 = 32 * (w >> 1) + 8 * (fl >> 4) + 4 * (lane2 & 1);
    const int bt = c >> 3, q = c & 7;
    float mw[8], lw[8];
    f32x4 pv[8];
#pragma unroll
    for (int j = 0; j < 8; ++j) {
      const LAS unsigned char* base = L + (4 * bt + (j >> 1)) * L_WAVE;
      const f32x2 ml = *(const LAS f32x2*)(base + L_ML + (q + 8 * (j & 1)) * 8);
      mw[j] = ml[0]; lw[j] = ml[1];
      pv[j] = *(const LAS f32x4*)(base + (q + 8 * (j & 1)) * QP_STRIDE + d0 * 4);
    }
    const float M = fmaxf(fmaxf(fmaxf(mw[0], mw[1]), fmaxf(mw[2], mw[3])), fmaxf(fmaxf(mw[4], mw[5]), fmaxf(mw[6], mw[7])));
    float Ls = 0.f;
    f32x4 a0 = {0.f, 0.f, 0.f, 0.f};
#pragma unroll
    for (int j = 0; j < 8; ++j) {
      const float wt = __builtin_amdgcn_exp2f((mw[j] - M) * CEXP);
      Ls += wt * lw[j];
      a0 += pv[j] * wt;
    }
    const float inv = 1.0f / Ls;
    v2u ov = {cvt_pk(a0[0] * inv, a0[1] * inv), cvt_pk(a0[2] * inv, a0[3] * inv)};
#pragma unroll
    for (int j = 0; j < 2; ++j) ov[j] = (ov[j] == SENT) ? 0x7FC07FC0u : ov[j];
    __builtin_amdgcn_raw_buffer_store_b64(ov, rws, lane2 * 8, o_slot + w * 512, AUX_SC1);
  }
  {
    unsigned spins = 0;
    while (!__all(flags_v[0] == tag_lo && flags_v[1] == tag_hi)) {
      __builtin_amdgcn_s_sleep(2);
      if (++spins > kSpinCap) break;
      asm volatile("" ::: "memory");
      flags_v = __builtin_amdgcn_raw_buffer_load_b64(rws, (lane2 & 31) * 8, WS_FLAG + bp * 256, AUX_SC1);
    }
    asm volatile("" ::: "memory");
    if (h == 0 && w == 0 && lane2 == 0) __builtin_amdgcn_raw_buffer_store_b64(v2u{tag_lo, tag_hi}, rws, 0, WS_EPOCH + bp * 256, AUX_SC1);
  }
  STAMP(4);
  f32x4 c0 = {0.f, 0.f, 0.f, 0.f}, c1 = {0.f, 0.f, 0.f, 0.f};
  {
    const int goff = WS_O + (bp * 128 + 16 * w) * 1024;
    unsigned spins = 0;
    for (;;) {
      asm volatile("" ::: "memory");
      const unsigned pr = __builtin_amdgcn_raw_buffer_load_b32(rws, (lane2 & 3) * 4096 + 4092, goff, AUX_SC1);
      if (__all(pr != SENT)) break;
      if (++spins > kSpinCap) break;
      __builtin_amdgcn_s_sleep(4);
    }
    v4u ob[16];
    for (;;) {
      asm volatile("" ::: "memory");
#pragma unroll
      for (int ks = 0; ks < 16; ++ks) ob[ks] = __builtin_amdgcn_raw_buffer_load_b128(rws, lane2 * 16, goff + ks * 1024, AUX_SC1);
      bool ok = true;
#pragma unroll
      for (int ks = 0; ks < 16; ++ks) ok &= (ob[ks][0] != SENT) & (ob[ks][1] != SENT) & (ob[ks][2] != SENT) & (ob[ks][3] != SENT);
      if (__all(ok)) break;
      if (++spins > kSpinCap) break;
      __builtin_amdgcn_s_sleep(2);
    }
    STAMP(5);
#pragma unroll
    for (int ks = 0; ks < 8; ++ks) { c0 = mfma16(wl[ks][0], ob[ks], c0); c1 = mfma16(wl[ks][1], ob[ks], c1); }
#pragma unroll
    for (int ks = 8; ks < 16; ++ks) {
      c0 = mfma16(v4u{wh[ks][0], wh[ks][2], wh[ks][4], wh[ks][6]}, ob[ks], c0);
      c1 = mfma16(v4u{wh[ks][1], wh[ks][3], wh[ks][5], wh[ks][7]}, ob[ks], c1);
    }
  }
  *(LAS f32x4*)(LW + L_RED + lane2 * 32) = f32x4{c0[0], c1[0], c0[1], c1[1]};
  *(LAS f32x4*)(LW + L_RED + lane2 * 32 + 16) = f32x4{c0[2], c1[2], c0[3], c1[3]};
  __syncthreads();
  if (w < 2) {
    const int ln = (w * 64 + lane2) >> 1, half = lane2 & 1;
    f32x4 s = {0.f, 0.f, 0.f, 0.f};
#pragma unroll
    for (int ww = 0; ww < 8; ++ww) s += *(LAS f32x4*)(L + ww * L_WAVE + L_RED + ln * 32 + half * 16);
    const int row = 16 * bp + (ln & 15), col = 32 * h + 8 * (ln >> 4) + 4 * half;
    *(GAS v2u*)(out + (size_t)row * DM + col) = v2u{cvt_pk(s[0], s[1]), cvt_pk(s[2], s[3])};
  }
}

}

extern "C" void kernel_launch(void* const* d_in, const int* in_sizes, int n_in, void* d_out, int out_size, void* d_ws,
                              size_t ws_size, hipStream_t stream) {
  (void)in_sizes; (void)n_in; (void)out_size; (void)ws_size;
  (void)hipFuncSetAttribute((const void*)mha_fused, hipFuncAttributeMaxDynamicSharedMemorySize, L_BYTES);
  hipLaunchKernelGGL(mha_fused, dim3(NWG), dim3(NTHR), L_BYTES, stream, (const unsigned short*)d_in[0], (const unsigned short*)d_in[1],
                     (const unsigned short*)d_in[2], (const unsigned short*)d_in[3], (const unsigned short*)d_in[4],
                     (unsigned short*)d_out, (unsigned char*)d_ws);
}
```

```cpp
#include <hip/hip_runtime.h>
#include <hip/hip_bf16.h>

#define GAS __attribute__((address_space(1)))
#define LAS __attribute__((address_space(3)))
typedef unsigned v4u __attribute__((ext_vector_type(4)));
typedef unsigned v2u __attribute__((ext_vector_type(2)));
typedef float f32x4 __attribute__((ext_vector_type(4)));
typedef __bf16 bf16x8 __attribute__((ext_vector_type(8)));
typedef __bf16 bf16x2 __attribute__((ext_vector_type(2)));
typedef float f32x2 __attribute__((ext_vector_type(2)));

#define AUX_WQ 2
#define AUX_WO 2
#define AUX_X 2
#define PRO_ORDER 0
#define WQ_ROT 1
#define BAR_TILES 16
#define NO_WO 0
#define ROT_H 4
#define ROT_BP 1
#define ROT_BETA 8
#define POLL_SLEEP 4

namespace {

constexpr int NB = 16, SQ = 8, DM = 1024, NH = 32, SK = 2048, DH = 128;
constexpr int NWG = 256, NTHR = 512;
constexpr int NT = 16;
constexpr float CEXP = 0.08838834764831845f * 1.4426950408889634f;
constexpr int AUX_NT = 2, AUX_SC1 = 16;
constexpr int RSRC_W3 = 0x00020000;
constexpr unsigned SENT = 0xFFFFFFFFu;
constexpr unsigned kSpinCap = 1u << 16;

constexpr int WS_EPOCH = 0;
constexpr int WS_FLAG = 4096;
constexpr int WS_O = 65536;
constexpr int WS_P = WS_O + 8 * 128 * 1024;
constexpr int WS_BYTES = WS_P + 8 * 8 * 16 * 2048;

constexpr int L_WAVE = 16384;
constexpr int L_QS = 8 * L_WAVE;
constexpr int QS_STRIDE = 272;
constexpr int L_ML = L_QS + 16 * QS_STRIDE;
constexpr int L_BYTES = L_ML + 8 * 128;
constexpr int QP_STRIDE = 528;

__device__ __forceinline__ f32x4 mfma16(v4u a, v4u b, f32x4 c) {
  return __builtin_amdgcn_mfma_f32_16x16x32_bf16(__builtin_bit_cast(bf16x8, a), __builtin_bit_cast(bf16x8, b), c, 0, 0, 0);
}
__device__ __forceinline__ unsigned pack_lo(unsigned a, unsigned b) { return __builtin_amdgcn_perm(b, a, 0x05040100u); }
__device__ __forceinline__ unsigned pack_hi(unsigned a, unsigned b) { return __builtin_amdgcn_perm(b, a, 0x07060302u); }
__device__ __forceinline__ unsigned cvt_pk(float lo, float hi) {
  f32x2 v = {lo, hi};
  return __builtin_bit_cast(unsigned, __builtin_convertvector(v, bf16x2));
}
__device__ __forceinline__ v4u tr_frag(const v4u (&R)[8], int e) {
  v4u f;
#pragma unroll
  for (int jj = 0; jj < 4; ++jj)
    f[jj] = (e & 1) ? pack_hi(R[2 * jj][e >> 1], R[2 * jj + 1][e >> 1]) : pack_lo(R[2 * jj][e >> 1], R[2 * jj + 1][e >> 1]);
  return f;
}
__device__ __forceinline__ float ror8(float x) {
  return __builtin_bit_cast(float, __builtin_amdgcn_mov_dpp(__builtin_bit_cast(int, x), 0x128, 0xf, 0xf, true));
}
__device__ __forceinline__ float bsel(unsigned mask, float a, float b) {
  return __uint_as_float((__float_as_uint(a) & mask) | (__float_as_uint(b) & ~mask));
}
__device__ __forceinline__ float xg_max(float x) {
  auto s = __builtin_amdgcn_permlane16_swap(__float_as_uint(x), __float_as_uint(x), false, false);
  x = fmaxf(__uint_as_float(s[0]), __uint_as_float(s[1]));
  auto t = __builtin_amdgcn_permlane32_swap(__float_as_uint(x), __float_as_uint(x), false, false);
  return fmaxf(__uint_as_float(t[0]), __uint_as_float(t[1]));
}
__device__ __forceinline__ float xg_sum(float x) {
  auto s = __builtin_amdgcn_permlane16_swap(__float_as_uint(x), __float_as_uint(x), false, false);
  x = __uint_as_float(s[0]) + __uint_as_float(s[1]);
  auto t = __builtin_amdgcn_permlane32_swap(__float_as_uint(x), __float_as_uint(x), false, false);
  return __uint_as_float(t[0]) + __uint_as_float(t[1]);
}
__device__ __forceinline__ bool written(v4u v) { return (v[0] != SENT) & (v[1] != SENT) & (v[2] != SENT) & (v[3] != SENT); }
#define SB() __builtin_amdgcn_sched_barrier(0)
#define BAR() __builtin_amdgcn_s_barrier()
#define STAMP(i)

template <int V> struct IC { static constexpr int value = V; };
template <int I, int N, class F> __device__ __forceinline__ void static_for(F&& f) {
  if constexpr (I < N) { f(IC<I>{}); static_for<I + 1, N>(f); }
}
constexpr int last_issued_raw(int c) { return c < 0 ? 4 : c == 0 ? 4 : c == 1 ? 5 : c == 2 ? 5 : c + 3; }
constexpr int last_issued(int c) { return last_issued_raw(c) > 2 * NT - 1 ? 2 * NT - 1 : last_issued_raw(c); }

__global__ void __launch_bounds__(NTHR, 2)
mha_fused(const unsigned short* __restrict__ x, const unsigned short* __restrict__ kc, const unsigned short* __restrict__ vc,
          const unsigned short* __restrict__ wq, const unsigned short* __restrict__ wo, unsigned short* __restrict__ out,
          unsigned char* ws) {
  extern __shared__ __attribute__((aligned(16))) unsigned char smem[];
  LAS unsigned char* L = (LAS unsigned char*)smem;
  const int tid = threadIdx.x, lane = tid & 63, w = __builtin_amdgcn_readfirstlane(tid >> 6);
  const int r = lane & 15, g = lane >> 4;
  const int wg = blockIdx.x, idx = wg >> 3, xg = wg & 7, i4 = idx & 3;
  const int h = 4 * xg + i4, bp = idx >> 2;
  const int kq = xg >> 1, ct = 4 * (xg & 1) + ((i4 + 2) & 3);
  const int beta = w >> 2, ws4 = w & 3;
  const int bh = (2 * bp + beta) * NH + h;
  const int rot = (ROT_H * h + ROT_BP * bp + ROT_BETA * beta) & (NT - 1);
  LAS unsigned char* LW = L + w * L_WAVE;
  STAMP(0);

  const __amdgpu_buffer_rsrc_t rk = __builtin_amdgcn_make_buffer_rsrc((void*)(kc + (size_t)bh * SK * DH), 0, SK * DH * 2, RSRC_W3);
  const __amdgpu_buffer_rsrc_t rv = __builtin_amdgcn_make_buffer_rsrc((void*)(vc + (size_t)bh * SK * DH), 0, SK * DH * 2, RSRC_W3);
  const __amdgpu_buffer_rsrc_t rws = __builtin_amdgcn_make_buffer_rsrc((void*)ws, 0, WS_BYTES, RSRC_W3);
  const __amdgpu_buffer_rsrc_t rwq = __builtin_amdgcn_make_buffer_rsrc((void*)wq, 0, DM * NH * DH * 2, RSRC_W3);
  const __amdgpu_buffer_rsrc_t rwo = __builtin_amdgcn_make_buffer_rsrc((void*)wo, 0, NH * DH * DM * 2, RSRC_W3);
  const __amdgpu_buffer_rsrc_t rx = __builtin_amdgcn_make_buffer_rsrc((void*)x, 0, NB * SQ * DM * 2, RSRC_W3);

  GAS unsigned short* outp = (GAS unsigned short*)out;
  asm volatile("" : "+s"(outp));

  const int koff = (4 * ((r >> 1) & 1) + (r >> 2)) * 256 + (2 * g + (r & 1)) * 16;
  const int voff = lane * 16;
  v4u item[2 * NT][8];
#define TILE_OFF(t) (((((t) + rot) & (NT - 1)) * 4 + ws4) * 8192)
#define LOAD_ITEM(n) { const int so_ = TILE_OFF((n) >> 1); _Pragma("unroll") for (int i_ = 0; i_ < 8; ++i_) \
    item[n][i_] = ((n) & 1) ? __builtin_amdgcn_raw_buffer_load_b128(rv, voff, so_ + i_ * 1024, AUX_NT) \
                            : __builtin_amdgcn_raw_buffer_load_b128(rk, koff, so_ + (i_ >> 1) * 2048 + (i_ & 1) * 128, AUX_NT); }
  LOAD_ITEM(0); SB();

  const v2u epoch0 = __builtin_amdgcn_raw_buffer_load_b64(rws, 0, WS_EPOCH + bp * 256, AUX_SC1);
  const int o_slot = WS_O + (bp * 128 + 4 * h) * 1024;
  const int p_base = WS_P + (bp * 8 + ct) * (16 * 2048);
  const int p_own = ((lane >> 3) & 3) * 8192 + (lane >> 5) * 128 + (lane & 7) * 16;
  __builtin_amdgcn_raw_buffer_store_b64(v2u{SENT, SENT}, rws, lane * 8, o_slot + w * 512, AUX_SC1);
  __builtin_amdgcn_raw_buffer_store_b128(v4u{SENT, SENT, SENT, SENT}, rws, p_own, p_base + kq * 2048 + w * 256, AUX_SC1);
  asm volatile("" ::: "memory");
  SB();

  {
    v4u xf[4];
#define KBLK(s) ((((4 * beta + (s) + WQ_ROT * bp) & 7) << 2) | ws4)
    const int xoff = ((16 * bp + r) * DM + 8 * g) * 2;
    const int wqoff = (8 * g) * (NH * DH * 2) + r * 16;
    v4u wa[4][8];
#define LOAD_WQ(s) { const int so_ = KBLK(s) * 32 * (NH * DH * 2) + h * (DH * 2); _Pragma("unroll") for (int j_ = 0; j_ < 8; ++j_) \
      wa[s][j_] = __builtin_amdgcn_raw_buffer_load_b128(rwq, wqoff, so_ + j_ * (NH * DH * 2), AUX_WQ); }
    f32x4 qa[8];
#pragma unroll
    for (int e = 0; e < 8; ++e) qa[e] = f32x4{0.f, 0.f, 0.f, 0.f};
#define MMA_WQ(s) { _Pragma("unroll") for (int e_ = 0; e_ < 8; ++e_) qa[e_] = mfma16(tr_frag(wa[s], e_), xf[s], qa[e_]); }
#pragma unroll
    for (int s = 0; s < 4; ++s) xf[s] = __builtin_amdgcn_raw_buffer_load_b128(rx, xoff, KBLK(s) * 64, AUX_X);
    STAMP(16);
    LOAD_WQ(0); SB(); LOAD_WQ(1); SB(); LOAD_WQ(2); SB(); LOAD_WQ(3); SB();
    STAMP(17);
    BAR(); SB();
    STAMP(18);
    MMA_WQ(0); SB();
    STAMP(19);
    LOAD_ITEM(1); SB();
    STAMP(20);
    MMA_WQ(1); SB();
    LOAD_ITEM(2); SB();
    STAMP(21);
    MMA_WQ(2); SB();
    LOAD_ITEM(3); SB();
    STAMP(22);
    MMA_WQ(3); SB();
    LOAD_ITEM(4); SB();
    STAMP(23);
#pragma unroll
    for (int rho = 0; rho < 4; ++rho) {
      LAS f32x4* p = (LAS f32x4*)(LW + r * QP_STRIDE + (32 * g + 8 * rho) * 4);
      p[0] = f32x4{qa[0][rho], qa[1][rho], qa[2][rho], qa[3][rho]};
      p[1] = f32x4{qa[4][rho], qa[5][rho], qa[6][rho], qa[7][rho]};
    }
  }
  const unsigned e_lo = __builtin_amdgcn_readfirstlane(epoch0[0]), e_hi = __builtin_amdgcn_readfirstlane(epoch0[1]);
  const unsigned long long tag64 = (((unsigned long long)e_hi << 32) | e_lo) + 1ull;
  const unsigned tag_lo = (unsigned)tag64, tag_hi = (unsigned)(tag64 >> 32);
  asm volatile("s_waitcnt vmcnt(16)" ::: "memory");
  __syncthreads();
  if (w == 0) __builtin_amdgcn_raw_buffer_store_b64(v2u{tag_lo, tag_hi}, rws, r * 8, WS_FLAG + (bp * 32 + h) * 128, AUX_SC1);
  {
    const int row = tid >> 5, n4 = (tid & 31) * 4;
    f32x4 s = {0.f, 0.f, 0.f, 0.f};
#pragma unroll
    for (int ww = 0; ww < 8; ++ww) s += *(LAS f32x4*)(L + ww * L_WAVE + row * QP_STRIDE + n4 * 4);
    *(LAS v2u*)(L + L_QS + row * QS_STRIDE + n4 * 2) = v2u{cvt_pk(s[0], s[1]), cvt_pk(s[2], s[3])};
  }
  __syncthreads();
  v4u qf[2];
#pragma unroll
  for (int hf = 0; hf < 2; ++hf) qf[hf] = *(LAS v4u*)(L + L_QS + (8 * beta + (r & 7)) * QS_STRIDE + (8 * hf + 2 * g + (r >> 3)) * 16);
  unsigned hmask = (r >> 3) ? 0xFFFFFFFFu : 0u;
  asm volatile("" : "+v"(hmask));
  const unsigned pmask = hmask ^ 0x0000FFFFu;

  STAMP(1);
  v4u wr[4][8], wf[4][8];
  const int wooff = (8 * g) * (DM * 2) + r * 16;
  const int wos = (1024 * kq + 128 * w) * (DM * 2) + ct * 256;
#define LOAD_WO(ks) { _Pragma("unroll") for (int j_ = 0; j_ < 8; ++j_) \
    wr[ks][j_] = NO_WO ? v4u{0u, 0u, 0u, 0u} : __builtin_amdgcn_raw_buffer_load_b128(rwo, wooff, wos + (32 * (ks) + j_) * (DM * 2), AUX_WO); }
#define PARK_WO(ks) { _Pragma("unroll") for (int e_ = 0; e_ < 8; ++e_) *(LAS v4u*)(LW + (ks) * 8192 + e_ * 1024 + voff) = tr_frag(wr[ks], e_); }
#define KEEP_WO(ks) { _Pragma("unroll") for (int e_ = 0; e_ < 8; ++e_) { wf[ks][e_] = tr_frag(wr[ks], e_); asm volatile("" : "+v"(wf[ks][e_])); } }
#define BACK_WO(ks) { _Pragma("unroll") for (int e_ = 0; e_ < 8; ++e_) wf[ks][e_] = *(LAS v4u*)(LW + (ks) * 8192 + e_ * 1024 + voff); }

  f32x4 oacc[8];
#pragma unroll
  for (int e = 0; e < 8; ++e) oacc[e] = f32x4{0.f, 0.f, 0.f, 0.f};
  float m = -INFINITY, lsum = 0.f;
  v2u flags_v;

  static_for<0, NT>([&](auto tc) __attribute__((always_inline)) {
    constexpr int t = decltype(tc)::value;
    f32x4 sa[4];
#pragma unroll
    for (int u = 0; u < 4; ++u) sa[u] = mfma16(item[2 * t][2 * u], qf[0], f32x4{0.f, 0.f, 0.f, 0.f});
#pragma unroll
    for (int u = 0; u < 4; ++u) sa[u] = mfma16(item[2 * t][2 * u + 1], qf[1], sa[u]);
    float s[4];
#pragma unroll
    for (int u = 0; u < 4; ++u) {
      const float xx = bsel(hmask, sa[u][3], sa[u][0]), yy = bsel(hmask, sa[u][1], sa[u][2]);
      s[u] = xx + ror8(yy);
      asm volatile("" : "+v"(s[u]));
    }
    SB();
    if constexpr (t == 0) LOAD_WO(0);
    if constexpr (t == 1) LOAD_WO(1);
    if constexpr (t == NT - 3) flags_v = __builtin_amdgcn_raw_buffer_load_b64(rws, (voff << 3) & 0xF80, WS_FLAG + bp * 4096, AUX_SC1);
    if constexpr (last_issued(2 * t) > last_issued(2 * t - 1)) LOAD_ITEM(last_issued(2 * t));
    SB();
    if constexpr (t < BAR_TILES && t < NT - 1) BAR();
    if constexpr (t == NT - 1) BACK_WO(1);
    SB();
    const float mx = xg_max(fmaxf(fmaxf(s[0], s[1]), fmaxf(s[2], s[3])));
    const float mn = fmaxf(m, mx);
    const float alpha = __builtin_amdgcn_exp2f((m - mn) * CEXP);
    const float mc = mn * CEXP;
    m = mn;
    float p[4];
#pragma unroll
    for (int u = 0; u < 4; ++u) p[u] = __builtin_amdgcn_exp2f(__builtin_fmaf(s[u], CEXP, -mc));
    lsum = lsum * alpha + ((p[0] + p[1]) + (p[2] + p[3]));
    asm volatile("" : "+v"(lsum));
    const v4u pf = {cvt_pk(p[0], p[0]) & pmask, cvt_pk(p[1], p[1]) & pmask, cvt_pk(p[2], p[2]) & pmask, cvt_pk(p[3], p[3]) & pmask};
#pragma unroll
    for (int e = 0; e < 8; ++e) oacc[e] *= alpha;
#pragma unroll
    for (int e = 0; e < 8; ++e) oacc[e] = mfma16(tr_frag(item[2 * t + 1], e), pf, oacc[e]);
    SB();
    if constexpr ((t & 1) == 1) STAMP(8 + (t >> 1));
    if constexpr (t == 2) { PARK_WO(0); SB(); LOAD_WO(2); }
    if constexpr (t == 3) { PARK_WO(1); SB(); LOAD_WO(3); }
    if constexpr (t == NT - 2) BACK_WO(0);
    SB();
    if constexpr (last_issued(2 * t + 1) > last_issued(2 * t)) LOAD_ITEM(last_issued(2 * t + 1));
    SB();
  });

  STAMP(2);
  const int lane2 = __builtin_amdgcn_mbcnt_hi(~0u, __builtin_amdgcn_mbcnt_lo(~0u, 0u));
  const int r2 = lane2 & 15, g2 = lane2 >> 4;
  lsum = xg_sum(lsum);
  asm volatile("s_waitcnt lgkmcnt(0)" ::: "memory");
  SB();
#pragma unroll
  for (int rho = 0; rho < 4; ++rho) {
    LAS f32x4* p = (LAS f32x4*)(LW + r2 * QP_STRIDE + (32 * g2 + 8 * rho) * 4);
    p[0] = f32x4{oacc[0][rho], oacc[1][rho], oacc[2][rho], oacc[3][rho]};
    p[1] = f32x4{oacc[4][rho], oacc[5][rho], oacc[6][rho], oacc[7][rho]};
  }
  if (g2 == 0) *(LAS f32x2*)(L + L_ML + w * 128 + r2 * 8) = f32x2{m, lsum};
  {
    unsigned spins = 0;
    while (!__all(flags_v[0] == tag_lo && flags_v[1] == tag_hi)) {
      __builtin_amdgcn_s_sleep(2);
      if (++spins > kSpinCap) break;
      asm volatile("" ::: "memory");
      flags_v = __builtin_amdgcn_raw_buffer_load_b64(rws, (lane2 & 31) * 128, WS_FLAG + bp * 4096, AUX_SC1);
    }
    asm volatile("" ::: "memory");
  }
  const int goff = WS_O + (bp * 128 + 4 * (8 * kq + w)) * 1024;
  v4u ob[4];
#pragma unroll
  for (int ks = 0; ks < 4; ++ks) ob[ks] = __builtin_amdgcn_raw_buffer_load_b128(rws, lane2 * 16, goff + ks * 1024, AUX_SC1);
  __syncthreads();
  STAMP(3);
  {
    const int fl = 32 * (w & 1) + (lane2 >> 1), c = fl & 15;
    const int d0 = 32 * (w >> 1) + 8 * (fl >> 4) + 4 * (lane2 & 1);
    const int bt = c >> 3, q = c & 7;
    float mw[8], lw[8];
    f32x4 pv[8];
#pragma unroll
    for (int j = 0; j < 8; ++j) {
      const f32x2 ml = *(const LAS f32x2*)(L + L_ML + (4 * bt + (j >> 1)) * 128 + (q + 8 * (j & 1)) * 8);
      mw[j] = ml[0]; lw[j] = ml[1];
      pv[j] = *(const LAS f32x4*)(L + (4 * bt + (j >> 1)) * L_WAVE + (q + 8 * (j & 1)) * QP_STRIDE + d0 * 4);
    }
    const float M = fmaxf(fmaxf(fmaxf(mw[0], mw[1]), fmaxf(mw[2], mw[3])), fmaxf(fmaxf(mw[4], mw[5]), fmaxf(mw[6], mw[7])));
    float Ls = 0.f;
    f32x4 a0 = {0.f, 0.f, 0.f, 0.f};
#pragma unroll
    for (int j = 0; j < 8; ++j) {
      const float wt = __builtin_amdgcn_exp2f((mw[j] - M) * CEXP);
      Ls += wt * lw[j];
      a0 += pv[j] * wt;
    }
    const float inv = 1.0f / Ls;
    v2u ov = {cvt_pk(a0[0] * inv, a0[1] * inv), cvt_pk(a0[2] * inv, a0[3] * inv)};
#pragma unroll
    for (int j = 0; j < 2; ++j) ov[j] = (ov[j] == SENT) ? 0x7FC07FC0u : ov[j];
    __builtin_amdgcn_raw_buffer_store_b64(ov, rws, lane2 * 8, o_slot + w * 512, AUX_SC1);
    if (h == 0 && w == 0) __builtin_amdgcn_raw_buffer_store_b64(v2u{tag_lo, tag_hi}, rws, r2 * 8, WS_EPOCH + bp * 256, AUX_SC1);
  }
  asm volatile("s_waitcnt lgkmcnt(0)" ::: "memory");
  BAR();
  STAMP(4);
  {
    KEEP_WO(2); KEEP_WO(3); SB();
    unsigned spins = 0;
    while (!__all(written(ob[0]) & written(ob[1]) & written(ob[2]) & written(ob[3]))) {
      if (++spins > kSpinCap) break;
      if (spins > 1) __builtin_amdgcn_s_sleep(POLL_SLEEP);
      asm volatile("" ::: "memory");
#pragma unroll
      for (int ks = 0; ks < 4; ++ks) ob[ks] = __builtin_amdgcn_raw_buffer_load_b128(rws, lane2 * 16, goff + ks * 1024, AUX_SC1);
    }
    f32x4 c[8];
#pragma unroll
    for (int e = 0; e < 8; ++e) c[e] = f32x4{0.f, 0.f, 0.f, 0.f};
#pragma unroll
    for (int ks = 0; ks < 4; ++ks)
#pragma unroll
      for (int e = 0; e < 8; ++e) c[e] = mfma16(wf[ks][e], ob[ks], c[e]);
#pragma unroll
    for (int rho = 0; rho < 4; ++rho) {
      LAS f32x4* p = (LAS f32x4*)(LW + r2 * QP_STRIDE + (32 * g2 + 8 * rho) * 4);
      p[0] = f32x4{c[0][rho], c[1][rho], c[2][rho], c[3][rho]};
      p[1] = f32x4{c[4][rho], c[5][rho], c[6][rho], c[7][rho]};
    }
  }
  __syncthreads();
  STAMP(5);
  {
    const int row = 2 * w + (lane2 >> 5), n4 = (lane2 & 31) * 4;
    f32x4 s = {0.f, 0.f, 0.f, 0.f};
#pragma unroll
    for (int ww = 0; ww < 8; ++ww) s += *(LAS f32x4*)(L + ww * L_WAVE + row * QP_STRIDE + n4 * 4);
    v4u sv = __builtin_bit_cast(v4u, s);
#pragma unroll
    for (int j = 0; j < 4; ++j) sv[j] = (sv[j] == SENT) ? 0x7FC00000u : sv[j];
    const int p_own2 = ((lane2 >> 3) & 3) * 8192 + (lane2 >> 5) * 128 + (lane2 & 7) * 16;
    __builtin_amdgcn_raw_buffer_store_b128(sv, rws, p_own2, p_base + kq * 2048 + w * 256, AUX_SC1);
  }
  {
    const int poff = g2 * 2048 + (r2 >> 3) * 128 + (r2 & 7) * 16;
    v4u pv;
    unsigned spins = 0;
    for (;;) {
      asm volatile("" ::: "memory");
      pv = __builtin_amdgcn_raw_buffer_load_b128(rws, poff, p_base + kq * 8192 + w * 256, AUX_SC1);
      if (__all(written(pv))) break;
      if (++spins > kSpinCap) break;
      __builtin_amdgcn_s_sleep(POLL_SLEEP);
    }
    float s[4];
#pragma unroll
    for (int j = 0; j < 4; ++j) s[j] = xg_sum(__uint_as_float(pv[j]));
    if (g2 == 0) {
      const int row = 16 * bp + 2 * w + (r2 >> 3), col = 128 * ct + 32 * kq + (r2 & 7) * 4;
      *(GAS v2u*)(outp + (size_t)row * DM + col) = v2u{cvt_pk(s[0], s[1]), cvt_pk(s[2], s[3])};
    }
  }
}

}

extern "C" void kernel_launch(void* const* d_in, const int* in_sizes, int n_in, void* d_out, int out_size, void* d_ws,
                              size_t ws_size, hipStream_t stream) {
  (void)in_sizes; (void)n_in; (void)out_size; (void)ws_size;
  (void)hipFuncSetAttribute((const void*)mha_fused, hipFuncAttributeMaxDynamicSharedMemorySize, L_BYTES);
  hipLaunchKernelGGL(mha_fused, dim3(NWG), dim3(NTHR), L_BYTES, stream, (const unsigned short*)d_in[0], (const unsigned short*)d_in[1],
                     (const unsigned short*)d_in[2], (const unsigned short*)d_in[3], (const unsigned short*)d_in[4],
                     (unsigned short*)d_out, (unsigned char*)d_ws);
}
```

```cpp
#include <hip/hip_runtime.h>
#include <hip/hip_bf16.h>

#define GAS __attribute__((address_space(1)))
#define LAS __attribute__((address_space(3)))
typedef unsigned v4u __attribute__((ext_vector_type(4)));
typedef unsigned v2u __attribute__((ext_vector_type(2)));
typedef float f32x4 __attribute__((ext_vector_type(4)));
typedef __bf16 bf16x8 __attribute__((ext_vector_type(8)));
typedef __bf16 bf16x2 __attribute__((ext_vector_type(2)));
typedef float f32x2 __attribute__((ext_vector_type(2)));

#define AUX_WQ 2
#define AUX_WO 2
#define AUX_X 2
#define PRO_ORDER 0
#define WQ_ROT 1
#define VDEF_B 16
#define BAR_TILES 16
#define NO_WO 0
#define ROT_H 4
#define ROT_BP 1
#define ROT_BETA 8
#define POLL_SLEEP 4

namespace {

constexpr int NB = 16, SQ = 8, DM = 1024, NH = 32, SK = 2048, DH = 128;
constexpr int NWG = 256, NTHR = 512;
constexpr int NT = 16;
constexpr float CEXP = 0.08838834764831845f * 1.4426950408889634f;
constexpr int AUX_NT = 2, AUX_SC1 = 16;
constexpr int RSRC_W3 = 0x00020000;
constexpr unsigned SENT = 0xFFFFFFFFu;
constexpr unsigned kSpinCap = 1u << 16;

constexpr int WS_EPOCH = 0;
constexpr int WS_FLAG = 4096;
constexpr int WS_O = 65536;
constexpr int WS_P = WS_O + 8 * 128 * 1024;
constexpr int WS_BYTES = WS_P + 8 * 8 * 16 * 2048;

constexpr int L_WAVE = 16384;
constexpr int L_QS = 8 * L_WAVE;
constexpr int QS_STRIDE = 272;
constexpr int L_ML = L_QS + 16 * QS_STRIDE;
constexpr int L_BYTES = L_ML + 8 * 128;
constexpr int QP_STRIDE = 528;

__device__ __forceinline__ f32x4 mfma16(v4u a, v4u b, f32x4 c) {
  return __builtin_amdgcn_mfma_f32_16x16x32_bf16(__builtin_bit_cast(bf16x8, a), __builtin_bit_cast(bf16x8, b), c, 0, 0, 0);
}
__device__ __forceinline__ unsigned pack_lo(unsigned a, unsigned b) { return __builtin_amdgcn_perm(b, a, 0x05040100u); }
__device__ __forceinline__ unsigned pack_hi(unsigned a, unsigned b) { return __builtin_amdgcn_perm(b, a, 0x07060302u); }
__device__ __forceinline__ unsigned cvt_pk(float lo, float hi) {
  f32x2 v = {lo, hi};
  return __builtin_bit_cast(unsigned, __builtin_convertvector(v, bf16x2));
}
__device__ __forceinline__ v4u tr_frag(const v4u (&R)[8], int e) {
  v4u f;
#pragma unroll
  for (int jj = 0; jj < 4; ++jj)
    f[jj] = (e & 1) ? pack_hi(R[2 * jj][e >> 1], R[2 * jj + 1][e >> 1]) : pack_lo(R[2 * jj][e >> 1], R[2 * jj + 1][e >> 1]);
  return f;
}
__device__ __forceinline__ float ror8(float x) {
  return __builtin_bit_cast(float, __builtin_amdgcn_mov_dpp(__builtin_bit_cast(int, x), 0x128, 0xf, 0xf, true));
}
__device__ __forceinline__ float bsel(unsigned mask, float a, float b) {
  return __uint_as_float((__float_as_uint(a) & mask) | (__float_as_uint(b) & ~mask));
}
__device__ __forceinline__ float xg_max(float x) {
  auto s = __builtin_amdgcn_permlane16_swap(__float_as_uint(x), __float_as_uint(x), false, false);
  x = fmaxf(__uint_as_float(s[0]), __uint_as_float(s[1]));
  auto t = __builtin_amdgcn_permlane32_swap(__float_as_uint(x), __float_as_uint(x), false, false);
  return fmaxf(__uint_as_float(t[0]), __uint_as_float(t[1]));
}
__device__ __forceinline__ float xg_sum(float x) {
  auto s = __builtin_amdgcn_permlane16_swap(__float_as_uint(x), __float_as_uint(x), false, false);
  x = __uint_as_float(s[0]) + __uint_as_float(s[1]);
  auto t = __builtin_amdgcn_permlane32_swap(__float_as_uint(x), __float_as_uint(x), false, false);
  return __uint_as_float(t[0]) + __uint_as_float(t[1]);
}
__device__ __forceinline__ bool written(v4u v) { return (v[0] != SENT) & (v[1] != SENT) & (v[2] != SENT) & (v[3] != SENT); }
#define SB() __builtin_amdgcn_sched_barrier(0)
#define BAR() __builtin_amdgcn_s_barrier()
#define STAMP(i)

template <int V> struct IC { static constexpr int value = V; };
template <int I, int N, class F> __device__ __forceinline__ void static_for(F&& f) {
  if constexpr (I < N) { f(IC<I>{}); static_for<I + 1, N>(f); }
}
constexpr int last_issued_raw(int c) { return c < 0 ? 4 : c == 0 ? 4 : c == 1 ? 5 : c == 2 ? 5 : c + 3; }
constexpr int last_issued(int c) { return last_issued_raw(c) > 2 * NT - 1 ? 2 * NT - 1 : last_issued_raw(c); }

__global__ void __launch_bounds__(NTHR, 2)
mha_fused(const unsigned short* __restrict__ x, const unsigned short* __restrict__ kc, const unsigned short* __restrict__ vc,
          const unsigned short* __restrict__ wq, const unsigned short* __restrict__ wo, unsigned short* __restrict__ out,
          unsigned char* ws) {
  extern __shared__ __attribute__((aligned(16))) unsigned char smem[];
  LAS unsigned char* L = (LAS unsigned char*)smem;
  const int tid = threadIdx.x, lane = tid & 63, w = __builtin_amdgcn_readfirstlane(tid >> 6);
  const int r = lane & 15, g = lane >> 4;
  const int wg = blockIdx.x, idx = wg >> 3, xg = wg & 7, i4 = idx & 3;
  const int h = 4 * xg + i4, bp = idx >> 2;
  const int kq = xg >> 1, ct = 4 * (xg & 1) + ((i4 + 2) & 3);
  const int beta = w >> 2, ws4 = w & 3;
  const int bh = (2 * bp + beta) * NH + h;
  const int rot = (ROT_H * h + ROT_BP * bp + ROT_BETA * beta) & (NT - 1);
  LAS unsigned char* LW = L + w * L_WAVE;
  STAMP(0);

  const __amdgpu_buffer_rsrc_t rk = __builtin_amdgcn_make_buffer_rsrc((void*)(kc + (size_t)bh * SK * DH), 0, SK * DH * 2, RSRC_W3);
  const __amdgpu_buffer_rsrc_t rv = __builtin_amdgcn_make_buffer_rsrc((void*)(vc + (size_t)bh * SK * DH), 0, SK * DH * 2, RSRC_W3);
  const __amdgpu_buffer_rsrc_t rws = __builtin_amdgcn_make_buffer_rsrc((void*)ws, 0, WS_BYTES, RSRC_W3);
  const __amdgpu_buffer_rsrc_t rwq = __builtin_amdgcn_make_buffer_rsrc((void*)wq, 0, DM * NH * DH * 2, RSRC_W3);
  const __amdgpu_buffer_rsrc_t rwo = __builtin_amdgcn_make_buffer_rsrc((void*)wo, 0, NH * DH * DM * 2, RSRC_W3);
  const __amdgpu_buffer_rsrc_t rx = __builtin_amdgcn_make_buffer_rsrc((void*)x, 0, NB * SQ * DM * 2, RSRC_W3);

  GAS unsigned short* outp = (GAS unsigned short*)out;
  asm volatile("" : "+s"(outp));

  const int koff = (4 * ((r >> 1) & 1) + (r >> 2)) * 256 + (2 * g + (r & 1)) * 16;
  const int voff = lane * 16;
  v4u item[2 * NT][8];
#define TILE_OFF(t) (((((t) + rot) & (NT - 1)) * 4 + ws4) * 8192)
#define LOAD_ITEM(n) { const int so_ = TILE_OFF((n) >> 1); \
    if (((n) & 1) && VDEF_B < 16 && 2 * bp + beta >= VDEF_B) { _Pragma("unroll") for (int i_ = 0; i_ < 8; ++i_) \
      item[n][i_] = __builtin_amdgcn_raw_buffer_load_b128(rv, voff, so_ + i_ * 1024, 0); } \
    else { _Pragma("unroll") for (int i_ = 0; i_ < 8; ++i_) \
    item[n][i_] = ((n) & 1) ? __builtin_amdgcn_raw_buffer_load_b128(rv, voff, so_ + i_ * 1024, AUX_NT) \
                            : __builtin_amdgcn_raw_buffer_load_b128(rk, koff, so_ + (i_ >> 1) * 2048 + (i_ & 1) * 128, AUX_NT); } }
  LOAD_ITEM(0); SB();

  const v2u epoch0 = __builtin_amdgcn_raw_buffer_load_b64(rws, 0, WS_EPOCH + bp * 256, AUX_SC1);
  const int o_slot = WS_O + (bp * 128 + 4 * h) * 1024;
  const int p_base = WS_P + (bp * 8 + ct) * (16 * 2048);
  const int p_own = ((lane >> 3) & 3) * 8192 + (lane >> 5) * 128 + (lane & 7) * 16;
  __builtin_amdgcn_raw_buffer_store_b64(v2u{SENT, SENT}, rws, lane * 8, o_slot + w * 512, AUX_SC1);
  __builtin_amdgcn_raw_buffer_store_b128(v4u{SENT, SENT, SENT, SENT}, rws, p_own, p_base + kq * 2048 + w * 256, AUX_SC1);
  asm volatile("" ::: "memory");
  SB();

  {
    v4u xf[4];
#define KBLK(s) ((((4 * beta + (s) + WQ_ROT * bp) & 7) << 2) | ws4)
    const int xoff = ((16 * bp + r) * DM + 8 * g) * 2;
    const int wqoff = (8 * g) * (NH * DH * 2) + r * 16;
    v4u wa[4][8];
#define LOAD_WQ(s) { const int so_ = KBLK(s) * 32 * (NH * DH * 2) + h * (DH * 2); _Pragma("unroll") for (int j_ = 0; j_ < 8; ++j_) \
      wa[s][j_] = __builtin_amdgcn_raw_buffer_load_b128(rwq, wqoff, so_ + j_ * (NH * DH * 2), AUX_WQ); }
    f32x4 qa[8];
#pragma unroll
    for (int e = 0; e < 8; ++e) qa[e] = f32x4{0.f, 0.f, 0.f, 0.f};
#define MMA_WQ(s) { _Pragma("unroll") for (int e_ = 0; e_ < 8; ++e_) qa[e_] = mfma16(tr_frag(wa[s], e_), xf[s], qa[e_]); }
#pragma unroll
    for (int s = 0; s < 4; ++s) xf[s] = __builtin_amdgcn_raw_buffer_load_b128(rx, xoff, KBLK(s) * 64, AUX_X);
    STAMP(16);
    LOAD_WQ(0); SB(); LOAD_WQ(1); SB(); LOAD_WQ(2); SB(); LOAD_WQ(3); SB();
    STAMP(17);
    BAR(); SB();
    STAMP(18);
    MMA_WQ(0); SB();
    STAMP(19);
    LOAD_ITEM(1); SB();
    STAMP(20);
    MMA_WQ(1); SB();
    LOAD_ITEM(2); SB();
    STAMP(21);
    MMA_WQ(2); SB();
    LOAD_ITEM(3); SB();
    STAMP(22);
    MMA_WQ(3); SB();
    LOAD_ITEM(4); SB();
    STAMP(23);
#pragma unroll
    for (int rho = 0; rho < 4; ++rho) {
      LAS f32x4* p = (LAS f32x4*)(LW + r * QP_STRIDE + (32 * g + 8 * rho) * 4);
      p[0] = f32x4{qa[0][rho], qa[1][rho], qa[2][rho], qa[3][rho]};
      p[1] = f32x4{qa[4][rho], qa[5][rho], qa[6][rho], qa[7][rho]};
    }
  }
  const unsigned e_lo = __builtin_amdgcn_readfirstlane(epoch0[0]), e_hi = __builtin_amdgcn_readfirstlane(epoch0[1]);
  const unsigned long long tag64 = (((unsigned long long)e_hi << 32) | e_lo) + 1ull;
  const unsigned tag_lo = (unsigned)tag64, tag_hi = (unsigned)(tag64 >> 32);
  asm volatile("s_waitcnt vmcnt(16)" ::: "memory");
  __syncthreads();
  if (w == 0) __builtin_amdgcn_raw_buffer_store_b64(v2u{tag_lo, tag_hi}, rws, r * 8, WS_FLAG + (bp * 32 + h) * 128, AUX_SC1);
  {
    const int row = tid >> 5, n4 = (tid & 31) * 4;
    f32x4 s = {0.f, 0.f, 0.f, 0.f};
#pragma unroll
    for (int ww = 0; ww < 8; ++ww) s += *(LAS f32x4*)(L + ww * L_WAVE + row * QP_STRIDE + n4 * 4);
    *(LAS v2u*)(L + L_QS + row * QS_STRIDE + n4 * 2) = v2u{cvt_pk(s[0], s[1]), cvt_pk(s[2], s[3])};
  }
  __syncthreads();
  v4u qf[2];
#pragma unroll
  for (int hf = 0; hf < 2; ++hf) qf[hf] = *(LAS v4u*)(L + L_QS + (8 * beta + (r & 7)) * QS_STRIDE + (8 * hf + 2 * g + (r >> 3)) * 16);
  unsigned hmask = (r >> 3) ? 0xFFFFFFFFu : 0u;
  asm volatile("" : "+v"(hmask));
  const unsigned pmask = hmask ^ 0x0000FFFFu;

  STAMP(1);
  v4u wr[4][8], wf[4][8];
  const int wooff = (8 * g) * (DM * 2) + r * 16;
  const int wos = (1024 * kq + 128 * w) * (DM * 2) + ct * 256;
#define LOAD_WO(ks) { _Pragma("unroll") for (int j_ = 0; j_ < 8; ++j_) \
    wr[ks][j_] = NO_WO ? v4u{0u, 0u, 0u, 0u} : __builtin_amdgcn_raw_buffer_load_b128(rwo, wooff, wos + (32 * (ks) + j_) * (DM * 2), AUX_WO); }
#define PARK_WO(ks) { _Pragma("unroll") for (int e_ = 0; e_ < 8; ++e_) *(LAS v4u*)(LW + (ks) * 8192 + e_ * 1024 + voff) = tr_frag(wr[ks], e_); }
#define KEEP_WO(ks) { _Pragma("unroll") for (int e_ = 0; e_ < 8; ++e_) { wf[ks][e_] = tr_frag(wr[ks], e_); asm volatile("" : "+v"(wf[ks][e_])); } }
#define BACK_WO(ks) { _Pragma("unroll") for (int e_ = 0; e_ < 8; ++e_) wf[ks][e_] = *(LAS v4u*)(LW + (ks) * 8192 + e_ * 1024 + voff); }

  f32x4 oacc[8];
#pragma unroll
  for (int e = 0; e < 8; ++e) oacc[e] = f32x4{0.f, 0.f, 0.f, 0.f};
  float m = -INFINITY, lsum = 0.f;
  v2u flags_v;

  static_for<0, NT>([&](auto tc) __attribute__((always_inline)) {
    constexpr int t = decltype(tc)::value;
    f32x4 sa[4];
#pragma unroll
    for (int u = 0; u < 4; ++u) sa[u] = mfma16(item[2 * t][2 * u], qf[0], f32x4{0.f, 0.f, 0.f, 0.f});
#pragma unroll
    for (int u = 0; u < 4; ++u) sa[u] = mfma16(item[2 * t][2 * u + 1], qf[1], sa[u]);
    float s[4];
#pragma unroll
    for (int u = 0; u < 4; ++u) {
      const float xx = bsel(hmask, sa[u][3], sa[u][0]), yy = bsel(hmask, sa[u][1], sa[u][2]);
      s[u] = xx + ror8(yy);
      asm volatile("" : "+v"(s[u]));
    }
    SB();
    if constexpr (t == 0) LOAD_WO(0);
    if constexpr (t == 1) LOAD_WO(1);
    if constexpr (t == NT - 3) flags_v = __builtin_amdgcn_raw_buffer_load_b64(rws, (voff << 3) & 0xF80, WS_FLAG + bp * 4096, AUX_SC1);
    if constexpr (last_issued(2 * t) > last_issued(2 * t - 1)) LOAD_ITEM(last_issued(2 * t));
    SB();
    if constexpr (t < BAR_TILES && t < NT - 1) BAR();
    if constexpr (t == NT - 1) BACK_WO(1);
    SB();
    const float mx = xg_max(fmaxf(fmaxf(s[0], s[1]), fmaxf(s[2], s[3])));
    const float mn = fmaxf(m, mx);
    const float alpha = __builtin_amdgcn_exp2f((m - mn) * CEXP);
    const float mc = mn * CEXP;
    m = mn;
    float p[4];
#pragma unroll
    for (int u = 0; u < 4; ++u) p[u] = __builtin_amdgcn_exp2f(__builtin_fmaf(s[u], CEXP, -mc));
    lsum = lsum * alpha + ((p[0] + p[1]) + (p[2] + p[3]));
    asm volatile("" : "+v"(lsum));
    const v4u pf = {cvt_pk(p[0], p[0]) & pmask, cvt_pk(p[1], p[1]) & pmask, cvt_pk(p[2], p[2]) & pmask, cvt_pk(p[3], p[3]) & pmask};
#pragma unroll
    for (int e = 0; e < 8; ++e) oacc[e] *= alpha;
#pragma unroll
    for (int e = 0; e < 8; ++e) oacc[e] = mfma16(tr_frag(item[2 * t + 1], e), pf, oacc[e]);
    SB();
    if constexpr ((t & 1) == 1) STAMP(8 + (t >> 1));
    if constexpr (t == 2) { PARK_WO(0); SB(); LOAD_WO(2); }
    if constexpr (t == 3) { PARK_WO(1); SB(); LOAD_WO(3); }
    if constexpr (t == NT - 2) BACK_WO(0);
    SB();
    if constexpr (last_issued(2 * t + 1) > last_issued(2 * t)) LOAD_ITEM(last_issued(2 * t + 1));
    SB();
  });

  STAMP(2);
  const int lane2 = __builtin_amdgcn_mbcnt_hi(~0u, __builtin_amdgcn_mbcnt_lo(~0u, 0u));
  const int r2 = lane2 & 15, g2 = lane2 >> 4;
  lsum = xg_sum(lsum);
  asm volatile("s_waitcnt lgkmcnt(0)" ::: "memory");
  SB();
#pragma unroll
  for (int rho = 0; rho < 4; ++rho) {
    LAS f32x4* p = (LAS f32x4*)(LW + r2 * QP_STRIDE + (32 * g2 + 8 * rho) * 4);
    p[0] = f32x4{oacc[0][rho], oacc[1][rho], oacc[2][rho], oacc[3][rho]};
    p[1] = f32x4{oacc[4][rho], oacc[5][rho], oacc[6][rho], oacc[7][rho]};
  }
  if (g2 == 0) *(LAS f32x2*)(L + L_ML + w * 128 + r2 * 8) = f32x2{m, lsum};
  {
    unsigned spins = 0;
    while (!__all(flags_v[0] == tag_lo && flags_v[1] == tag_hi)) {
      __builtin_amdgcn_s_sleep(2);
      if (++spins > kSpinCap) break;
      asm volatile("" ::: "memory");
      flags_v = __builtin_amdgcn_raw_buffer_load_b64(rws, (lane2 & 31) * 128, WS_FLAG + bp * 4096, AUX_SC1);
    }
    asm volatile("" ::: "memory");
  }
  const int goff = WS_O + (bp * 128 + 4 * (8 * kq + w)) * 1024;
  v4u ob[4];
#pragma unroll
  for (int ks = 0; ks < 4; ++ks) ob[ks] = __builtin_amdgcn_raw_buffer_load_b128(rws, lane2 * 16, goff + ks * 1024, AUX_SC1);
  __syncthreads();
  STAMP(3);
  {
    const int fl = 32 * (w & 1) + (lane2 >> 1), c = fl & 15;
    const int d0 = 32 * (w >> 1) + 8 * (fl >> 4) + 4 * (lane2 & 1);
    const int bt = c >> 3, q = c & 7;
    float mw[8], lw[8];
    f32x4 pv[8];
#pragma unroll
    for (int j = 0; j < 8; ++j) {
      const f32x2 ml = *(const LAS f32x2*)(L + L_ML + (4 * bt + (j >> 1)) * 128 + (q + 8 * (j & 1)) * 8);
      mw[j] = ml[0]; lw[j] = ml[1];
      pv[j] = *(const LAS f32x4*)(L + (4 * bt + (j >> 1)) * L_WAVE + (q + 8 * (j & 1)) * QP_STRIDE + d0 * 4);
    }
    const float M = fmaxf(fmaxf(fmaxf(mw[0], mw[1]), fmaxf(mw[2], mw[3])), fmaxf(fmaxf(mw[4], mw[5]), fmaxf(mw[6], mw[7])));
    float Ls = 0.f;
    f32x4 a0 = {0.f, 0.f, 0.f, 0.f};
#pragma unroll
    for (int j = 0; j < 8; ++j) {
      const float wt = __builtin_amdgcn_exp2f((mw[j] - M) * CEXP);
      Ls += wt * lw[j];
      a0 += pv[j] * wt;
    }
    const float inv = 1.0f / Ls;
    v2u ov = {cvt_pk(a0[0] * inv, a0[1] * inv), cvt_pk(a0[2] * inv, a0[3] * inv)};
#pragma unroll
    for (int j = 0; j < 2; ++j) ov[j] = (ov[j] == SENT) ? 0x7FC07FC0u : ov[j];
    __builtin_amdgcn_raw_buffer_store_b64(ov, rws, lane2 * 8, o_slot + w * 512, AUX_SC1);
    if (h == 0 && w == 0) __builtin_amdgcn_raw_buffer_store_b64(v2u{tag_lo, tag_hi}, rws, r2 * 8, WS_EPOCH + bp * 256, AUX_SC1);
  }
  asm volatile("s_waitcnt lgkmcnt(0)" ::: "memory");
  BAR();
  STAMP(4);
  {
    KEEP_WO(2); KEEP_WO(3); SB();
    unsigned spins = 0;
    while (!__all(written(ob[0]) & written(ob[1]) & written(ob[2]) & written(ob[3]))) {
      if (++spins > kSpinCap) break;
      if (spins > 1) __builtin_amdgcn_s_sleep(POLL_SLEEP);
      asm volatile("" ::: "memory");
#pragma unroll
      for (int ks = 0; ks < 4; ++ks) ob[ks] = __builtin_amdgcn_raw_buffer_load_b128(rws, lane2 * 16, goff + ks * 1024, AUX_SC1);
    }
    f32x4 c[8];
#pragma unroll
    for (int e = 0; e < 8; ++e) c[e] = f32x4{0.f, 0.f, 0.f, 0.f};
#pragma unroll
    for (int ks = 0; ks < 4; ++ks)
#pragma unroll
      for (int e = 0; e < 8; ++e) c[e] = mfma16(wf[ks][e], ob[ks], c[e]);
#pragma unroll
    for (int rho = 0; rho < 4; ++rho) {
      LAS f32x4* p = (LAS f32x4*)(LW + r2 * QP_STRIDE + (32 * g2 + 8 * rho) * 4);
      p[0] = f32x4{c[0][rho], c[1][rho], c[2][rho], c[3][rho]};
      p[1] = f32x4{c[4][rho], c[5][rho], c[6][rho], c[7][rho]};
    }
  }
  __syncthreads();
  STAMP(5);
  {
    const int row = 2 * w + (lane2 >> 5), n4 = (lane2 & 31) * 4;
    f32x4 s = {0.f, 0.f, 0.f, 0.f};
#pragma unroll
    for (int ww = 0; ww < 8; ++ww) s += *(LAS f32x4*)(L + ww * L_WAVE + row * QP_STRIDE + n4 * 4);
    v4u sv = __builtin_bit_cast(v4u, s);
#pragma unroll
    for (int j = 0; j < 4; ++j) sv[j] = (sv[j] == SENT) ? 0x7FC00000u : sv[j];
    const int p_own2 = ((lane2 >> 3) & 3) * 8192 + (lane2 >> 5) * 128 + (lane2 & 7) * 16;
    __builtin_amdgcn_raw_buffer_store_b128(sv, rws, p_own2, p_base + kq * 2048 + w * 256, AUX_SC1);
  }
  {
    const int poff = g2 * 2048 + (r2 >> 3) * 128 + (r2 & 7) * 16;
    v4u pv;
    unsigned spins = 0;
    for (;;) {
      asm volatile("" ::: "memory");
      pv = __builtin_amdgcn_raw_buffer_load_b128(rws, poff, p_base + kq * 8192 + w * 256, AUX_SC1);
      if (__all(written(pv))) break;
      if (++spins > kSpinCap) break;
      __builtin_amdgcn_s_sleep(POLL_SLEEP);
    }
    float s[4];
#pragma unroll
    for (int j = 0; j < 4; ++j) s[j] = xg_sum(__uint_as_float(pv[j]));
    if (g2 == 0) {
      const int row = 16 * bp + 2 * w + (r2 >> 3), col = 128 * ct + 32 * kq + (r2 & 7) * 4;
      *(GAS v2u*)(outp + (size_t)row * DM + col) = v2u{cvt_pk(s[0], s[1]), cvt_pk(s[2], s[3])};
    }
  }
}

}

extern "C" void kernel_launch(void* const* d_in, const int* in_sizes, int n_in, void* d_out, int out_size, void* d_ws,
                              size_t ws_size, hipStream_t stream) {
  (void)in_sizes; (void)n_in; (void)out_size; (void)ws_size;
  (void)hipFuncSetAttribute((const void*)mha_fused, hipFuncAttributeMaxDynamicSharedMemorySize, L_BYTES);
  hipLaunchKernelGGL(mha_fused, dim3(NWG), dim3(NTHR), L_BYTES, stream, (const unsigned short*)d_in[0], (const unsigned short*)d_in[1],
                     (const unsigned short*)d_in[2], (const unsigned short*)d_in[3], (const unsigned short*)d_in[4],
                     (unsigned short*)d_out, (unsigned char*)d_ws);
}
```

```cpp
#define AUX_WQ 0
#define AUX_WO 0
#define AUX_X 0
#define VDEF_B 14
#include <hip/hip_runtime.h>
#include <hip/hip_bf16.h>

#define GAS __attribute__((address_space(1)))
#define LAS __attribute__((address_space(3)))
typedef unsigned v4u __attribute__((ext_vector_type(4)));
typedef unsigned v2u __attribute__((ext_vector_type(2)));
typedef float f32x4 __attribute__((ext_vector_type(4)));
typedef __bf16 bf16x8 __attribute__((ext_vector_type(8)));
typedef __bf16 bf16x2 __attribute__((ext_vector_type(2)));
typedef float f32x2 __attribute__((ext_vector_type(2)));

#define PRO_ORDER 0
#define WQ_ROT 1
#define BAR_TILES 16
#define NO_WO 0
#define ROT_H 4
#define ROT_BP 1
#define ROT_BETA 8
#define POLL_SLEEP 4

namespace {

constexpr int NB = 16, SQ = 8, DM = 1024, NH = 32, SK = 2048, DH = 128;
constexpr int NWG = 256, NTHR = 512;
constexpr int NT = 16;
constexpr float CEXP = 0.08838834764831845f * 1.4426950408889634f;
constexpr int AUX_NT = 2, AUX_SC1 = 16;
constexpr int RSRC_W3 = 0x00020000;
constexpr unsigned SENT = 0xFFFFFFFFu;
constexpr unsigned kSpinCap = 1u << 16;

constexpr int WS_EPOCH = 0;
constexpr int WS_FLAG = 4096;
constexpr int WS_O = 65536;
constexpr int WS_P = WS_O + 8 * 128 * 1024;
constexpr int WS_BYTES = WS_P + 8 * 8 * 16 * 2048;

constexpr int L_WAVE = 16384;
constexpr int L_QS = 8 * L_WAVE;
constexpr int QS_STRIDE = 272;
constexpr int L_ML = L_QS + 16 * QS_STRIDE;
constexpr int L_BYTES = L_ML + 8 * 128;
constexpr int QP_STRIDE = 528;

__device__ __forceinline__ f32x4 mfma16(v4u a, v4u b, f32x4 c) {
  return __builtin_amdgcn_mfma_f32_16x16x32_bf16(__builtin_bit_cast(bf16x8, a), __builtin_bit_cast(bf16x8, b), c, 0, 0, 0);
}
__device__ __forceinline__ unsigned pack_lo(unsigned a, unsigned b) { return __builtin_amdgcn_perm(b, a, 0x05040100u); }
__device__ __forceinline__ unsigned pack_hi(unsigned a, unsigned b) { return __builtin_amdgcn_perm(b, a, 0x07060302u); }
__device__ __forceinline__ unsigned cvt_pk(float lo, float hi) {
  f32x2 v = {lo, hi};
  return __builtin_bit_cast(unsigned, __builtin_convertvector(v, bf16x2));
}
__device__ __forceinline__ v4u tr_frag(const v4u (&R)[8], int e) {
  v4u f;
#pragma unroll
  for (int jj = 0; jj < 4; ++jj)
    f[jj] = (e & 1) ? pack_hi(R[2 * jj][e >> 1], R[2 * jj + 1][e >> 1]) : pack_lo(R[2 * jj][e >> 1], R[2 * jj + 1][e >> 1]);
  return f;
}
__device__ __forceinline__ float ror8(float x) {
  return __builtin_bit_cast(float, __builtin_amdgcn_mov_dpp(__builtin_bit_cast(int, x), 0x128, 0xf, 0xf, true));
}
__device__ __forceinline__ float bsel(unsigned mask, float a, float b) {
  return __uint_as_float((__float_as_uint(a) & mask) | (__float_as_uint(b) & ~mask));
}
__device__ __forceinline__ float xg_max(float x) {
  auto s = __builtin_amdgcn_permlane16_swap(__float_as_uint(x), __float_as_uint(x), false, false);
  x = fmaxf(__uint_as_float(s[0]), __uint_as_float(s[1]));
  auto t = __builtin_amdgcn_permlane32_swap(__float_as_uint(x), __float_as_uint(x), false, false);
  return fmaxf(__uint_as_float(t[0]), __uint_as_float(t[1]));
}
__device__ __forceinline__ float xg_sum(float x) {
  auto s = __builtin_amdgcn_permlane16_swap(__float_as_uint(x), __float_as_uint(x), false, false);
  x = __uint_as_float(s[0]) + __uint_as_float(s[1]);
  auto t = __builtin_amdgcn_permlane32_swap(__float_as_uint(x), __float_as_uint(x), false, false);
  return __uint_as_float(t[0]) + __uint_as_float(t[1]);
}
__device__ __forceinline__ bool written(v4u v) { return (v[0] != SENT) & (v[1] != SENT) & (v[2] != SENT) & (v[3] != SENT); }
#define SB() __builtin_amdgcn_sched_barrier(0)
#define BAR() __builtin_amdgcn_s_barrier()
#define STAMP(i)

template <int V> struct IC { static constexpr int value = V; };
template <int I, int N, class F> __device__ __forceinline__ void static_for(F&& f) {
  if constexpr (I < N) { f(IC<I>{}); static_for<I + 1, N>(f); }
}
constexpr int last_issued_raw(int c) { return c < 0 ? 4 : c == 0 ? 4 : c == 1 ? 5 : c == 2 ? 5 : c + 3; }
constexpr int last_issued(int c) { return last_issued_raw(c) > 2 * NT - 1 ? 2 * NT - 1 : last_issued_raw(c); }

__global__ void __launch_bounds__(NTHR, 2)
mha_fused(const unsigned short* __restrict__ x, const unsigned short* __restrict__ kc, const unsigned short* __restrict__ vc,
          const unsigned short* __restrict__ wq, const unsigned short* __restrict__ wo, unsigned short* __restrict__ out,
          unsigned char* ws) {
  extern __shared__ __attribute__((aligned(16))) unsigned char smem[];
  LAS unsigned char* L = (LAS unsigned char*)smem;
  const int tid = threadIdx.x, lane = tid & 63, w = __builtin_amdgcn_readfirstlane(tid >> 6);
  const int r = lane & 15, g = lane >> 4;
  const int wg = blockIdx.x, idx = wg >> 3, xg = wg & 7, i4 = idx & 3;
  const int h = 4 * xg + i4, bp = idx >> 2;
  const int kq = xg >> 1, ct = 4 * (xg & 1) + ((i4 + 2) & 3);
  const int beta = w >> 2, ws4 = w & 3;
  const int bh = (2 * bp + beta) * NH + h;
  const int rot = (ROT_H * h + ROT_BP * bp + ROT_BETA * beta) & (NT - 1);
  LAS unsigned char* LW = L + w * L_WAVE;
  STAMP(0);

  const __amdgpu_buffer_rsrc_t rk = __builtin_amdgcn_make_buffer_rsrc((void*)(kc + (size_t)bh * SK * DH), 0, SK * DH * 2, RSRC_W3);
  const __amdgpu_buffer_rsrc_t rv = __builtin_amdgcn_make_buffer_rsrc((void*)(vc + (size_t)bh * SK * DH), 0, SK * DH * 2, RSRC_W3);
  const __amdgpu_buffer_rsrc_t rws = __builtin_amdgcn_make_buffer_rsrc((void*)ws, 0, WS_BYTES, RSRC_W3);
  const __amdgpu_buffer_rsrc_t rwq = __builtin_amdgcn_make_buffer_rsrc((void*)wq, 0, DM * NH * DH * 2, RSRC_W3);
  const __amdgpu_buffer_rsrc_t rwo = __builtin_amdgcn_make_buffer_rsrc((void*)wo, 0, NH * DH * DM * 2, RSRC_W3);
  const __amdgpu_buffer_rsrc_t rx = __builtin_amdgcn_make_buffer_rsrc((void*)x, 0, NB * SQ * DM * 2, RSRC_W3);

  GAS unsigned short* outp = (GAS unsigned short*)out;
  asm volatile("" : "+s"(outp));

  const int koff = (4 * ((r >> 1) & 1) + (r >> 2)) * 256 + (2 * g + (r & 1)) * 16;
  const int voff = lane * 16;
  v4u item[2 * NT][8];
#define TILE_OFF(t) (((((t) + rot) & (NT - 1)) * 4 + ws4) * 8192)
#define LOAD_ITEM(n) { const int so_ = TILE_OFF((n) >> 1); \
    if (((n) & 1) && VDEF_B < 16 && 2 * bp + beta >= VDEF_B) { _Pragma("unroll") for (int i_ = 0; i_ < 8; ++i_) \
      item[n][i_] = __builtin_amdgcn_raw_buffer_load_b128(rv, voff, so_ + i_ * 1024, 0); } \
    else { _Pragma("unroll") for (int i_ = 0; i_ < 8; ++i_) \
    item[n][i_] = ((n) & 1) ? __builtin_amdgcn_raw_buffer_load_b128(rv, voff, so_ + i_ * 1024, AUX_NT) \
                            : __builtin_amdgcn_raw_buffer_load_b128(rk, koff, so_ + (i_ >> 1) * 2048 + (i_ & 1) * 128, AUX_NT); } }
  LOAD_ITEM(0); SB();

  const v2u epoch0 = __builtin_amdgcn_raw_buffer_load_b64(rws, 0, WS_EPOCH + bp * 256, AUX_SC1);
  const int o_slot = WS_O + (bp * 128 + 4 * h) * 1024;
  const int p_base = WS_P + (bp * 8 + ct) * (16 * 2048);
  const int p_own = ((lane >> 3) & 3) * 8192 + (lane >> 5) * 128 + (lane & 7) * 16;
  __builtin_amdgcn_raw_buffer_store_b64(v2u{SENT, SENT}, rws, lane * 8, o_slot + w * 512, AUX_SC1);
  __builtin_amdgcn_raw_buffer_store_b128(v4u{SENT, SENT, SENT, SENT}, rws, p_own, p_base + kq * 2048 + w * 256, AUX_SC1);
  asm volatile("" ::: "memory");
  SB();

  {
    v4u xf[4];
#define KBLK(s) ((((4 * beta + (s) + WQ_ROT * bp) & 7) << 2) | ws4)
    const int xoff = ((16 * bp + r) * DM + 8 * g) * 2;
    const int wqoff = (8 * g) * (NH * DH * 2) + r * 16;
    v4u wa[4][8];
#define LOAD_WQ(s) { const int so_ = KBLK(s) * 32 * (NH * DH * 2) + h * (DH * 2); _Pragma("unroll") for (int j_ = 0; j_ < 8; ++j_) \
      wa[s][j_] = __builtin_amdgcn_raw_buffer_load_b128(rwq, wqoff, so_ + j_ * (NH * DH * 2), AUX_WQ); }
    f32x4 qa[8];
#pragma unroll
    for (int e = 0; e < 8; ++e) qa[e] = f32x4{0.f, 0.f, 0.f, 0.f};
#define MMA_WQ(s) { _Pragma("unroll") for (int e_ = 0; e_ < 8; ++e_) qa[e_] = mfma16(tr_frag(wa[s], e_), xf[s], qa[e_]); }
#pragma unroll
    for (int s = 0; s < 4; ++s) xf[s] = __builtin_amdgcn_raw_buffer_load_b128(rx, xoff, KBLK(s) * 64, AUX_X);
    STAMP(16);
    LOAD_WQ(0); SB(); LOAD_WQ(1); SB(); LOAD_WQ(2); SB(); LOAD_WQ(3); SB();
    STAMP(17);
    BAR(); SB();
    STAMP(18);
    MMA_WQ(0); SB();
    STAMP(19);
    LOAD_ITEM(1); SB();
    STAMP(20);
    MMA_WQ(1); SB();
    LOAD_ITEM(2); SB();
    STAMP(21);
    MMA_WQ(2); SB();
    LOAD_ITEM(3); SB();
    STAMP(22);
    MMA_WQ(3); SB();
    LOAD_ITEM(4); SB();
    STAMP(23);
#pragma unroll
    for (int rho = 0; rho < 4; ++rho) {
      LAS f32x4* p = (LAS f32x4*)(LW + r * QP_STRIDE + (32 * g + 8 * rho) * 4);
      p[0] = f32x4{qa[0][rho], qa[1][rho], qa[2][rho], qa[3][rho]};
      p[1] = f32x4{qa[4][rho], qa[5][rho], qa[6][rho], qa[7][rho]};
    }
  }
  const unsigned e_lo = __builtin_amdgcn_readfirstlane(epoch0[0]), e_hi = __builtin_amdgcn_readfirstlane(epoch0[1]);
  const unsigned long long tag64 = (((unsigned long long)e_hi << 32) | e_lo) + 1ull;
  const unsigned tag_lo = (unsigned)tag64, tag_hi = (unsigned)(tag64 >> 32);
  asm volatile("s_waitcnt vmcnt(16)" ::: "memory");
  __syncthreads();
  if (w == 0) __builtin_amdgcn_raw_buffer_store_b64(v2u{tag_lo, tag_hi}, rws, r * 8, WS_FLAG + (bp * 32 + h) * 128, AUX_SC1);
  {
    const int row = tid >> 5, n4 = (tid & 31) * 4;
    f32x4 s = {0.f, 0.f, 0.f, 0.f};
#pragma unroll
    for (int ww = 0; ww < 8; ++ww) s += *(LAS f32x4*)(L + ww * L_WAVE + row * QP_STRIDE + n4 * 4);
    *(LAS v2u*)(L + L_QS + row * QS_STRIDE + n4 * 2) = v2u{cvt_pk(s[0], s[1]), cvt_pk(s[2], s[3])};
  }
  __syncthreads();
  v4u qf[2];
#pragma unroll
  for (int hf = 0; hf < 2; ++hf) qf[hf] = *(LAS v4u*)(L + L_QS + (8 * beta + (r & 7)) * QS_STRIDE + (8 * hf + 2 * g + (r >> 3)) * 16);
  unsigned hmask = (r >> 3) ? 0xFFFFFFFFu : 0u;
  asm volatile("" : "+v"(hmask));
  const unsigned pmask = hmask ^ 0x0000FFFFu;

  STAMP(1);
  v4u wr[4][8], wf[4][8];
  const int wooff = (8 * g) * (DM * 2) + r * 16;
  const int wos = (1024 * kq + 128 * w) * (DM * 2) + ct * 256;
#define LOAD_WO(ks) { _Pragma("unroll") for (int j_ = 0; j_ < 8; ++j_) \
    wr[ks][j_] = NO_WO ? v4u{0u, 0u, 0u, 0u} : __builtin_amdgcn_raw_buffer_load_b128(rwo, wooff, wos + (32 * (ks) + j_) * (DM * 2), AUX_WO); }
#define PARK_WO(ks) { _Pragma("unroll") for (int e_ = 0; e_ < 8; ++e_) *(LAS v4u*)(LW + (ks) * 8192 + e_ * 1024 + voff) = tr_frag(wr[ks], e_); }
#define KEEP_WO(ks) { _Pragma("unroll") for (int e_ = 0; e_ < 8; ++e_) { wf[ks][e_] = tr_frag(wr[ks], e_); asm volatile("" : "+v"(wf[ks][e_])); } }
#define BACK_WO(ks) { _Pragma("unroll") for (int e_ = 0; e_ < 8; ++e_) wf[ks][e_] = *(LAS v4u*)(LW + (ks) * 8192 + e_ * 1024 + voff); }

  f32x4 oacc[8];
#pragma unroll
  for (int e = 0; e < 8; ++e) oacc[e] = f32x4{0.f, 0.f, 0.f, 0.f};
  float m = -INFINITY, lsum = 0.f;
  v2u flags_v;

  static_for<0, NT>([&](auto tc) __attribute__((always_inline)) {
    constexpr int t = decltype(tc)::value;
    f32x4 sa[4];
#pragma unroll
    for (int u = 0; u < 4; ++u) sa[u] = mfma16(item[2 * t][2 * u], qf[0], f32x4{0.f, 0.f, 0.f, 0.f});
#pragma unroll
    for (int u = 0; u < 4; ++u) sa[u] = mfma16(item[2 * t][2 * u + 1], qf[1], sa[u]);
    float s[4];
#pragma unroll
    for (int u = 0; u < 4; ++u) {
      const float xx = bsel(hmask, sa[u][3], sa[u][0]), yy = bsel(hmask, sa[u][1], sa[u][2]);
      s[u] = xx + ror8(yy);
      asm volatile("" : "+v"(s[u]));
    }
    SB();
    if constexpr (t == 0) LOAD_WO(0);
    if constexpr (t == 1) LOAD_WO(1);
    if constexpr (t == NT - 3) flags_v = __builtin_amdgcn_raw_buffer_load_b64(rws, (voff << 3) & 0xF80, WS_FLAG + bp * 4096, AUX_SC1);
    if constexpr (last_issued(2 * t) > last_issued(2 * t - 1)) LOAD_ITEM(last_issued(2 * t));
    SB();
    if constexpr (t < BAR_TILES && t < NT - 1) BAR();
    if constexpr (t == NT - 1) BACK_WO(1);
    SB();
    const float mx = xg_max(fmaxf(fmaxf(s[0], s[1]), fmaxf(s[2], s[3])));
    const float mn = fmaxf(m, mx);
    const float alpha = __builtin_amdgcn_exp2f((m - mn) * CEXP);
    const float mc = mn * CEXP;
    m = mn;
    float p[4];
#pragma unroll
    for (int u = 0; u < 4; ++u) p[u] = __builtin_amdgcn_exp2f(__builtin_fmaf(s[u], CEXP, -mc));
    lsum = lsum * alpha + ((p[0] + p[1]) + (p[2] + p[3]));
    asm volatile("" : "+v"(lsum));
    const v4u pf = {cvt_pk(p[0], p[0]) & pmask, cvt_pk(p[1], p[1]) & pmask, cvt_pk(p[2], p[2]) & pmask, cvt_pk(p[3], p[3]) & pmask};
#pragma unroll
    for (int e = 0; e < 8; ++e) oacc[e] *= alpha;
#pragma unroll
    for (int e = 0; e < 8; ++e) oacc[e] = mfma16(tr_frag(item[2 * t + 1], e), pf, oacc[e]);
    SB();
    if constexpr ((t & 1) == 1) STAMP(8 + (t >> 1));
    if constexpr (t == 2) { PARK_WO(0); SB(); LOAD_WO(2); }
    if constexpr (t == 3) { PARK_WO(1); SB(); LOAD_WO(3); }
    if constexpr (t == NT - 2) BACK_WO(0);
    SB();
    if constexpr (last_issued(2 * t + 1) > last_issued(2 * t)) LOAD_ITEM(last_issued(2 * t + 1));
    SB();
  });

  STAMP(2);
  const int lane2 = __builtin_amdgcn_mbcnt_hi(~0u, __builtin_amdgcn_mbcnt_lo(~0u, 0u));
  const int r2 = lane2 & 15, g2 = lane2 >> 4;
  lsum = xg_sum(lsum);
  asm volatile("s_waitcnt lgkmcnt(0)" ::: "memory");
  SB();
#pragma unroll
  for (int rho = 0; rho < 4; ++rho) {
    LAS f32x4* p = (LAS f32x4*)(LW + r2 * QP_STRIDE + (32 * g2 + 8 * rho) * 4);
    p[0] = f32x4{oacc[0][rho], oacc[1][rho], oacc[2][rho], oacc[3][rho]};
    p[1] = f32x4{oacc[4][rho], oacc[5][rho], oacc[6][rho], oacc[7][rho]};
  }
  if (g2 == 0) *(LAS f32x2*)(L + L_ML + w * 128 + r2 * 8) = f32x2{m, lsum};
  {
    unsigned spins = 0;
    while (!__all(flags_v[0] == tag_lo && flags_v[1] == tag_hi)) {
      __builtin_amdgcn_s_sleep(2);
      if (++spins > kSpinCap) break;
      asm volatile("" ::: "memory");
      flags_v = __builtin_amdgcn_raw_buffer_load_b64(rws, (lane2 & 31) * 128, WS_FLAG + bp * 4096, AUX_SC1);
    }
    asm volatile("" ::: "memory");
  }
  const int goff = WS_O + (bp * 128 + 4 * (8 * kq + w)) * 1024;
  v4u ob[4];
#pragma unroll
  for (int ks = 0; ks < 4; ++ks) ob[ks] = __builtin_amdgcn_raw_buffer_load_b128(rws, lane2 * 16, goff + ks * 1024, AUX_SC1);
  __syncthreads();
  STAMP(3);
  {
    const int fl = 32 * (w & 1) + (lane2 >> 1), c = fl & 15;
    const int d0 = 32 * (w >> 1) + 8 * (fl >> 4) + 4 * (lane2 & 1);
    const int bt = c >> 3, q = c & 7;
    float mw[8], lw[8];
    f32x4 pv[8];
#pragma unroll
    for (int j = 0; j < 8; ++j) {
      const f32x2 ml = *(const LAS f32x2*)(L + L_ML + (4 * bt + (j >> 1)) * 128 + (q + 8 * (j & 1)) * 8);
      mw[j] = ml[0]; lw[j] = ml[1];
      pv[j] = *(const LAS f32x4*)(L + (4 * bt + (j >> 1)) * L_WAVE + (q + 8 * (j & 1)) * QP_STRIDE + d0 * 4);
    }
    const float M = fmaxf(fmaxf(fmaxf(mw[0], mw[1]), fmaxf(mw[2], mw[3])), fmaxf(fmaxf(mw[4], mw[5]), fmaxf(mw[6], mw[7])));
    float Ls = 0.f;
    f32x4 a0 = {0.f, 0.f, 0.f, 0.f};
#pragma unroll
    for (int j = 0; j < 8; ++j) {
      const float wt = __builtin_amdgcn_exp2f((mw[j] - M) * CEXP);
      Ls += wt * lw[j];
      a0 += pv[j] * wt;
    }
    const float inv = 1.0f / Ls;
    v2u ov = {cvt_pk(a0[0] * inv, a0[1] * inv), cvt_pk(a0[2] * inv, a0[3] * inv)};
#pragma unroll
    for (int j = 0; j < 2; ++j) ov[j] = (ov[j] == SENT) ? 0x7FC07FC0u : ov[j];
    __builtin_amdgcn_raw_buffer_store_b64(ov, rws, lane2 * 8, o_slot + w * 512, AUX_SC1);
    if (h == 0 && w == 0) __builtin_amdgcn_raw_buffer_store_b64(v2u{tag_lo, tag_hi}, rws, r2 * 8, WS_EPOCH + bp * 256, AUX_SC1);
  }
  asm volatile("s_waitcnt lgkmcnt(0)" ::: "memory");
  BAR();
  STAMP(4);
  {
    KEEP_WO(2); KEEP_WO(3); SB();
    unsigned spins = 0;
    while (!__all(written(ob[0]) & written(ob[1]) & written(ob[2]) & written(ob[3]))) {
      if (++spins > kSpinCap) break;
      if (spins > 1) __builtin_amdgcn_s_sleep(POLL_SLEEP);
      asm volatile("" ::: "memory");
#pragma unroll
      for (int ks = 0; ks < 4; ++ks) ob[ks] = __builtin_amdgcn_raw_buffer_load_b128(rws, lane2 * 16, goff + ks * 1024, AUX_SC1);
    }
    f32x4 c[8];
#pragma unroll
    for (int e = 0; e < 8; ++e) c[e] = f32x4{0.f, 0.f, 0.f, 0.f};
#pragma unroll
    for (int ks = 0; ks < 4; ++ks)
#pragma unroll
      for (int e = 0; e < 8; ++e) c[e] = mfma16(wf[ks][e], ob[ks], c[e]);
#pragma unroll
    for (int rho = 0; rho < 4; ++rho) {
      LAS f32x4* p = (LAS f32x4*)(LW + r2 * QP_STRIDE + (32 * g2 + 8 * rho) * 4);
      p[0] = f32x4{c[0][rho], c[1][rho], c[2][rho], c[3][rho]};
      p[1] = f32x4{c[4][rho], c[5][rho], c[6][rho], c[7][rho]};
    }
  }
  __syncthreads();
  STAMP(5);
  {
    const int row = 2 * w + (lane2 >> 5), n4 = (lane2 & 31) * 4;
    f32x4 s = {0.f, 0.f, 0.f, 0.f};
#pragma unroll
    for (int ww = 0; ww < 8; ++ww) s += *(LAS f32x4*)(L + ww * L_WAVE + row * QP_STRIDE + n4 * 4);
    v4u sv = __builtin_bit_cast(v4u, s);
#pragma unroll
    for (int j = 0; j < 4; ++j) sv[j] = (sv[j] == SENT) ? 0x7FC00000u : sv[j];
    const int p_own2 = ((lane2 >> 3) & 3) * 8192 + (lane2 >> 5) * 128 + (lane2 & 7) * 16;
    __builtin_amdgcn_raw_buffer_store_b128(sv, rws, p_own2, p_base + kq * 2048 + w * 256, AUX_SC1);
  }
  {
    const int poff = g2 * 2048 + (r2 >> 3) * 128 + (r2 & 7) * 16;
    v4u pv;
    unsigned spins = 0;
    for (;;) {
      asm volatile("" ::: "memory");
      pv = __builtin_amdgcn_raw_buffer_load_b128(rws, poff, p_base + kq * 8192 + w * 256, AUX_SC1);
      if (__all(written(pv))) break;
      if (++spins > kSpinCap) break;
      __builtin_amdgcn_s_sleep(POLL_SLEEP);
    }
    float s[4];
#pragma unroll
    for (int j = 0; j < 4; ++j) s[j] = xg_sum(__uint_as_float(pv[j]));
    if (g2 == 0) {
      const int row = 16 * bp + 2 * w + (r2 >> 3), col = 128 * ct + 32 * kq + (r2 & 7) * 4;
      *(GAS v2u*)(outp + (size_t)row * DM + col) = v2u{cvt_pk(s[0], s[1]), cvt_pk(s[2], s[3])};
    }
  }
}

}

extern "C" void kernel_launch(void* const* d_in, const int* in_sizes, int n_in, void* d_out, int out_size, void* d_ws,
                              size_t ws_size, hipStream_t stream) {
  (void)in_sizes; (void)n_in; (void)out_size; (void)ws_size;
  (void)hipFuncSetAttribute((const void*)mha_fused, hipFuncAttributeMaxDynamicSharedMemorySize, L_BYTES);
  hipLaunchKernelGGL(mha_fused, dim3(NWG), dim3(NTHR), L_BYTES, stream, (const unsigned short*)d_in[0], (const unsigned short*)d_in[1],
                     (const unsigned short*)d_in[2], (const unsigned short*)d_in[3], (const unsigned short*)d_in[4],
                     (unsigned short*)d_out, (unsigned char*)d_ws);
}
```

```cpp
#define VDEF_LO 0
#define VDEF_HI 8
#include <hip/hip_runtime.h>
#include <hip/hip_bf16.h>

#define GAS __attribute__((address_space(1)))
#define LAS __attribute__((address_space(3)))
typedef unsigned v4u __attribute__((ext_vector_type(4)));
typedef unsigned v2u __attribute__((ext_vector_type(2)));
typedef float f32x4 __attribute__((ext_vector_type(4)));
typedef __bf16 bf16x8 __attribute__((ext_vector_type(8)));
typedef __bf16 bf16x2 __attribute__((ext_vector_type(2)));
typedef float f32x2 __attribute__((ext_vector_type(2)));

#define AUX_WQ 0
#define AUX_WO 0
#define AUX_X 0
#define PRO_ORDER 0
#define WQ_ROT 1
#define KDEF_LO 0
#define KDEF_HI 0
#define BAR_TILES 16
#define NO_WO 0
#define ROT_H 4
#define ROT_BP 1
#define ROT_BETA 8
#define POLL_SLEEP 4

namespace {

constexpr int NB = 16, SQ = 8, DM = 1024, NH = 32, SK = 2048, DH = 128;
constexpr int NWG = 256, NTHR = 512;
constexpr int NT = 16;
constexpr float CEXP = 0.08838834764831845f * 1.4426950408889634f;
constexpr int AUX_NT = 2, AUX_SC1 = 16;
constexpr int RSRC_W3 = 0x00020000;
constexpr unsigned SENT = 0xFFFFFFFFu;
constexpr unsigned kSpinCap = 1u << 16;

constexpr int WS_EPOCH = 0;
constexpr int WS_FLAG = 4096;
constexpr int WS_O = 65536;
constexpr int WS_P = WS_O + 8 * 128 * 1024;
constexpr int WS_BYTES = WS_P + 8 * 8 * 16 * 2048;

constexpr int L_WAVE = 16384;
constexpr int L_QS = 8 * L_WAVE;
constexpr int QS_STRIDE = 272;
constexpr int L_ML = L_QS + 16 * QS_STRIDE;
constexpr int L_BYTES = L_ML + 8 * 128;
constexpr int QP_STRIDE = 528;

__device__ __forceinline__ f32x4 mfma16(v4u a, v4u b, f32x4 c) {
  return __builtin_amdgcn_mfma_f32_16x16x32_bf16(__builtin_bit_cast(bf16x8, a), __builtin_bit_cast(bf16x8, b), c, 0, 0, 0);
}
__device__ __forceinline__ unsigned pack_lo(unsigned a, unsigned b) { return __builtin_amdgcn_perm(b, a, 0x05040100u); }
__device__ __forceinline__ unsigned pack_hi(unsigned a, unsigned b) { return __builtin_amdgcn_perm(b, a, 0x07060302u); }
__device__ __forceinline__ unsigned cvt_pk(float lo, float hi) {
  f32x2 v = {lo, hi};
  return __builtin_bit_cast(unsigned, __builtin_convertvector(v, bf16x2));
}
__device__ __forceinline__ v4u tr_frag(const v4u (&R)[8], int e) {
  v4u f;
#pragma unroll
  for (int jj = 0; jj < 4; ++jj)
    f[jj] = (e & 1) ? pack_hi(R[2 * jj][e >> 1], R[2 * jj + 1][e >> 1]) : pack_lo(R[2 * jj][e >> 1], R[2 * jj + 1][e >> 1]);
  return f;
}
__device__ __forceinline__ float ror8(float x) {
  return __builtin_bit_cast(float, __builtin_amdgcn_mov_dpp(__builtin_bit_cast(int, x), 0x128, 0xf, 0xf, true));
}
__device__ __forceinline__ float bsel(unsigned mask, float a, float b) {
  return __uint_as_float((__float_as_uint(a) & mask) | (__float_as_uint(b) & ~mask));
}
__device__ __forceinline__ float xg_max(float x) {
  auto s = __builtin_amdgcn_permlane16_swap(__float_as_uint(x), __float_as_uint(x), false, false);
  x = fmaxf(__uint_as_float(s[0]), __uint_as_float(s[1]));
  auto t = __builtin_amdgcn_permlane32_swap(__float_as_uint(x), __float_as_uint(x), false, false);
  return fmaxf(__uint_as_float(t[0]), __uint_as_float(t[1]));
}
__device__ __forceinline__ float xg_sum(float x) {
  auto s = __builtin_amdgcn_permlane16_swap(__float_as_uint(x), __float_as_uint(x), false, false);
  x = __uint_as_float(s[0]) + __uint_as_float(s[1]);
  auto t = __builtin_amdgcn_permlane32_swap(__float_as_uint(x), __float_as_uint(x), false, false);
  return __uint_as_float(t[0]) + __uint_as_float(t[1]);
}
__device__ __forceinline__ bool written(v4u v) { return (v[0] != SENT) & (v[1] != SENT) & (v[2] != SENT) & (v[3] != SENT); }
#define SB() __builtin_amdgcn_sched_barrier(0)
#define BAR() __builtin_amdgcn_s_barrier()
#define STAMP(i)

template <int V> struct IC { static constexpr int value = V; };
template <int I, int N, class F> __device__ __forceinline__ void static_for(F&& f) {
  if constexpr (I < N) { f(IC<I>{}); static_for<I + 1, N>(f); }
}
constexpr int last_issued_raw(int c) { return c < 0 ? 4 : c == 0 ? 4 : c == 1 ? 5 : c == 2 ? 5 : c + 3; }
constexpr int last_issued(int c) { return last_issued_raw(c) > 2 * NT - 1 ? 2 * NT - 1 : last_issued_raw(c); }

__global__ void __launch_bounds__(NTHR, 2)
mha_fused(const unsigned short* __restrict__ x, const unsigned short* __restrict__ kc, const unsigned short* __restrict__ vc,
          const unsigned short* __restrict__ wq, const unsigned short* __restrict__ wo, unsigned short* __restrict__ out,
          unsigned char* ws) {
  extern __shared__ __attribute__((aligned(16))) unsigned char smem[];
  LAS unsigned char* L = (LAS unsigned char*)smem;
  const int tid = threadIdx.x, lane = tid & 63, w = __builtin_amdgcn_readfirstlane(tid >> 6);
  const int r = lane & 15, g = lane >> 4;
  const int wg = blockIdx.x, idx = wg >> 3, xg = wg & 7, i4 = idx & 3;
  const int h = 4 * xg + i4, bp = idx >> 2;
  const int kq = xg >> 1, ct = 4 * (xg & 1) + ((i4 + 2) & 3);
  const int beta = w >> 2, ws4 = w & 3;
  const int bh = (2 * bp + beta) * NH + h;
  const int rot = (ROT_H * h + ROT_BP * bp + ROT_BETA * beta) & (NT - 1);
  LAS unsigned char* LW = L + w * L_WAVE;
  STAMP(0);

  const __amdgpu_buffer_rsrc_t rk = __builtin_amdgcn_make_buffer_rsrc((void*)(kc + (size_t)bh * SK * DH), 0, SK * DH * 2, RSRC_W3);
  const __amdgpu_buffer_rsrc_t rv = __builtin_amdgcn_make_buffer_rsrc((void*)(vc + (size_t)bh * SK * DH), 0, SK * DH * 2, RSRC_W3);
  const __amdgpu_buffer_rsrc_t rws = __builtin_amdgcn_make_buffer_rsrc((void*)ws, 0, WS_BYTES, RSRC_W3);
  const __amdgpu_buffer_rsrc_t rwq = __builtin_amdgcn_make_buffer_rsrc((void*)wq, 0, DM * NH * DH * 2, RSRC_W3);
  const __amdgpu_buffer_rsrc_t rwo = __builtin_amdgcn_make_buffer_rsrc((void*)wo, 0, NH * DH * DM * 2, RSRC_W3);
  const __amdgpu_buffer_rsrc_t rx = __builtin_amdgcn_make_buffer_rsrc((void*)x, 0, NB * SQ * DM * 2, RSRC_W3);

  GAS unsigned short* outp = (GAS unsigned short*)out;
  asm volatile("" : "+s"(outp));

  const int koff = (4 * ((r >> 1) & 1) + (r >> 2)) * 256 + (2 * g + (r & 1)) * 16;
  const int voff = lane * 16;
  v4u item[2 * NT][8];
#define TILE_OFF(t) (((((t) + rot) & (NT - 1)) * 4 + ws4) * 8192)
#define LOAD_ITEM(n) { const int so_ = TILE_OFF((n) >> 1); \
    if (((n) & 1) && VDEF_LO < VDEF_HI && 2 * bp + beta >= VDEF_LO && 2 * bp + beta < VDEF_HI) { _Pragma("unroll") for (int i_ = 0; i_ < 8; ++i_) \
      item[n][i_] = __builtin_amdgcn_raw_buffer_load_b128(rv, voff, so_ + i_ * 1024, 0); } \
    else if (!((n) & 1) && KDEF_LO < KDEF_HI && 2 * bp + beta >= KDEF_LO && 2 * bp + beta < KDEF_HI) { _Pragma("unroll") for (int i_ = 0; i_ < 8; ++i_) \
      item[n][i_] = __builtin_amdgcn_raw_buffer_load_b128(rk, koff, so_ + (i_ >> 1) * 2048 + (i_ & 1) * 128, 0); } \
    else { _Pragma("unroll") for (int i_ = 0; i_ < 8; ++i_) \
    item[n][i_] = ((n) & 1) ? __builtin_amdgcn_raw_buffer_load_b128(rv, voff, so_ + i_ * 1024, AUX_NT) \
                            : __builtin_amdgcn_raw_buffer_load_b128(rk, koff, so_ + (i_ >> 1) * 2048 + (i_ & 1) * 128, AUX_NT); } }
  LOAD_ITEM(0); SB();

  const v2u epoch0 = __builtin_amdgcn_raw_buffer_load_b64(rws, 0, WS_EPOCH + bp * 256, AUX_SC1);
  const int o_slot = WS_O + (bp * 128 + 4 * h) * 1024;
  const int p_base = WS_P + (bp * 8 + ct) * (16 * 2048);
  const int p_own = ((lane >> 3) & 3) * 8192 + (lane >> 5) * 128 + (lane & 7) * 16;
  __builtin_amdgcn_raw_buffer_store_b64(v2u{SENT, SENT}, rws, lane * 8, o_slot + w * 512, AUX_SC1);
  __builtin_amdgcn_raw_buffer_store_b128(v4u{SENT, SENT, SENT, SENT}, rws, p_own, p_base + kq * 2048 + w * 256, AUX_SC1);
  asm volatile("" ::: "memory");
  SB();

  {
    v4u xf[4];
#define KBLK(s) ((((4 * beta + (s) + WQ_ROT * bp) & 7) << 2) | ws4)
    const int xoff = ((16 * bp + r) * DM + 8 * g) * 2;
    const int wqoff = (8 * g) * (NH * DH * 2) + r * 16;
    v4u wa[4][8];
#define LOAD_WQ(s) { const int so_ = KBLK(s) * 32 * (NH * DH * 2) + h * (DH * 2); _Pragma("unroll") for (int j_ = 0; j_ < 8; ++j_) \
      wa[s][j_] = __builtin_amdgcn_raw_buffer_load_b128(rwq, wqoff, so_ + j_ * (NH * DH * 2), AUX_WQ); }
    f32x4 qa[8];
#pragma unroll
    for (int e = 0; e < 8; ++e) qa[e] = f32x4{0.f, 0.f, 0.f, 0.f};
#define MMA_WQ(s) { _Pragma("unroll") for (int e_ = 0; e_ < 8; ++e_) qa[e_] = mfma16(tr_frag(wa[s], e_), xf[s], qa[e_]); }
#pragma unroll
    for (int s = 0; s < 4; ++s) xf[s] = __builtin_amdgcn_raw_buffer_load_b128(rx, xoff, KBLK(s) * 64, AUX_X);
    STAMP(16);
    LOAD_WQ(0); SB(); LOAD_WQ(1); SB(); LOAD_WQ(2); SB(); LOAD_WQ(3); SB();
    STAMP(17);
    BAR(); SB();
    STAMP(18);
    MMA_WQ(0); SB();
    STAMP(19);
    LOAD_ITEM(1); SB();
    STAMP(20);
    MMA_WQ(1); SB();
    LOAD_ITEM(2); SB();
    STAMP(21);
    MMA_WQ(2); SB();
    LOAD_ITEM(3); SB();
    STAMP(22);
    MMA_WQ(3); SB();
    LOAD_ITEM(4); SB();
    STAMP(23);
#pragma unroll
    for (int rho = 0; rho < 4; ++rho) {
      LAS f32x4* p = (LAS f32x4*)(LW + r * QP_STRIDE + (32 * g + 8 * rho) * 4);
      p[0] = f32x4{qa[0][rho], qa[1][rho], qa[2][rho], qa[3][rho]};
      p[1] = f32x4{qa[4][rho], qa[5][rho], qa[6][rho], qa[7][rho]};
    }
  }
  const unsigned e_lo = __builtin_amdgcn_readfirstlane(epoch0[0]), e_hi = __builtin_amdgcn_readfirstlane(epoch0[1]);
  const unsigned long long tag64 = (((unsigned long long)e_hi << 32) | e_lo) + 1ull;
  const unsigned tag_lo = (unsigned)tag64, tag_hi = (unsigned)(tag64 >> 32);
  asm volatile("s_waitcnt vmcnt(16)" ::: "memory");
  __syncthreads();
  if (w == 0) __builtin_amdgcn_raw_buffer_store_b64(v2u{tag_lo, tag_hi}, rws, r * 8, WS_FLAG + (bp * 32 + h) * 128, AUX_SC1);
  {
    const int row = tid >> 5, n4 = (tid & 31) * 4;
    f32x4 s = {0.f, 0.f, 0.f, 0.f};
#pragma unroll
    for (int ww = 0; ww < 8; ++ww) s += *(LAS f32x4*)(L + ww * L_WAVE + row * QP_STRIDE + n4 * 4);
    *(LAS v2u*)(L + L_QS + row * QS_STRIDE + n4 * 2) = v2u{cvt_pk(s[0], s[1]), cvt_pk(s[2], s[3])};
  }
  __syncthreads();
  v4u qf[2];
#pragma unroll
  for (int hf = 0; hf < 2; ++hf) qf[hf] = *(LAS v4u*)(L + L_QS + (8 * beta + (r & 7)) * QS_STRIDE + (8 * hf + 2 * g + (r >> 3)) * 16);
  unsigned hmask = (r >> 3) ? 0xFFFFFFFFu : 0u;
  asm volatile("" : "+v"(hmask));
  const unsigned pmask = hmask ^ 0x0000FFFFu;

  STAMP(1);
  v4u wr[4][8], wf[4][8];
  const int wooff = (8 * g) * (DM * 2) + r * 16;
  const int wos = (1024 * kq + 128 * w) * (DM * 2) + ct * 256;
#define LOAD_WO(ks) { _Pragma("unroll") for (int j_ = 0; j_ < 8; ++j_) \
    wr[ks][j_] = NO_WO ? v4u{0u, 0u, 0u, 0u} : __builtin_amdgcn_raw_buffer_load_b128(rwo, wooff, wos + (32 * (ks) + j_) * (DM * 2), AUX_WO); }
#define PARK_WO(ks) { _Pragma("unroll") for (int e_ = 0; e_ < 8; ++e_) *(LAS v4u*)(LW + (ks) * 8192 + e_ * 1024 + voff) = tr_frag(wr[ks], e_); }
#define KEEP_WO(ks) { _Pragma("unroll") for (int e_ = 0; e_ < 8; ++e_) { wf[ks][e_] = tr_frag(wr[ks], e_); asm volatile("" : "+v"(wf[ks][e_])); } }
#define BACK_WO(ks) { _Pragma("unroll") for (int e_ = 0; e_ < 8; ++e_) wf[ks][e_] = *(LAS v4u*)(LW + (ks) * 8192 + e_ * 1024 + voff); }

  f32x4 oacc[8];
#pragma unroll
  for (int e = 0; e < 8; ++e) oacc[e] = f32x4{0.f, 0.f, 0.f, 0.f};
  float m = -INFINITY, lsum = 0.f;
  v2u flags_v;

  static_for<0, NT>([&](auto tc) __attribute__((always_inline)) {
    constexpr int t = decltype(tc)::value;
    f32x4 sa[4];
#pragma unroll
    for (int u = 0; u < 4; ++u) sa[u] = mfma16(item[2 * t][2 * u], qf[0], f32x4{0.f, 0.f, 0.f, 0.f});
#pragma unroll
    for (int u = 0; u < 4; ++u) sa[u] = mfma16(item[2 * t][2 * u + 1], qf[1], sa[u]);
    float s[4];
#pragma unroll
    for (int u = 0; u < 4; ++u) {
      const float xx = bsel(hmask, sa[u][3], sa[u][0]), yy = bsel(hmask, sa[u][1], sa[u][2]);
      s[u] = xx + ror8(yy);
      asm volatile("" : "+v"(s[u]));
    }
    SB();
    if constexpr (t == 0) LOAD_WO(0);
    if constexpr (t == 1) LOAD_WO(1);
    if constexpr (t == NT - 3) flags_v = __builtin_amdgcn_raw_buffer_load_b64(rws, (voff << 3) & 0xF80, WS_FLAG + bp * 4096, AUX_SC1);
    if constexpr (last_issued(2 * t) > last_issued(2 * t - 1)) LOAD_ITEM(last_issued(2 * t));
    SB();
    if constexpr (t < BAR_TILES && t < NT - 1) BAR();
    if constexpr (t == NT - 1) BACK_WO(1);
    SB();
    const float mx = xg_max(fmaxf(fmaxf(s[0], s[1]), fmaxf(s[2], s[3])));
    const float mn = fmaxf(m, mx);
    const float alpha = __builtin_amdgcn_exp2f((m - mn) * CEXP);
    const float mc = mn * CEXP;
    m = mn;
    float p[4];
#pragma unroll
    for (int u = 0; u < 4; ++u) p[u] = __builtin_amdgcn_exp2f(__builtin_fmaf(s[u], CEXP, -mc));
    lsum = lsum * alpha + ((p[0] + p[1]) + (p[2] + p[3]));
    asm volatile("" : "+v"(lsum));
    const v4u pf = {cvt_pk(p[0], p[0]) & pmask, cvt_pk(p[1], p[1]) & pmask, cvt_pk(p[2], p[2]) & pmask, cvt_pk(p[3], p[3]) & pmask};
#pragma unroll
    for (int e = 0; e < 8; ++e) oacc[e] *= alpha;
#pragma unroll
    for (int e = 0; e < 8; ++e) oacc[e] = mfma16(tr_frag(item[2 * t + 1], e), pf, oacc[e]);
    SB();
    if constexpr ((t & 1) == 1) STAMP(8 + (t >> 1));
    if constexpr (t == 2) { PARK_WO(0); SB(); LOAD_WO(2); }
    if constexpr (t == 3) { PARK_WO(1); SB(); LOAD_WO(3); }
    if constexpr (t == NT - 2) BACK_WO(0);
    SB();
    if constexpr (last_issued(2 * t + 1) > last_issued(2 * t)) LOAD_ITEM(last_issued(2 * t + 1));
    SB();
  });

  STAMP(2);
  const int lane2 = __builtin_amdgcn_mbcnt_hi(~0u, __builtin_amdgcn_mbcnt_lo(~0u, 0u));
  const int r2 = lane2 & 15, g2 = lane2 >> 4;
  lsum = xg_sum(lsum);
  asm volatile("s_waitcnt lgkmcnt(0)" ::: "memory");
  SB();
#pragma unroll
  for (int rho = 0; rho < 4; ++rho) {
    LAS f32x4* p = (LAS f32x4*)(LW + r2 * QP_STRIDE + (32 * g2 + 8 * rho) * 4);
    p[0] = f32x4{oacc[0][rho], oacc[1][rho], oacc[2][rho], oacc[3][rho]};
    p[1] = f32x4{oacc[4][rho], oacc[5][rho], oacc[6][rho], oacc[7][rho]};
  }
  if (g2 == 0) *(LAS f32x2*)(L + L_ML + w * 128 + r2 * 8) = f32x2{m, lsum};
  {
    unsigned spins = 0;
    while (!__all(flags_v[0] == tag_lo && flags_v[1] == tag_hi)) {
      __builtin_amdgcn_s_sleep(2);
      if (++spins > kSpinCap) break;
      asm volatile("" ::: "memory");
      flags_v = __builtin_amdgcn_raw_buffer_load_b64(rws, (lane2 & 31) * 128, WS_FLAG + bp * 4096, AUX_SC1);
    }
    asm volatile("" ::: "memory");
  }
  const int goff = WS_O + (bp * 128 + 4 * (8 * kq + w)) * 1024;
  v4u ob[4];
#pragma unroll
  for (int ks = 0; ks < 4; ++ks) ob[ks] = __builtin_amdgcn_raw_buffer_load_b128(rws, lane2 * 16, goff + ks * 1024, AUX_SC1);
  __syncthreads();
  STAMP(3);
  {
    const int fl = 32 * (w & 1) + (lane2 >> 1), c = fl & 15;
    const int d0 = 32 * (w >> 1) + 8 * (fl >> 4) + 4 * (lane2 & 1);
    const int bt = c >> 3, q = c & 7;
    float mw[8], lw[8];
    f32x4 pv[8];
#pragma unroll
    for (int j = 0; j < 8; ++j) {
      const f32x2 ml = *(const LAS f32x2*)(L + L_ML + (4 * bt + (j >> 1)) * 128 + (q + 8 * (j & 1)) * 8);
      mw[j] = ml[0]; lw[j] = ml[1];
      pv[j] = *(const LAS f32x4*)(L + (4 * bt + (j >> 1)) * L_WAVE + (q + 8 * (j & 1)) * QP_STRIDE + d0 * 4);
    }
    const float M = fmaxf(fmaxf(fmaxf(mw[0], mw[1]), fmaxf(mw[2], mw[3])), fmaxf(fmaxf(mw[4], mw[5]), fmaxf(mw[6], mw[7])));
    float Ls = 0.f;
    f32x4 a0 = {0.f, 0.f, 0.f, 0.f};
#pragma unroll
    for (int j = 0; j < 8; ++j) {
      const float wt = __builtin_amdgcn_exp2f((mw[j] - M) * CEXP);
      Ls += wt * lw[j];
      a0 += pv[j] * wt;
    }
    const float inv = 1.0f / Ls;
    v2u ov = {cvt_pk(a0[0] * inv, a0[1] * inv), cvt_pk(a0[2] * inv, a0[3] * inv)};
#pragma unroll
    for (int j = 0; j < 2; ++j) ov[j] = (ov[j] == SENT) ? 0x7FC07FC0u : ov[j];
    __builtin_amdgcn_raw_buffer_store_b64(ov, rws, lane2 * 8, o_slot + w * 512, AUX_SC1);
    if (h == 0 && w == 0) __builtin_amdgcn_raw_buffer_store_b64(v2u{tag_lo, tag_hi}, rws, r2 * 8, WS_EPOCH + bp * 256, AUX_SC1);
  }
  asm volatile("s_waitcnt lgkmcnt(0)" ::: "memory");
  BAR();
  STAMP(4);
  {
    KEEP_WO(2); KEEP_WO(3); SB();
    unsigned spins = 0;
    while (!__all(written(ob[0]) & written(ob[1]) & written(ob[2]) & written(ob[3]))) {
      if (++spins > kSpinCap) break;
      if (spins > 1) __builtin_amdgcn_s_sleep(POLL_SLEEP);
      asm volatile("" ::: "memory");
#pragma unroll
      for (int ks = 0; ks < 4; ++ks) ob[ks] = __builtin_amdgcn_raw_buffer_load_b128(rws, lane2 * 16, goff + ks * 1024, AUX_SC1);
    }
    f32x4 c[8];
#pragma unroll
    for (int e = 0; e < 8; ++e) c[e] = f32x4{0.f, 0.f, 0.f, 0.f};
#pragma unroll
    for (int ks = 0; ks < 4; ++ks)
#pragma unroll
      for (int e = 0; e < 8; ++e) c[e] = mfma16(wf[ks][e], ob[ks], c[e]);
#pragma unroll
    for (int rho = 0; rho < 4; ++rho) {
      LAS f32x4* p = (LAS f32x4*)(LW + r2 * QP_STRIDE + (32 * g2 + 8 * rho) * 4);
      p[0] = f32x4{c[0][rho], c[1][rho], c[2][rho], c[3][rho]};
      p[1] = f32x4{c[4][rho], c[5][rho], c[6][rho], c[7][rho]};
    }
  }
  __syncthreads();
  STAMP(5);
  {
    const int row = 2 * w + (lane2 >> 5), n4 = (lane2 & 31) * 4;
    f32x4 s = {0.f, 0.f, 0.f, 0.f};
#pragma unroll
    for (int ww = 0; ww < 8; ++ww) s += *(LAS f32x4*)(L + ww * L_WAVE + row * QP_STRIDE + n4 * 4);
    v4u sv = __builtin_bit_cast(v4u, s);
#pragma unroll
    for (int j = 0; j < 4; ++j) sv[j] = (sv[j] == SENT) ? 0x7FC00000u : sv[j];
    const int p_own2 = ((lane2 >> 3) & 3) * 8192 + (lane2 >> 5) * 128 + (lane2 & 7) * 16;
    __builtin_amdgcn_raw_buffer_store_b128(sv, rws, p_own2, p_base + kq * 2048 + w * 256, AUX_SC1);
  }
  {
    const int poff = g2 * 2048 + (r2 >> 3) * 128 + (r2 & 7) * 16;
    v4u pv;
    unsigned spins = 0;
    for (;;) {
      asm volatile("" ::: "memory");
      pv = __builtin_amdgcn_raw_buffer_load_b128(rws, poff, p_base + kq * 8192 + w * 256, AUX_SC1);
      if (__all(written(pv))) break;
      if (++spins > kSpinCap) break;
      __builtin_amdgcn_s_sleep(POLL_SLEEP);
    }
    float s[4];
#pragma unroll
    for (int j = 0; j < 4; ++j) s[j] = xg_sum(__uint_as_float(pv[j]));
    if (g2 == 0) {
      const int row = 16 * bp + 2 * w + (r2 >> 3), col = 128 * ct + 32 * kq + (r2 & 7) * 4;
      *(GAS v2u*)(outp + (size_t)row * DM + col) = v2u{cvt_pk(s[0], s[1]), cvt_pk(s[2], s[3])};
    }
  }
}

}

extern "C" void kernel_launch(void* const* d_in, const int* in_sizes, int n_in, void* d_out, int out_size, void* d_ws,
                              size_t ws_size, hipStream_t stream) {
  (void)in_sizes; (void)n_in; (void)out_size; (void)ws_size;
  (void)hipFuncSetAttribute((const void*)mha_fused, hipFuncAttributeMaxDynamicSharedMemorySize, L_BYTES);
  hipLaunchKernelGGL(mha_fused, dim3(NWG), dim3(NTHR), L_BYTES, stream, (const unsigned short*)d_in[0], (const unsigned short*)d_in[1],
                     (const unsigned short*)d_in[2], (const unsigned short*)d_in[3], (const unsigned short*)d_in[4],
                     (unsigned short*)d_out, (unsigned char*)d_ws);
}
```

```cpp
#define TOUCH_OUT 0
#include <hip/hip_runtime.h>
#include <hip/hip_bf16.h>

#define GAS __attribute__((address_space(1)))
#define LAS __attribute__((address_space(3)))
typedef unsigned v4u __attribute__((ext_vector_type(4)));
typedef unsigned v2u __attribute__((ext_vector_type(2)));
typedef float f32x4 __attribute__((ext_vector_type(4)));
typedef __bf16 bf16x8 __attribute__((ext_vector_type(8)));
typedef __bf16 bf16x2 __attribute__((ext_vector_type(2)));
typedef float f32x2 __attribute__((ext_vector_type(2)));

#define AUX_W 0
#define AUX_OUT 0
#define DESYNC 1
#define WQ_ROT 1
#define PACE 1
#define PACE_C 445
#define PACE_D 0
#define PACE_A 16
#define PACE_S0 10
#define PACE_B1 20
#define PACE_MIX 1
#define PACE_END 14
#define PACE_MAX 500
#define PACE_SP 3
#define PACE_P0 1
#define ROT_H 4
#define ROT_BP 1
#define ROT_BETA 8
#define POLL_SLEEP 2
#define DEPTH 2

namespace {

constexpr int NB = 16, SQ = 8, DM = 1024, NH = 32, SK = 2048, DH = 128;
constexpr int NWG = 256, NTHR = 512;
constexpr int NT = 16;
constexpr float CEXP = 0.08838834764831845f * 1.4426950408889634f;
constexpr int AUX_NT = 2, AUX_SC1 = 16;
constexpr int RSRC_W3 = 0x00020000;
constexpr unsigned SENT = 0xFFFFFFFFu;
constexpr unsigned kSpinCap = 1u << 16;

constexpr int WS_EPOCH = 0;
constexpr int WS_FLAG = 4096;
constexpr int WS_PACE = WS_FLAG + 32768;
constexpr int WS_O = 131072;
constexpr int WS_P = WS_O + 8 * 128 * 1024;
constexpr int WS_BYTES = WS_P + 8 * 8 * 16 * 2048;

constexpr int L_WAVE = 16384;
constexpr int L_QS = 8 * L_WAVE;
constexpr int QS_STRIDE = 272;
constexpr int L_ML = L_QS + 16 * QS_STRIDE;
constexpr int L_BYTES = L_ML + 1024;
constexpr int QP_STRIDE = 528;
constexpr int L_X = 9216, X_ODD = 2064;
constexpr int L_C = 4608;

__device__ __forceinline__ f32x4 mfma16(v4u a, v4u b, f32x4 c) {
  return __builtin_amdgcn_mfma_f32_16x16x32_bf16(__builtin_bit_cast(bf16x8, a), __builtin_bit_cast(bf16x8, b), c, 0, 0, 0);
}
__device__ __forceinline__ unsigned pack_lo(unsigned a, unsigned b) { return __builtin_amdgcn_perm(b, a, 0x05040100u); }
__device__ __forceinline__ unsigned pack_hi(unsigned a, unsigned b) { return __builtin_amdgcn_perm(b, a, 0x07060302u); }
__device__ __forceinline__ unsigned cvt_pk(float lo, float hi) {
  f32x2 v = {lo, hi};
  return __builtin_bit_cast(unsigned, __builtin_convertvector(v, bf16x2));
}
__device__ __forceinline__ v4u tr_frag(const v4u (&R)[8], int e) {
  v4u f;
#pragma unroll
  for (int jj = 0; jj < 4; ++jj)
    f[jj] = (e & 1) ? pack_hi(R[2 * jj][e >> 1], R[2 * jj + 1][e >> 1]) : pack_lo(R[2 * jj][e >> 1], R[2 * jj + 1][e >> 1]);
  return f;
}
template <int CTRL> __device__ __forceinline__ int dppi(int x) { return __builtin_amdgcn_mov_dpp(x, CTRL, 0xf, 0xf, true); }
__device__ __forceinline__ float ror8(float x) {
  return __builtin_bit_cast(float, dppi<0x128>(__builtin_bit_cast(int, x)));
}
__device__ __forceinline__ float bsel(unsigned mask, float a, float b) {
  return __uint_as_float((__float_as_uint(a) & mask) | (__float_as_uint(b) & ~mask));
}
__device__ __forceinline__ float xg_max(float x) {
  auto s = __builtin_amdgcn_permlane16_swap(__float_as_uint(x), __float_as_uint(x), false, false);
  x = fmaxf(__uint_as_float(s[0]), __uint_as_float(s[1]));
  auto t = __builtin_amdgcn_permlane32_swap(__float_as_uint(x), __float_as_uint(x), false, false);
  return fmaxf(__uint_as_float(t[0]), __uint_as_float(t[1]));
}
__device__ __forceinline__ float xg_sum(float x) {
  auto s = __builtin_amdgcn_permlane16_swap(__float_as_uint(x), __float_as_uint(x), false, false);
  x = __uint_as_float(s[0]) + __uint_as_float(s[1]);
  auto t = __builtin_amdgcn_permlane32_swap(__float_as_uint(x), __float_as_uint(x), false, false);
  return __uint_as_float(t[0]) + __uint_as_float(t[1]);
}
__device__ __forceinline__ bool written(v4u v) { return (v[0] != SENT) & (v[1] != SENT) & (v[2] != SENT) & (v[3] != SENT); }
#define SB() __builtin_amdgcn_sched_barrier(0)
#define BAR() __builtin_amdgcn_s_barrier()
#define MEMFENCE() asm volatile("" ::: "memory")
#define LDS_BAR() { asm volatile("s_waitcnt lgkmcnt(0)" ::: "memory"); BAR(); MEMFENCE(); }
#define STAMP(i)

template <int V> struct IC { static constexpr int value = V; };
template <int I, int N, class F> __device__ __forceinline__ void static_for(F&& f) {
  if constexpr (I < N) { f(IC<I>{}); static_for<I + 1, N>(f); }
}

__global__ void __launch_bounds__(NTHR, 2)
mha_fused(const unsigned short* __restrict__ x, const unsigned short* __restrict__ kc, const unsigned short* __restrict__ vc,
          const unsigned short* __restrict__ wq, const unsigned short* __restrict__ wo, unsigned short* out, unsigned char* ws) {
  extern __shared__ __attribute__((aligned(16))) unsigned char smem[];
  LAS unsigned char* L = (LAS unsigned char*)smem;
  const int tid = threadIdx.x, lane = tid & 63, w = __builtin_amdgcn_readfirstlane(tid >> 6);
  const int r = lane & 15, g = lane >> 4;
  const int wg = blockIdx.x, idx = wg >> 3, xg = wg & 7, i4 = idx & 3;
  const int h = 4 * xg + i4, bp = idx >> 2;
  const int kq = xg >> 1, ct = 4 * (xg & 1) + ((i4 + 2) & 3);
  const int beta = w >> 2, ws4 = w & 3;
  const int bh = (2 * bp + beta) * NH + h;
  const int rot = (ROT_H * h + ROT_BP * bp + ROT_BETA * beta) & (NT - 1);
  LAS unsigned char* LW = L + w * L_WAVE;
  STAMP(0);

  asm volatile("" : "+s"(out));
  const __amdgpu_buffer_rsrc_t rk = __builtin_amdgcn_make_buffer_rsrc((void*)(kc + (size_t)bh * SK * DH), 0, SK * DH * 2, RSRC_W3);
  const __amdgpu_buffer_rsrc_t rv = __builtin_amdgcn_make_buffer_rsrc((void*)(vc + (size_t)bh * SK * DH), 0, SK * DH * 2, RSRC_W3);
  const __amdgpu_buffer_rsrc_t rws = __builtin_amdgcn_make_buffer_rsrc((void*)ws, 0, WS_BYTES, RSRC_W3);
  const __amdgpu_buffer_rsrc_t rw0 = __builtin_amdgcn_make_buffer_rsrc((void*)ws, 0, w == 0 ? WS_BYTES : 0, RSRC_W3);
  const __amdgpu_buffer_rsrc_t rwq = __builtin_amdgcn_make_buffer_rsrc((void*)wq, 0, DM * NH * DH * 2, RSRC_W3);
  const __amdgpu_buffer_rsrc_t rwo = __builtin_amdgcn_make_buffer_rsrc((void*)wo, 0, NH * DH * DM * 2, RSRC_W3);
  const __amdgpu_buffer_rsrc_t rx = __builtin_amdgcn_make_buffer_rsrc((void*)x, 0, NB * SQ * DM * 2, RSRC_W3);
  const __amdgpu_buffer_rsrc_t rout = __builtin_amdgcn_make_buffer_rsrc((void*)out, 0, NB * SQ * DM * 2, RSRC_W3);

#define ITEM_AT(pos) ((pos) == 2 * NT - 3 ? 2 * NT - 2 : (pos) == 2 * NT - 2 ? 2 * NT - 3 : (pos))
#define NEXT_AFTER(pos) ((pos) + DEPTH < 3 || (pos) + DEPTH >= 2 * NT ? -1 : ITEM_AT((pos) + DEPTH))
#define NEXT_K(t) NEXT_AFTER(2 * (t))
#define NEXT_V(t) NEXT_AFTER(2 * (t) + 1)
  const int koff = (4 * ((r >> 1) & 1) + (r >> 2)) * 256 + (2 * g + (r & 1)) * 16;
  const int voff = lane * 16;
  v4u item[2 * NT][8];
#define TILE_OFF(t) (((((t) + rot) & (NT - 1)) * 4 + ws4) * 8192)
#define LOAD_ITEM(n) { const int so_ = TILE_OFF((n) >> 1); _Pragma("unroll") for (int i_ = 0; i_ < 8; ++i_) \
    item[n][i_] = ((n) & 1) ? __builtin_amdgcn_raw_buffer_load_b128(rv, voff, so_ + i_ * 1024, AUX_NT) \
                            : __builtin_amdgcn_raw_buffer_load_b128(rk, koff, so_ + (i_ >> 1) * 2048 + (i_ & 1) * 128, AUX_NT); }

  const int o_slot = WS_O + (bp * 128 + 4 * h) * 1024;
  const int p_base = WS_P + (bp * 8 + ct) * (16 * 2048);
  const int out_off = ((16 * bp + 2 * w) * DM + 128 * ct + 32 * kq) * 2;

  unsigned e_lo, e_hi;
  {
#define KBLK(s) ((((4 * beta + (s) + WQ_ROT * (bp & 6)) & 7) << 2) | ws4)
    const int wqoff = (8 * g) * (NH * DH * 2) + r * 16;
    v4u wa[4][8], xr[4];
    v2u epoch0;
#define LOAD_WQ(s) { const int so_ = KBLK(s) * 32 * (NH * DH * 2) + h * (DH * 2); _Pragma("unroll") for (int j_ = 0; j_ < 8; ++j_) \
      wa[s][j_] = __builtin_amdgcn_raw_buffer_load_b128(rwq, wqoff, so_ + j_ * (NH * DH * 2), AUX_W); }
#define LOAD_X() { MEMFENCE(); epoch0 = __builtin_amdgcn_raw_buffer_load_b64(rws, 0, WS_EPOCH + bp * 256, AUX_SC1); \
      _Pragma("unroll") for (int i_ = 0; i_ < 4; ++i_) \
      xr[i_] = __builtin_amdgcn_raw_buffer_load_b128(rx, voff, (16 * bp + 2 * w) * (DM * 2) + i_ * 1024, AUX_W); }
#define STAGE_X() { _Pragma("unroll") for (int i_ = 0; i_ < 4; ++i_) \
      *(LAS v4u*)(LW + L_X + w * 32 + (i_ >> 1) * X_ODD + (i_ & 1) * 1024 + voff) = xr[i_]; \
      e_lo = __builtin_amdgcn_readfirstlane(epoch0[0]); e_hi = __builtin_amdgcn_readfirstlane(epoch0[1]); \
      asm volatile("s_waitcnt lgkmcnt(0)" : "+s"(e_lo), "+s"(e_hi) :: "memory"); }
    const LAS unsigned char* xrow = L + (r >> 1) * (L_WAVE + 32) + L_X + (r & 1) * X_ODD + g * 16;
    f32x4 qa[8];
#pragma unroll
    for (int e = 0; e < 8; ++e) qa[e] = f32x4{0.f, 0.f, 0.f, 0.f};
#define MMA_WQ(s) { const v4u xf_ = *(const LAS v4u*)(xrow + KBLK(s) * 64); \
      _Pragma("unroll") for (int e_ = 0; e_ < 8; ++e_) qa[e_] = mfma16(tr_frag(wa[s], e_), xf_, qa[e_]); }
#define PUT_Q() { _Pragma("unroll") for (int rho = 0; rho < 4; ++rho) { \
      LAS f32x4* p_ = (LAS f32x4*)(LW + r * QP_STRIDE + (32 * g + 8 * rho) * 4); \
      p_[0] = f32x4{qa[0][rho], qa[1][rho], qa[2][rho], qa[3][rho]}; \
      p_[1] = f32x4{qa[4][rho], qa[5][rho], qa[6][rho], qa[7][rho]}; } }
#define REDUCE_Q() { const int row_ = tid >> 5, n4_ = (tid & 31) * 4; f32x4 pa_[8]; \
      _Pragma("unroll") for (int ww = 0; ww < 8; ++ww) pa_[ww] = *(LAS f32x4*)(L + ww * L_WAVE + row_ * QP_STRIDE + n4_ * 4); \
      const f32x4 s_ = ((pa_[0] + pa_[1]) + (pa_[2] + pa_[3])) + ((pa_[4] + pa_[5]) + (pa_[6] + pa_[7])); \
      *(LAS v2u*)(L + L_QS + row_ * QS_STRIDE + n4_ * 2) = v2u{cvt_pk(s_[0], s_[1]), cvt_pk(s_[2], s_[3])}; }
    if (!DESYNC || (bp & 1) == 0) {
      LOAD_X(); SB(); LOAD_WQ(0); SB(); LOAD_WQ(1); SB(); LOAD_WQ(2); SB(); LOAD_WQ(3); SB();
      STAMP(16);
      STAGE_X(); SB();
      BAR(); MEMFENCE(); SB();
      STAMP(17);
      LOAD_ITEM(0); SB();
      STAMP(18);
      MMA_WQ(0); MMA_WQ(1); MMA_WQ(2); MMA_WQ(3);
      PUT_Q(); SB();
      STAMP(19);
      LOAD_ITEM(1); SB();
      STAMP(20);
      LDS_BAR(); SB();
      STAMP(21);
      REDUCE_Q(); SB();
      LOAD_ITEM(2); SB();
      STAMP(22);
    } else {
      LOAD_ITEM(0); SB(); LOAD_ITEM(1); SB();
      STAMP(16);
      BAR(); SB();
      STAMP(17);
      LOAD_X(); SB(); LOAD_WQ(0); SB(); LOAD_WQ(1); SB(); LOAD_WQ(2); SB();
      STAGE_X(); SB();
      BAR(); MEMFENCE(); SB();
      STAMP(18);
      MMA_WQ(0); SB();
      LOAD_WQ(3); SB();
      STAMP(19);
      MMA_WQ(1); SB();
      LOAD_ITEM(2); SB();
      MMA_WQ(2); MMA_WQ(3);
      PUT_Q(); SB();
      STAMP(20);
      LDS_BAR(); SB();
      STAMP(21);
      REDUCE_Q(); SB();
      STAMP(22);
    }
  }
  LDS_BAR();
  v4u qf[2];
#pragma unroll
  for (int hf = 0; hf < 2; ++hf) qf[hf] = *(LAS v4u*)(L + L_QS + (8 * beta + (r & 7)) * QS_STRIDE + (8 * hf + 2 * g + (r >> 3)) * 16);
  unsigned hmask = (r >> 3) ? 0xFFFFFFFFu : 0u;
  asm volatile("" : "+v"(hmask));
  const unsigned pmask = hmask ^ 0x0000FFFFu;

  STAMP(1);
  v4u wr[4][8], wf[4][8];
  const int wooff = (8 * g) * (DM * 2) + r * 16;
  const int wos = (1024 * kq + 128 * w) * (DM * 2) + ct * 256;
#define LOAD_WO(ks) { _Pragma("unroll") for (int j_ = 0; j_ < 8; ++j_) \
    wr[ks][j_] = __builtin_amdgcn_raw_buffer_load_b128(rwo, wooff, wos + (32 * (ks) + j_) * (DM * 2), AUX_W); }
#define PARK_WO(ks) { _Pragma("unroll") for (int e_ = 0; e_ < 8; ++e_) *(LAS v4u*)(LW + (ks) * 8192 + e_ * 1024 + voff) = tr_frag(wr[ks], e_); }
#define KEEP_WO(ks) { _Pragma("unroll") for (int e_ = 0; e_ < 8; ++e_) { wf[ks][e_] = tr_frag(wr[ks], e_); asm volatile("" : "+v"(wf[ks][e_])); } }
#define BACK_WO(ks) { _Pragma("unroll") for (int e_ = 0; e_ < 8; ++e_) wf[ks][e_] = *(LAS v4u*)(LW + (ks) * 8192 + e_ * 1024 + voff); }

  f32x4 oacc[8];
#pragma unroll
  for (int e = 0; e < 8; ++e) oacc[e] = f32x4{0.f, 0.f, 0.f, 0.f};
  float m = -INFINITY, lsum = 0.f;
  const unsigned long long tag64 = (((unsigned long long)e_hi << 32) | e_lo) + 1ull;
  const unsigned tag_lo = (unsigned)tag64, tag_hi = (unsigned)(tag64 >> 32);
  v2u flags_v, snap;
  unsigned touch;
  const unsigned tbase = PACE ? (unsigned)__builtin_amdgcn_s_memrealtime() : 0u;
  int tau_ref = -1000000, tau_slope = 0;

  static_for<0, NT - 2>([&](auto tc) __attribute__((always_inline)) {
    constexpr int t = decltype(tc)::value;
    f32x4 sa[4];
#pragma unroll
    for (int u = 0; u < 4; ++u) sa[u] = mfma16(item[2 * t][2 * u], qf[0], f32x4{0.f, 0.f, 0.f, 0.f});
#pragma unroll
    for (int u = 0; u < 4; ++u) sa[u] = mfma16(item[2 * t][2 * u + 1], qf[1], sa[u]);
    float s[4];
#pragma unroll
    for (int u = 0; u < 4; ++u) {
      const float xx = bsel(hmask, sa[u][3], sa[u][0]), yy = bsel(hmask, sa[u][1], sa[u][2]);
      s[u] = xx + ror8(yy);
    }
    asm volatile("" : "+v"(s[0]), "+v"(s[1]), "+v"(s[2]), "+v"(s[3]));
    SB();
    if constexpr (t == 0) {
      const int p_own = ((voff >> 7) & 3) * 8192 + (voff >> 9) * 128 + (voff & 0x70);
      __builtin_amdgcn_raw_buffer_store_b64(v2u{SENT, SENT}, rws, voff >> 1, o_slot + w * 512, AUX_SC1);
      __builtin_amdgcn_raw_buffer_store_b128(v4u{SENT, SENT, SENT, SENT}, rws, p_own, p_base + kq * 2048 + w * 256, AUX_SC1);
      MEMFENCE();
      SB();
      LOAD_WO(0);
    }
    if constexpr (t == 1) LOAD_WO(1);
    if constexpr (t == NT - 5) {
      flags_v = __builtin_amdgcn_raw_buffer_load_b64(rw0, (voff << 3) & 0xF80, WS_FLAG + bp * 4096, AUX_SC1);
      if (TOUCH_OUT) touch = __builtin_amdgcn_raw_buffer_load_b32(rout, 0, out_off, AUX_NT);
    }
    if constexpr (PACE && t >= PACE_P0 + 2 && (t - 2) % PACE_SP == PACE_P0 % PACE_SP && t < PACE_END) {
      int v = (int)(snap[0] - tbase);
      const bool ok = snap[1] == ~snap[0] && v > -3000 && v < 3000;
      v += dppi<0xB1>(v); v += dppi<0x4E>(v); v += dppi<0x141>(v);
      const bool all_ok = (__builtin_amdgcn_ballot_w64(ok) & 0xFFull) == 0xFFull;
      const int mean = __builtin_amdgcn_readfirstlane(v) >> 3;
      if constexpr (PACE_A && t >= PACE_P0 + 2 + PACE_SP) {
        const int drift = (mean - tau_ref) * PACE_A / (16 * PACE_SP) - PACE_S0 - (t - 2 - PACE_SP < 3 ? PACE_B1 : 0);
        tau_slope = (all_ok && tau_ref > -1000000) ? min(max(drift, 0), 60) : 0;
      }
      tau_ref = all_ok ? mean : -1000000;
    }
    if constexpr (PACE && t >= PACE_P0 + 1 && (t - 1) % PACE_SP == PACE_P0 % PACE_SP && t < PACE_END - 1) {
      const int px = (voff >> 4) & 7;
      const int peer = PACE_MIX ? ((((bp + px) & 7) * 4 + ((i4 + px) & 3)) * 8 + ((xg + px) & 7)) : idx * 8 + px;
      snap = __builtin_amdgcn_raw_buffer_load_b64(rw0, peer * 128, WS_PACE, AUX_SC1);
      SB();
    }
    if constexpr (NEXT_K(t) >= 0) LOAD_ITEM(NEXT_K(t));
    SB();
    if constexpr (PACE && t >= PACE_P0 + 2 && t < PACE_END) {
      constexpr int age = 2 + (t - 2 - PACE_P0) % PACE_SP;
      int now = (int)((unsigned)__builtin_amdgcn_s_memrealtime() - tbase);
      const int allow = min(tau_ref - PACE_D + t * PACE_C + age * tau_slope, now + PACE_MAX);
      while (now < allow) { __builtin_amdgcn_s_sleep(2); now = (int)((unsigned)__builtin_amdgcn_s_memrealtime() - tbase); }
    }
    if constexpr (t == 2) {
      if (DEPTH == 3) asm volatile("s_waitcnt vmcnt(42)" ::: "memory"); else asm volatile("s_waitcnt vmcnt(26)" ::: "memory");
      BAR();
      MEMFENCE();
      __builtin_amdgcn_raw_buffer_store_b64(v2u{tag_lo, tag_hi}, rw0, (voff >> 1) & 0x78, WS_FLAG + (bp * 32 + h) * 128, AUX_SC1);
    } else if constexpr (t == NT - 3) {
      if (w == 0) {
        if (__builtin_expect(!__all(flags_v[0] == tag_lo && flags_v[1] == tag_hi), 0)) {
          for (unsigned spins = 0; spins < kSpinCap; ++spins) {
            __builtin_amdgcn_s_sleep(2);
            MEMFENCE();
            const v2u fv = __builtin_amdgcn_raw_buffer_load_b64(rws, (voff << 3) & 0xF80, WS_FLAG + bp * 4096, AUX_SC1);
            if (__all(fv[0] == tag_lo && fv[1] == tag_hi)) break;
          }
        }
        if (h == 0) __builtin_amdgcn_raw_buffer_store_b64(v2u{tag_lo, tag_hi}, rws, (voff >> 1) & 0x78, WS_EPOCH + bp * 256, AUX_SC1);
      }
      if (TOUCH_OUT) asm volatile("" :: "v"(touch));
      MEMFENCE();
      BAR();
      MEMFENCE();
    } else BAR();
    if constexpr (PACE && t >= PACE_P0 && t % PACE_SP == PACE_P0 % PACE_SP && t < PACE_END - 2) {
      const unsigned tv = (unsigned)__builtin_amdgcn_s_memrealtime() - (unsigned)(t * PACE_C);
      __builtin_amdgcn_raw_buffer_store_b64(v2u{tv, ~tv}, rw0, (voff >> 1) & 0x78, WS_PACE + wg * 128, AUX_SC1);
    }
    SB();
    const float mx = xg_max(fmaxf(fmaxf(s[0], s[1]), fmaxf(s[2], s[3])));
    const float mn = fmaxf(m, mx);
    const float alpha = __builtin_amdgcn_exp2f((m - mn) * CEXP);
    const float mc = mn * CEXP;
    m = mn;
    float p[4];
#pragma unroll
    for (int u = 0; u < 4; ++u) p[u] = __builtin_amdgcn_exp2f(__builtin_fmaf(s[u], CEXP, -mc));
    lsum = lsum * alpha + ((p[0] + p[1]) + (p[2] + p[3]));
    asm volatile("" : "+v"(lsum));
    const v4u pf = {cvt_pk(p[0], p[0]) & pmask, cvt_pk(p[1], p[1]) & pmask, cvt_pk(p[2], p[2]) & pmask, cvt_pk(p[3], p[3]) & pmask};
#pragma unroll
    for (int e = 0; e < 8; ++e) oacc[e] *= alpha;
#pragma unroll
    for (int e = 0; e < 8; ++e) oacc[e] = mfma16(tr_frag(item[2 * t + 1], e), pf, oacc[e]);
    SB();
    if constexpr ((t & 1) == 1) STAMP(8 + (t >> 1));
    if constexpr (t == 1) { PARK_WO(0); SB(); LOAD_WO(2); }
    if constexpr (t == 2) { PARK_WO(1); SB(); LOAD_WO(3); }
    SB();
    if constexpr (NEXT_V(t) >= 0) LOAD_ITEM(NEXT_V(t));
    SB();
  });
  {
    float s[8];
    static_for<0, 2>([&](auto hc) __attribute__((always_inline)) {
      constexpr int hh = decltype(hc)::value, n = 2 * (NT - 2 + hh);
      f32x4 sa[4];
#pragma unroll
      for (int u = 0; u < 4; ++u) sa[u] = mfma16(item[n][2 * u], qf[0], f32x4{0.f, 0.f, 0.f, 0.f});
#pragma unroll
      for (int u = 0; u < 4; ++u) sa[u] = mfma16(item[n][2 * u + 1], qf[1], sa[u]);
#pragma unroll
      for (int u = 0; u < 4; ++u) s[4 * hh + u] = bsel(hmask, sa[u][3], sa[u][0]) + ror8(bsel(hmask, sa[u][1], sa[u][2]));
      asm volatile("" : "+v"(s[4 * hh]), "+v"(s[4 * hh + 1]), "+v"(s[4 * hh + 2]), "+v"(s[4 * hh + 3]));
      SB();
      if constexpr (hh == 0) { LOAD_ITEM(NEXT_AFTER(2 * NT - 4)); SB(); }
    });
    BACK_WO(0);
    const float mx = xg_max(fmaxf(fmaxf(fmaxf(s[0], s[1]), fmaxf(s[2], s[3])), fmaxf(fmaxf(s[4], s[5]), fmaxf(s[6], s[7]))));
    const float mn = fmaxf(m, mx);
    float alpha = __builtin_amdgcn_exp2f((m - mn) * CEXP);
    const float mc = mn * CEXP;
    m = mn;
    float p[8];
#pragma unroll
    for (int u = 0; u < 8; ++u) p[u] = __builtin_amdgcn_exp2f(__builtin_fmaf(s[u], CEXP, -mc));
    lsum = xg_sum(lsum * alpha + (((p[0] + p[1]) + (p[2] + p[3])) + ((p[4] + p[5]) + (p[6] + p[7]))));
    LAS unsigned char* ml = L + L_ML + beta * 512 + ((voff >> 4) & 7) * 64;
    if (voff < 256) *(LAS f32x2*)(ml + ws4 * 16 + (voff >> 7) * 8) = f32x2{m, lsum};
    SB();
    if constexpr (DEPTH == 2) { LOAD_ITEM(2 * NT - 1); SB(); }
    LDS_BAR();
    f32x4 rec[4];
#pragma unroll
    for (int j = 0; j < 4; ++j) rec[j] = *(LAS f32x4*)(ml + j * 16);
    const float M = fmaxf(fmaxf(fmaxf(rec[0][0], rec[0][2]), fmaxf(rec[1][0], rec[1][2])), fmaxf(fmaxf(rec[2][0], rec[2][2]), fmaxf(rec[3][0], rec[3][2])));
    float Ls = 0.f;
#pragma unroll
    for (int j = 0; j < 4; ++j)
      Ls += __builtin_amdgcn_exp2f((rec[j][0] - M) * CEXP) * rec[j][1] + __builtin_amdgcn_exp2f((rec[j][2] - M) * CEXP) * rec[j][3];
    const float scale = __builtin_amdgcn_exp2f((m - M) * CEXP) / Ls;
    alpha *= scale;
    v4u pf[2];
#pragma unroll
    for (int u = 0; u < 8; ++u) { const float ps = p[u] * scale; pf[u >> 2][u & 3] = cvt_pk(ps, ps) & pmask; }
#pragma unroll
    for (int e = 0; e < 8; ++e) oacc[e] *= alpha;
#pragma unroll
    for (int e = 0; e < 8; ++e) oacc[e] = mfma16(tr_frag(item[2 * NT - 3], e), pf[0], oacc[e]);
    SB();
    BACK_WO(1);
    SB();
#pragma unroll
    for (int e = 0; e < 8; ++e) oacc[e] = mfma16(tr_frag(item[2 * NT - 1], e), pf[1], oacc[e]);
    SB();
    STAMP(15);
  }

  STAMP(2);
  const int lane2 = __builtin_amdgcn_mbcnt_hi(~0u, __builtin_amdgcn_mbcnt_lo(~0u, 0u));
  const int r2 = lane2 & 15, g2 = lane2 >> 4;
  const int goff = WS_O + (bp * 128 + 4 * (8 * kq + w)) * 1024;
  v4u ob[4];
#pragma unroll
  for (int ks = 0; ks < 4; ++ks) ob[ks] = __builtin_amdgcn_raw_buffer_load_b128(rws, lane2 * 16, goff + ks * 1024, AUX_SC1);
  asm volatile("s_waitcnt lgkmcnt(0)" ::: "memory");
#pragma unroll
  for (int rho = 0; rho < 4; ++rho) {
    f32x4 v;
#pragma unroll
    for (int k = 0; k < 4; ++k) v[k] = bsel(hmask, oacc[4 + k][rho], oacc[k][rho]) + ror8(bsel(hmask, oacc[k][rho], oacc[4 + k][rho]));
    *(LAS f32x4*)(LW + (r2 & 7) * QP_STRIDE + (32 * g2 + 8 * rho + 4 * (r2 >> 3)) * 4) = v;
  }
  LDS_BAR();
  STAMP(3);
  {
    const int fl = 32 * (w & 1) + (lane2 >> 1), c = fl & 15;
    const int d0 = 32 * (w >> 1) + 8 * (fl >> 4) + 4 * (lane2 & 1);
    const LAS unsigned char* src = L + (c >> 3) * (4 * L_WAVE) + (c & 7) * QP_STRIDE + d0 * 4;
    const f32x4 a0 = (*(const LAS f32x4*)(src) + *(const LAS f32x4*)(src + L_WAVE)) +
                     (*(const LAS f32x4*)(src + 2 * L_WAVE) + *(const LAS f32x4*)(src + 3 * L_WAVE));
    v2u ov = {cvt_pk(a0[0], a0[1]), cvt_pk(a0[2], a0[3])};
#pragma unroll
    for (int j = 0; j < 2; ++j) ov[j] = (ov[j] == SENT) ? 0x7FC07FC0u : ov[j];
    __builtin_amdgcn_raw_buffer_store_b64(ov, rws, lane2 * 8, o_slot + w * 512, AUX_SC1);
  }
  STAMP(4);
  {
    SB(); KEEP_WO(2); KEEP_WO(3); SB();
    unsigned spins = 0;
    while (!__all(written(ob[0]) & written(ob[1]) & written(ob[2]) & written(ob[3]))) {
      if (++spins > kSpinCap) break;
      if (spins > 1) __builtin_amdgcn_s_sleep(POLL_SLEEP);
      MEMFENCE();
#pragma unroll
      for (int ks = 0; ks < 4; ++ks) ob[ks] = __builtin_amdgcn_raw_buffer_load_b128(rws, lane2 * 16, goff + ks * 1024, AUX_SC1);
    }
    f32x4 c[8];
#pragma unroll
    for (int e = 0; e < 8; ++e) c[e] = f32x4{0.f, 0.f, 0.f, 0.f};
#pragma unroll
    for (int ks = 0; ks < 4; ++ks)
#pragma unroll
      for (int e = 0; e < 8; ++e) c[e] = mfma16(wf[ks][e], ob[ks], c[e]);
#pragma unroll
    for (int rho = 0; rho < 4; ++rho) {
      LAS f32x4* p = (LAS f32x4*)(LW + L_C + r2 * QP_STRIDE + (32 * g2 + 8 * rho) * 4);
      p[0] = f32x4{c[0][rho], c[1][rho], c[2][rho], c[3][rho]};
      p[1] = f32x4{c[4][rho], c[5][rho], c[6][rho], c[7][rho]};
    }
  }
  LDS_BAR();
  STAMP(5);
  {
    const int row = 2 * w + (lane2 >> 5), n4 = (lane2 & 31) * 4;
    f32x4 pa[8];
#pragma unroll
    for (int ww = 0; ww < 8; ++ww) pa[ww] = *(LAS f32x4*)(L + ww * L_WAVE + L_C + row * QP_STRIDE + n4 * 4);
    const f32x4 s = ((pa[0] + pa[1]) + (pa[2] + pa[3])) + ((pa[4] + pa[5]) + (pa[6] + pa[7]));
    v4u sv = __builtin_bit_cast(v4u, s);
#pragma unroll
    for (int j = 0; j < 4; ++j) sv[j] = (sv[j] == SENT) ? 0x7FC00000u : sv[j];
    const int p_own2 = ((lane2 >> 3) & 3) * 8192 + (lane2 >> 5) * 128 + (lane2 & 7) * 16;
    __builtin_amdgcn_raw_buffer_store_b128(sv, rws, p_own2, p_base + kq * 2048 + w * 256, AUX_SC1);
  }
  {
    const int poff = g2 * 2048 + (r2 >> 3) * 128 + (r2 & 7) * 16;
    v4u pv;
    unsigned spins = 0;
    for (;;) {
      MEMFENCE();
      pv = __builtin_amdgcn_raw_buffer_load_b128(rws, poff, p_base + kq * 8192 + w * 256, AUX_SC1);
      if (__all(written(pv))) break;
      if (++spins > kSpinCap) break;
      __builtin_amdgcn_s_sleep(POLL_SLEEP);
    }
    float s[4];
#pragma unroll
    for (int j = 0; j < 4; ++j) s[j] = xg_sum(__uint_as_float(pv[j]));
    if (g2 == 0)
      __builtin_amdgcn_raw_buffer_store_b64(v2u{cvt_pk(s[0], s[1]), cvt_pk(s[2], s[3])}, rout, (r2 >> 3) * (DM * 2) + (r2 & 7) * 8, out_off, AUX_OUT);
  }
}

}

extern "C" void kernel_launch(void* const* d_in, const int* in_sizes, int n_in, void* d_out, int out_size, void* d_ws,
                              size_t ws_size, hipStream_t stream) {
  (void)in_sizes; (void)n_in; (void)out_size; (void)ws_size;
  (void)hipFuncSetAttribute((const void*)mha_fused, hipFuncAttributeMaxDynamicSharedMemorySize, L_BYTES);
  hipLaunchKernelGGL(mha_fused, dim3(NWG), dim3(NTHR), L_BYTES, stream, (const unsigned short*)d_in[0], (const unsigned short*)d_in[1],
                     (const unsigned short*)d_in[2], (const unsigned short*)d_in[3], (const unsigned short*)d_in[4],
                     (unsigned short*)d_out, (unsigned char*)d_ws);
}
```

```cpp
#include <hip/hip_runtime.h>
#include <hip/hip_bf16.h>

#define GAS __attribute__((address_space(1)))
#define LAS __attribute__((address_space(3)))
typedef unsigned v4u __attribute__((ext_vector_type(4)));
typedef unsigned v2u __attribute__((ext_vector_type(2)));
typedef float f32x4 __attribute__((ext_vector_type(4)));
typedef __bf16 bf16x8 __attribute__((ext_vector_type(8)));
typedef __bf16 bf16x2 __attribute__((ext_vector_type(2)));
typedef float f32x2 __attribute__((ext_vector_type(2)));

#define AUX_W 0
#define AUX_OUT 0
#define DESYNC 1
#define BMODE 3
#define BBAR 0
#define WQ_ROT 1
#define PACE 1
#define PACE_C 445
#define PACE_D 0
#define PACE_A 16
#define PACE_S0 10
#define PACE_B1 20
#define PACE_MIX 1
#define PACE_END 14
#define PACE_MAX 500
#define PACE_SP 3
#define PACE_P0 1
#define ROT_H 4
#define ROT_BP 1
#define ROT_BETA 8
#define POLL_SLEEP 2
#define SKIP_POS 2
#define WO_T0 1
#define TOUCH 1

namespace {

constexpr int NB = 16, SQ = 8, DM = 1024, NH = 32, SK = 2048, DH = 128;
constexpr int NWG = 256, NTHR = 512;
constexpr int NT = 16;
constexpr float CEXP = 0.08838834764831845f * 1.4426950408889634f;
constexpr int AUX_NT = 2, AUX_SC1 = 16;
constexpr int RSRC_W3 = 0x00020000;
constexpr unsigned SENT = 0xFFFFFFFFu;
constexpr unsigned kSpinCap = 1u << 16;

constexpr int WS_EPOCH = 0;
constexpr int WS_FLAG = 4096;
constexpr int WS_PACE = WS_FLAG + 32768;
constexpr int WS_O = 131072;
constexpr int WS_P = WS_O + 8 * 128 * 1024;
constexpr int WS_BYTES = WS_P + 8 * 8 * 16 * 2048;

constexpr int L_WAVE = 16384;
constexpr int L_QS = 8 * L_WAVE;
constexpr int QS_STRIDE = 272;
constexpr int L_ML = L_QS + 16 * QS_STRIDE;
constexpr int L_BYTES = L_ML + 1024;
constexpr int QP_STRIDE = 528;
constexpr int L_X = 9216, X_ODD = 2064;
constexpr int L_C = 8192;

__device__ __forceinline__ f32x4 mfma16(v4u a, v4u b, f32x4 c) {
  return __builtin_amdgcn_mfma_f32_16x16x32_bf16(__builtin_bit_cast(bf16x8, a), __builtin_bit_cast(bf16x8, b), c, 0, 0, 0);
}
__device__ __forceinline__ unsigned pack_lo(unsigned a, unsigned b) { return __builtin_amdgcn_perm(b, a, 0x05040100u); }
__device__ __forceinline__ unsigned pack_hi(unsigned a, unsigned b) { return __builtin_amdgcn_perm(b, a, 0x07060302u); }
__device__ __forceinline__ unsigned cvt_pk(float lo, float hi) {
  f32x2 v = {lo, hi};
  return __builtin_bit_cast(unsigned, __builtin_convertvector(v, bf16x2));
}
__device__ __forceinline__ v4u tr_frag(const v4u (&R)[8], int e) {
  v4u f;
#pragma unroll
  for (int jj = 0; jj < 4; ++jj)
    f[jj] = (e & 1) ? pack_hi(R[2 * jj][e >> 1], R[2 * jj + 1][e >> 1]) : pack_lo(R[2 * jj][e >> 1], R[2 * jj + 1][e >> 1]);
  return f;
}
template <int CTRL> __device__ __forceinline__ int dppi(int x) { return __builtin_amdgcn_mov_dpp(x, CTRL, 0xf, 0xf, true); }
__device__ __forceinline__ float ror8(float x) {
  return __builtin_bit_cast(float, dppi<0x128>(__builtin_bit_cast(int, x)));
}
__device__ __forceinline__ float bsel(unsigned mask, float a, float b) {
  return __uint_as_float((__float_as_uint(a) & mask) | (__float_as_uint(b) & ~mask));
}
__device__ __forceinline__ float xg_max(float x) {
  auto s = __builtin_amdgcn_permlane16_swap(__float_as_uint(x), __float_as_uint(x), false, false);
  x = fmaxf(__uint_as_float(s[0]), __uint_as_float(s[1]));
  auto t = __builtin_amdgcn_permlane32_swap(__float_as_uint(x), __float_as_uint(x), false, false);
  return fmaxf(__uint_as_float(t[0]), __uint_as_float(t[1]));
}
__device__ __forceinline__ float xg_sum(float x) {
  auto s = __builtin_amdgcn_permlane16_swap(__float_as_uint(x), __float_as_uint(x), false, false);
  x = __uint_as_float(s[0]) + __uint_as_float(s[1]);
  auto t = __builtin_amdgcn_permlane32_swap(__float_as_uint(x), __float_as_uint(x), false, false);
  return __uint_as_float(t[0]) + __uint_as_float(t[1]);
}
__device__ __forceinline__ unsigned umax4(v4u v) { return max(max(v[0], v[1]), max(v[2], v[3])); }
__device__ __forceinline__ float rs32(float a, float b) {
  auto t = __builtin_amdgcn_permlane32_swap(__float_as_uint(a), __float_as_uint(b), false, false);
  return __uint_as_float(t[0]) + __uint_as_float(t[1]);
}
__device__ __forceinline__ float rs16(float a, float b) {
  auto t = __builtin_amdgcn_permlane16_swap(__float_as_uint(a), __float_as_uint(b), false, false);
  return __uint_as_float(t[0]) + __uint_as_float(t[1]);
}
#define SB() __builtin_amdgcn_sched_barrier(0)
#define BAR() __builtin_amdgcn_s_barrier()
#define MEMFENCE() asm volatile("" ::: "memory")
#define LDS_BAR() { asm volatile("s_waitcnt lgkmcnt(0)" ::: "memory"); BAR(); MEMFENCE(); }
#define STAMP(i)
#define TSTAMP(i)

template <int V> struct IC { static constexpr int value = V; };
template <int I, int N, class F> __device__ __forceinline__ void static_for(F&& f) {
  if constexpr (I < N) { f(IC<I>{}); static_for<I + 1, N>(f); }
}

__global__ void __launch_bounds__(NTHR, 2)
mha_fused(const unsigned short* __restrict__ x, const unsigned short* __restrict__ kc, const unsigned short* __restrict__ vc,
          const unsigned short* __restrict__ wq, const unsigned short* __restrict__ wo, unsigned short* out, unsigned char* ws) {
  extern __shared__ __attribute__((aligned(16))) unsigned char smem[];
  LAS unsigned char* L = (LAS unsigned char*)smem;
  const int tid = threadIdx.x, lane = tid & 63, w = __builtin_amdgcn_readfirstlane(tid >> 6);
  const int r = lane & 15, g = lane >> 4;
  const int wg = blockIdx.x, idx = wg >> 3, xg = wg & 7, i4 = idx & 3;
  const int h = 4 * xg + i4, bp = idx >> 2;
  const int kq = xg >> 1, ct = 4 * (xg & 1) + ((i4 + 2) & 3);
  const int beta = w >> 2, ws4 = w & 3;
  const int bh = (2 * bp + beta) * NH + h;
  int rot = (ROT_H * h + ROT_BP * bp + ROT_BETA * beta) & (NT - 1);
  LAS unsigned char* LW = L + w * L_WAVE;
  STAMP(0);

  asm volatile("" : "+s"(out));
  const __amdgpu_buffer_rsrc_t rk = __builtin_amdgcn_make_buffer_rsrc((void*)(kc + (size_t)bh * SK * DH), 0, SK * DH * 2, RSRC_W3);
  const __amdgpu_buffer_rsrc_t rv = __builtin_amdgcn_make_buffer_rsrc((void*)(vc + (size_t)bh * SK * DH), 0, SK * DH * 2, RSRC_W3);
  const __amdgpu_buffer_rsrc_t rws = __builtin_amdgcn_make_buffer_rsrc((void*)ws, 0, WS_BYTES, RSRC_W3);
  const __amdgpu_buffer_rsrc_t rw0 = __builtin_amdgcn_make_buffer_rsrc((void*)ws, 0, w == 0 ? WS_BYTES : 0, RSRC_W3);
  const __amdgpu_buffer_rsrc_t rwq = __builtin_amdgcn_make_buffer_rsrc((void*)wq, 0, DM * NH * DH * 2, RSRC_W3);
  const __amdgpu_buffer_rsrc_t rwo = __builtin_amdgcn_make_buffer_rsrc((void*)wo, 0, NH * DH * DM * 2, RSRC_W3);
  const __amdgpu_buffer_rsrc_t rx = __builtin_amdgcn_make_buffer_rsrc((void*)x, 0, NB * SQ * DM * 2, RSRC_W3);
  const __amdgpu_buffer_rsrc_t rout = __builtin_amdgcn_make_buffer_rsrc((void*)out, 0, NB * SQ * DM * 2, RSRC_W3);

#define ITEM_AT(pos) ((pos) == 2 * NT - 3 ? 2 * NT - 2 : (pos) == 2 * NT - 2 ? 2 * NT - 3 : (pos))
#define NEXT_AFTER(pos) ((pos) < SKIP_POS ? (pos) + 3 : (pos) == SKIP_POS || (pos) + 2 >= 2 * NT ? -1 : ITEM_AT((pos) + 2))
#define NEXT_K(t) NEXT_AFTER(2 * (t))
#define NEXT_V(t) NEXT_AFTER(2 * (t) + 1)
  const int koff = (4 * ((r >> 1) & 1) + (r >> 2)) * 256 + (2 * g + (r & 1)) * 16;
  const int voff = lane * 16;
  v4u item[2 * NT][8];
#define TILE_OFF(t) (((((t) + rot) & (NT - 1)) * 4 + ws4) * 8192)
#define LOAD_ITEM(n) { const int so_ = TILE_OFF((n) >> 1); _Pragma("unroll") for (int i_ = 0; i_ < 8; ++i_) \
    item[n][i_] = ((n) & 1) ? __builtin_amdgcn_raw_buffer_load_b128(rv, voff, so_ + i_ * 1024, AUX_NT) \
                            : __builtin_amdgcn_raw_buffer_load_b128(rk, koff, so_ + (i_ >> 1) * 2048 + (i_ & 1) * 128, AUX_NT); }

  const int o_slot = WS_O + (bp * 128 + 4 * h) * 1024;
  const int p_base = WS_P + (bp * 8 + ct) * (16 * 2048);
  const int out_off = ((16 * bp + 2 * w) * DM + 128 * ct + 32 * kq) * 2;

  unsigned tch[6];
  if (TOUCH) {
    typedef const __attribute__((address_space(4))) unsigned* cptr;
    tch[0] = *(cptr)(kc + (size_t)bh * SK * DH);
    tch[1] = *(cptr)(vc + (size_t)bh * SK * DH);
    tch[2] = *(cptr)(wo + (size_t)(1024 * kq + 128 * w) * DM);
    tch[3] = *(cptr)(ws + o_slot);
    tch[4] = *(cptr)(ws + p_base);
    tch[5] = *(cptr)((const unsigned char*)out + (out_off & ~63));
    SB();
  }
  unsigned e_lo, e_hi;
  {
#define KBLK(s) ((((4 * beta + (s) + WQ_ROT * bp) & 7) << 2) | ws4)
    const int wqoff = (8 * g) * (NH * DH * 2) + r * 16;
    v4u wa[4][8], xr[4];
    v2u epoch0;
#define LOAD_WQ(s) { const int so_ = KBLK(s) * 32 * (NH * DH * 2) + h * (DH * 2); _Pragma("unroll") for (int j_ = 0; j_ < 8; ++j_) \
      wa[s][j_] = __builtin_amdgcn_raw_buffer_load_b128(rwq, wqoff, so_ + j_ * (NH * DH * 2), AUX_W); }
#define LOAD_X() { MEMFENCE(); epoch0 = __builtin_amdgcn_raw_buffer_load_b64(rws, 0, WS_EPOCH + bp * 256, AUX_SC1); \
      _Pragma("unroll") for (int i_ = 0; i_ < 4; ++i_) \
      xr[i_] = __builtin_amdgcn_raw_buffer_load_b128(rx, voff, (16 * bp + 2 * w) * (DM * 2) + i_ * 1024, AUX_W); }
#define STAGE_X() { _Pragma("unroll") for (int i_ = 0; i_ < 4; ++i_) \
      *(LAS v4u*)(LW + L_X + w * 32 + (i_ >> 1) * X_ODD + (i_ & 1) * 1024 + voff) = xr[i_]; \
      e_lo = __builtin_amdgcn_readfirstlane(epoch0[0]); e_hi = __builtin_amdgcn_readfirstlane(epoch0[1]); \
      asm volatile("s_waitcnt lgkmcnt(0)" : "+s"(e_lo), "+s"(e_hi) :: "memory"); }
    const LAS unsigned char* xrow = L + (r >> 1) * (L_WAVE + 32) + L_X + (r & 1) * X_ODD + g * 16;
    f32x4 qa[8];
#pragma unroll
    for (int e = 0; e < 8; ++e) qa[e] = f32x4{0.f, 0.f, 0.f, 0.f};
#define MMA_WQ(s) { const v4u xf_ = *(const LAS v4u*)(xrow + KBLK(s) * 64); \
      _Pragma("unroll") for (int e_ = 0; e_ < 8; ++e_) qa[e_] = mfma16(tr_frag(wa[s], e_), xf_, qa[e_]); }
#define PUT_Q() { _Pragma("unroll") for (int rho = 0; rho < 4; ++rho) { \
      LAS f32x4* p_ = (LAS f32x4*)(LW + r * QP_STRIDE + (32 * g + 8 * rho) * 4); \
      p_[0] = f32x4{qa[0][rho], qa[1][rho], qa[2][rho], qa[3][rho]}; \
      p_[1] = f32x4{qa[4][rho], qa[5][rho], qa[6][rho], qa[7][rho]}; } }
#define REDUCE_Q() { const int row_ = tid >> 5, n4_ = (tid & 31) * 4; f32x4 pa_[8]; \
      _Pragma("unroll") for (int ww = 0; ww < 8; ++ww) pa_[ww] = *(LAS f32x4*)(L + ww * L_WAVE + row_ * QP_STRIDE + n4_ * 4); \
      const f32x4 s_ = ((pa_[0] + pa_[1]) + (pa_[2] + pa_[3])) + ((pa_[4] + pa_[5]) + (pa_[6] + pa_[7])); \
      *(LAS v2u*)(L + L_QS + row_ * QS_STRIDE + n4_ * 2) = v2u{cvt_pk(s_[0], s_[1]), cvt_pk(s_[2], s_[3])}; }
    if (!DESYNC || (bp & 1) == 0) {
      LOAD_X(); SB(); LOAD_WQ(0); SB(); LOAD_WQ(1); SB(); LOAD_WQ(2); SB(); LOAD_WQ(3); SB();
      STAMP(16);
      STAGE_X(); SB();
      BAR(); MEMFENCE(); SB();
      STAMP(17);
      LOAD_ITEM(0); SB();
      STAMP(18);
      MMA_WQ(0); MMA_WQ(1); MMA_WQ(2); MMA_WQ(3);
      PUT_Q(); SB();
      STAMP(19);
      LOAD_ITEM(1); SB();
      STAMP(20);
      LDS_BAR(); SB();
      STAMP(21);
      REDUCE_Q(); SB();
      LOAD_ITEM(2); SB();
      STAMP(22);
    } else {
      LOAD_X(); SB(); LOAD_WQ(0); SB();
      LOAD_ITEM(0); SB(); LOAD_ITEM(1); SB();
      STAMP(16);
      STAGE_X(); SB();
      BAR(); MEMFENCE(); SB();
      STAMP(17);
      LOAD_WQ(1); SB(); LOAD_WQ(2); SB();
      MMA_WQ(0); SB();
      LOAD_WQ(3); SB();
      STAMP(18);
      MMA_WQ(1); SB();
      if (BBAR) { BAR(); SB(); }
      LOAD_ITEM(2); SB();
      STAMP(19);
      MMA_WQ(2); MMA_WQ(3);
      PUT_Q(); SB();
      STAMP(20);
      LDS_BAR(); SB();
      STAMP(21);
      REDUCE_Q(); SB();
      STAMP(22);
    }
  }
  if (TOUCH) asm volatile("" :: "s"(tch[0]), "s"(tch[1]), "s"(tch[2]), "s"(tch[3]), "s"(tch[4]), "s"(tch[5]));
  LDS_BAR();
  asm volatile("" : "+s"(rot));
  v4u qf[2];
#pragma unroll
  for (int hf = 0; hf < 2; ++hf) qf[hf] = *(LAS v4u*)(L + L_QS + (8 * beta + (r & 7)) * QS_STRIDE + (8 * hf + 2 * g + (r >> 3)) * 16);
  unsigned hmask = (r >> 3) ? 0xFFFFFFFFu : 0u;
  asm volatile("" : "+v"(hmask));
  const unsigned pmask = hmask ^ 0x0000FFFFu;

  STAMP(1);
  v4u wr[4][8], wf[4][8];
  const int wooff = (32 * g) * (DM * 2) + r * 16;
  const int wos = (1024 * kq + 128 * w) * (DM * 2) + ct * 256;
#define LOAD_WO(ks) { _Pragma("unroll") for (int j_ = 0; j_ < 8; ++j_) \
    wr[ks][j_] = __builtin_amdgcn_raw_buffer_load_b128(rwo, wooff, wos + (8 * (j_ & 3) + 2 * (((ks) + w) & 3) + (j_ >> 2)) * (DM * 2), AUX_W); }
#define PARK_WO(ks) { _Pragma("unroll") for (int e_ = 0; e_ < 8; ++e_) *(LAS v4u*)(LW + (ks) * 8192 + e_ * 1024 + voff) = tr_frag(wr[ks], e_); }
#define KEEP_WO(ks) { _Pragma("unroll") for (int e_ = 0; e_ < 8; ++e_) { wf[ks][e_] = tr_frag(wr[ks], e_); asm volatile("" : "+v"(wf[ks][e_])); } }
#define BACK_WO(ks) { _Pragma("unroll") for (int e_ = 0; e_ < 8; ++e_) wf[ks][e_] = *(LAS v4u*)(LW + (ks) * 8192 + e_ * 1024 + voff); }

  f32x4 oacc[8];
#pragma unroll
  for (int e = 0; e < 8; ++e) oacc[e] = f32x4{0.f, 0.f, 0.f, 0.f};
  float m = -INFINITY, lsum = 0.f;
  const unsigned long long tag64 = (((unsigned long long)e_hi << 32) | e_lo) + 1ull;
  const unsigned tag_lo = (unsigned)tag64, tag_hi = (unsigned)(tag64 >> 32);
  v2u flags_v, snap;
  const unsigned tbase = PACE ? (unsigned)__builtin_amdgcn_s_memrealtime() : 0u;
  int tau_ref = -1000000, tau_slope = 0;

  static_for<0, NT - 2>([&](auto tc) __attribute__((always_inline)) {
    constexpr int t = decltype(tc)::value;
    f32x4 sa[4];
#pragma unroll
    for (int u = 0; u < 4; ++u) sa[u] = mfma16(item[2 * t][2 * u], qf[0], f32x4{0.f, 0.f, 0.f, 0.f});
#pragma unroll
    for (int u = 0; u < 4; ++u) sa[u] = mfma16(item[2 * t][2 * u + 1], qf[1], sa[u]);
    float s[4];
#pragma unroll
    for (int u = 0; u < 4; ++u) {
      const float xx = bsel(hmask, sa[u][3], sa[u][0]), yy = bsel(hmask, sa[u][1], sa[u][2]);
      s[u] = xx + ror8(yy);
    }
    asm volatile("" : "+v"(s[0]), "+v"(s[1]), "+v"(s[2]), "+v"(s[3]));
    SB();
    if constexpr (t == 0) {
      const int p_own = ((voff >> 7) & 3) * 8192 + (voff >> 9) * 128 + (voff & 0x70);
      __builtin_amdgcn_raw_buffer_store_b64(v2u{SENT, SENT}, rws, voff >> 1, o_slot + w * 512, AUX_SC1);
      __builtin_amdgcn_raw_buffer_store_b128(v4u{SENT, SENT, SENT, SENT}, rws, p_own, p_base + kq * 2048 + w * 256, AUX_SC1);
      MEMFENCE();
      SB();
    }
    if constexpr (t >= WO_T0 && t < WO_T0 + 4) LOAD_WO(t - WO_T0);
    if constexpr (t == NT - 5) {
      flags_v = __builtin_amdgcn_raw_buffer_load_b64(rw0, (voff << 3) & 0xF80, WS_FLAG + bp * 4096, AUX_SC1);
    }
    if constexpr (PACE && t >= PACE_P0 + 2 && (t - 2) % PACE_SP == PACE_P0 % PACE_SP && t < PACE_END) {
      int v = (int)(snap[0] - tbase);
      const bool ok = snap[1] == ~snap[0] && v > -3000 && v < 3000;
      v += dppi<0xB1>(v); v += dppi<0x4E>(v); v += dppi<0x141>(v);
      const bool all_ok = (__builtin_amdgcn_ballot_w64(ok) & 0xFFull) == 0xFFull;
      const int mean = __builtin_amdgcn_readfirstlane(v) >> 3;
      if constexpr (PACE_A && t >= PACE_P0 + 2 + PACE_SP) {
        const int drift = (mean - tau_ref) * PACE_A / (16 * PACE_SP) - PACE_S0 - (t - 2 - PACE_SP < 3 ? PACE_B1 : 0);
        tau_slope = (all_ok && tau_ref > -1000000) ? min(max(drift, 0), 60) : 0;
      }
      tau_ref = all_ok ? mean : -1000000;
    }
    if constexpr (PACE && t >= PACE_P0 + 1 && (t - 1) % PACE_SP == PACE_P0 % PACE_SP && t < PACE_END - 1) {
      const int px = (voff >> 4) & 7;
      const int peer = PACE_MIX ? ((((bp + px) & 7) * 4 + ((i4 + px) & 3)) * 8 + ((xg + px) & 7)) : idx * 8 + px;
      snap = __builtin_amdgcn_raw_buffer_load_b64(rw0, peer * 128, WS_PACE, AUX_SC1);
      SB();
    }
    if constexpr (NEXT_K(t) >= 0) LOAD_ITEM(NEXT_K(t));
    SB();
    if constexpr (PACE && t >= PACE_P0 + 2 && t < PACE_END) {
      constexpr int age = 2 + (t - 2 - PACE_P0) % PACE_SP;
      int now = (int)((unsigned)__builtin_amdgcn_s_memrealtime() - tbase);
      const int allow = min(tau_ref - PACE_D + t * PACE_C + age * tau_slope, now + PACE_MAX);
      while (now < allow) { __builtin_amdgcn_s_sleep(2); now = (int)((unsigned)__builtin_amdgcn_s_memrealtime() - tbase); }
    }
    if constexpr (t == 3) {
      asm volatile("s_waitcnt vmcnt(42)" ::: "memory");
      BAR();
      MEMFENCE();
      __builtin_amdgcn_raw_buffer_store_b64(v2u{tag_lo, tag_hi}, rw0, (voff >> 1) & 0x78, WS_FLAG + (bp * 32 + h) * 128, AUX_SC1);
    } else if constexpr (t == NT - 3) {
      if (w == 0) {
        if (__builtin_expect(!__all(flags_v[0] == tag_lo && flags_v[1] == tag_hi), 0)) {
          for (unsigned spins = 0; spins < kSpinCap; ++spins) {
            __builtin_amdgcn_s_sleep(2);
            MEMFENCE();
            const v2u fv = __builtin_amdgcn_raw_buffer_load_b64(rws, (voff << 3) & 0xF80, WS_FLAG + bp * 4096, AUX_SC1);
            if (__all(fv[0] == tag_lo && fv[1] == tag_hi)) break;
          }
        }
        if (h == 0) __builtin_amdgcn_raw_buffer_store_b64(v2u{tag_lo, tag_hi}, rws, (voff >> 1) & 0x78, WS_EPOCH + bp * 256, AUX_SC1);
      }
      MEMFENCE();
      BAR();
      MEMFENCE();
    } else BAR();
    if constexpr (PACE && t >= PACE_P0 && t % PACE_SP == PACE_P0 % PACE_SP && t < PACE_END - 2) {
      const unsigned tv = (unsigned)__builtin_amdgcn_s_memrealtime() - (unsigned)(t * PACE_C);
      __builtin_amdgcn_raw_buffer_store_b64(v2u{tv, ~tv}, rw0, (voff >> 1) & 0x78, WS_PACE + wg * 128, AUX_SC1);
    }
    SB();
    const float mx = xg_max(fmaxf(fmaxf(s[0], s[1]), fmaxf(s[2], s[3])));
    const float mn = fmaxf(m, mx);
    const float alpha = __builtin_amdgcn_exp2f((m - mn) * CEXP);
    const float mc = mn * CEXP;
    m = mn;
    float p[4];
#pragma unroll
    for (int u = 0; u < 4; ++u) p[u] = __builtin_amdgcn_exp2f(__builtin_fmaf(s[u], CEXP, -mc));
    lsum = lsum * alpha + ((p[0] + p[1]) + (p[2] + p[3]));
    asm volatile("" : "+v"(lsum));
    const v4u pf = {cvt_pk(p[0], p[0]) & pmask, cvt_pk(p[1], p[1]) & pmask, cvt_pk(p[2], p[2]) & pmask, cvt_pk(p[3], p[3]) & pmask};
#pragma unroll
    for (int e = 0; e < 8; ++e) oacc[e] *= alpha;
#pragma unroll
    for (int e = 0; e < 8; ++e) oacc[e] = mfma16(tr_frag(item[2 * t + 1], e), pf, oacc[e]);
    SB();
    if constexpr ((t & 1) == 1) STAMP(8 + (t >> 1));
    if constexpr (t == WO_T0 + 1) PARK_WO(0);
    if constexpr (t == WO_T0 + 2) PARK_WO(1);
    if constexpr (t == WO_T0 + 3) KEEP_WO(2);
    if constexpr (t == WO_T0 + 4) KEEP_WO(3);
    SB();
    if constexpr (NEXT_V(t) >= 0) LOAD_ITEM(NEXT_V(t));
    SB();
  });
  {
    float s[8];
    static_for<0, 2>([&](auto hc) __attribute__((always_inline)) {
      constexpr int hh = decltype(hc)::value, n = 2 * (NT - 2 + hh);
      f32x4 sa[4];
#pragma unroll
      for (int u = 0; u < 4; ++u) sa[u] = mfma16(item[n][2 * u], qf[0], f32x4{0.f, 0.f, 0.f, 0.f});
#pragma unroll
      for (int u = 0; u < 4; ++u) sa[u] = mfma16(item[n][2 * u + 1], qf[1], sa[u]);
#pragma unroll
      for (int u = 0; u < 4; ++u) s[4 * hh + u] = bsel(hmask, sa[u][3], sa[u][0]) + ror8(bsel(hmask, sa[u][1], sa[u][2]));
      asm volatile("" : "+v"(s[4 * hh]), "+v"(s[4 * hh + 1]), "+v"(s[4 * hh + 2]), "+v"(s[4 * hh + 3]));
      SB();
      if constexpr (hh == 0) { LOAD_ITEM(NEXT_AFTER(2 * NT - 4)); SB(); }
    });
    BACK_WO(0);
    const float mx = xg_max(fmaxf(fmaxf(fmaxf(s[0], s[1]), fmaxf(s[2], s[3])), fmaxf(fmaxf(s[4], s[5]), fmaxf(s[6], s[7]))));
    const float mn = fmaxf(m, mx);
    float alpha = __builtin_amdgcn_exp2f((m - mn) * CEXP);
    const float mc = mn * CEXP;
    m = mn;
    float p[8];
#pragma unroll
    for (int u = 0; u < 8; ++u) p[u] = __builtin_amdgcn_exp2f(__builtin_fmaf(s[u], CEXP, -mc));
    lsum = xg_sum(lsum * alpha + (((p[0] + p[1]) + (p[2] + p[3])) + ((p[4] + p[5]) + (p[6] + p[7]))));
    LAS unsigned char* ml = L + L_ML + beta * 512 + ((voff >> 4) & 7) * 64;
    if (voff < 256) *(LAS f32x2*)(ml + ws4 * 16 + (voff >> 7) * 8) = f32x2{m, lsum};
    SB();
    LOAD_ITEM(2 * NT - 1); SB();
    LDS_BAR();
    f32x4 rec[4];
#pragma unroll
    for (int j = 0; j < 4; ++j) rec[j] = *(LAS f32x4*)(ml + j * 16);
    const float M = fmaxf(fmaxf(fmaxf(rec[0][0], rec[0][2]), fmaxf(rec[1][0], rec[1][2])), fmaxf(fmaxf(rec[2][0], rec[2][2]), fmaxf(rec[3][0], rec[3][2])));
    float Ls = 0.f;
#pragma unroll
    for (int j = 0; j < 4; ++j)
      Ls += __builtin_amdgcn_exp2f((rec[j][0] - M) * CEXP) * rec[j][1] + __builtin_amdgcn_exp2f((rec[j][2] - M) * CEXP) * rec[j][3];
    const float scale = __builtin_amdgcn_exp2f((m - M) * CEXP) / Ls;
    alpha *= scale;
    v4u pf[2];
#pragma unroll
    for (int u = 0; u < 8; ++u) { const float ps = p[u] * scale; pf[u >> 2][u & 3] = cvt_pk(ps, ps) & pmask; }
#pragma unroll
    for (int e = 0; e < 8; ++e) oacc[e] *= alpha;
#pragma unroll
    for (int e = 0; e < 8; ++e) oacc[e] = mfma16(tr_frag(item[2 * NT - 3], e), pf[0], oacc[e]);
    SB();
    BACK_WO(1);
    SB();
#pragma unroll
    for (int e = 0; e < 8; ++e) oacc[e] = mfma16(tr_frag(item[2 * NT - 1], e), pf[1], oacc[e]);
    SB();
    STAMP(15);
  }

  STAMP(2);
  const int lane2 = __builtin_amdgcn_mbcnt_hi(~0u, __builtin_amdgcn_mbcnt_lo(~0u, 0u));
  asm volatile("s_waitcnt lgkmcnt(0)" ::: "memory");
#pragma unroll
  for (int e = 0; e < 8; ++e) *(LAS f32x4*)(LW + e * 1024 + lane2 * 16) = oacc[e];
  const int goff = WS_O + (bp * 128 + 4 * (8 * kq + w)) * 1024;
  v4u ob[4];
#pragma unroll
  for (int ks = 0; ks < 4; ++ks) ob[ks] = __builtin_amdgcn_raw_buffer_load_b128(rws, lane2 * 16, goff + ((ks + w) & 3) * 1024, AUX_SC1);
  TSTAMP(21);
  LDS_BAR();
  STAMP(3);
  {
    const int fl = 32 * (w & 1) + (lane2 >> 1);
    const LAS unsigned char* src = L + ((fl >> 3) & 1) * (4 * L_WAVE) + ((w & ~1) + (lane2 & 1)) * 1024 + (fl & 0x37) * 16;
    const int par = (lane2 & 1) * 128;
    f32x4 pa[8];
#pragma unroll
    for (int j = 0; j < 8; ++j) pa[j] = *(const LAS f32x4*)(src + (j >> 1) * L_WAVE + ((j & 1) ? 128 - par : par));
    const f32x4 a0 = ((pa[0] + pa[1]) + (pa[2] + pa[3])) + ((pa[4] + pa[5]) + (pa[6] + pa[7]));
    v2u ov = {cvt_pk(a0[0], a0[1]), cvt_pk(a0[2], a0[3])};
#pragma unroll
    for (int j = 0; j < 2; ++j) ov[j] = (ov[j] == SENT) ? 0x7FC07FC0u : ov[j];
    __builtin_amdgcn_raw_buffer_store_b64(ov, rws, lane2 * 8, o_slot + w * 512, AUX_SC1);
  }
  SB();
  STAMP(4);
  {
    TSTAMP(16);
    unsigned spins = 0;
    while (__any(max(max(umax4(ob[0]), umax4(ob[1])), max(umax4(ob[2]), umax4(ob[3]))) == SENT)) {
      if (++spins > kSpinCap) break;
      if (spins > 1) __builtin_amdgcn_s_sleep(POLL_SLEEP);
      MEMFENCE();
#pragma unroll
      for (int ks = 0; ks < 4; ++ks) ob[ks] = __builtin_amdgcn_raw_buffer_load_b128(rws, lane2 * 16, goff + ((ks + w) & 3) * 1024, AUX_SC1);
    }
    TSTAMP(17);
#pragma unroll
    for (int hf = 0; hf < 2; ++hf) {
      f32x4 c[4];
#pragma unroll
      for (int e = 0; e < 4; ++e) c[e] = f32x4{0.f, 0.f, 0.f, 0.f};
#pragma unroll
      for (int ks = 0; ks < 4; ++ks)
#pragma unroll
        for (int e = 0; e < 4; ++e) c[e] = mfma16(wf[ks][4 * hf + e], ob[ks], c[e]);
#pragma unroll
      for (int rho = 0; rho < 4; ++rho)
        *(LAS f32x4*)(LW + L_C + (((lane2 & 15) * 512 + (lane2 >> 4) * 128 + (lane2 & 7) * 16) ^ (rho * 32 + hf * 16))) =
            f32x4{c[0][rho], c[1][rho], c[2][rho], c[3][rho]};
    }
  }
  TSTAMP(18);
  LDS_BAR();
  STAMP(5);
  {
    const int row = 2 * w + (lane2 >> 5);
    const int so = row * 512 + (((lane2 & 31) * 16) ^ ((row & 7) * 16));
    f32x4 pa[8];
#pragma unroll
    for (int ww = 0; ww < 8; ++ww) pa[ww] = *(const LAS f32x4*)(L + ww * L_WAVE + L_C + so);
    const f32x4 s = ((pa[0] + pa[1]) + (pa[2] + pa[3])) + ((pa[4] + pa[5]) + (pa[6] + pa[7]));
    v4u sv = __builtin_bit_cast(v4u, s);
#pragma unroll
    for (int j = 0; j < 4; ++j) sv[j] = (sv[j] == SENT) ? 0x7FC00000u : sv[j];
    const int p_own2 = ((lane2 >> 3) & 3) * 8192 + (lane2 >> 5) * 128 + (lane2 & 7) * 16;
    __builtin_amdgcn_raw_buffer_store_b128(sv, rws, p_own2, p_base + kq * 2048 + w * 256, AUX_SC1);
  }
  TSTAMP(19);
  {
    const int poff = (lane2 >> 4) * 2048 + (lane2 & 15) * 16;
    int oo = ((lane2 >> 3) & 1) * (DM * 2) + (lane2 & 7) * 8 + (lane2 >> 5) * 4 + (lane2 & 16) * 0x1000000;
    asm volatile("" : "+v"(oo));
    v4u pv;
    unsigned spins = 0;
    for (;;) {
      MEMFENCE();
      pv = __builtin_amdgcn_raw_buffer_load_b128(rws, poff, p_base + kq * 8192 + w * 256, AUX_SC1);
      if (!__any(umax4(pv) == SENT)) break;
      if (++spins > kSpinCap) break;
      __builtin_amdgcn_s_sleep(POLL_SLEEP);
    }
    TSTAMP(20);
    const float u0 = rs32(__uint_as_float(pv[0]), __uint_as_float(pv[2])), u1 = rs32(__uint_as_float(pv[1]), __uint_as_float(pv[3]));
    const float t0 = rs16(u0, u0), t1 = rs16(u1, u1);
    __builtin_amdgcn_raw_buffer_store_b32(cvt_pk(t0, t1), rout, oo, out_off, AUX_OUT);
  }
}

}

extern "C" void kernel_launch(void* const* d_in, const int* in_sizes, int n_in, void* d_out, int out_size, void* d_ws,
                              size_t ws_size, hipStream_t stream) {
  (void)in_sizes; (void)n_in; (void)out_size; (void)ws_size;
  (void)hipFuncSetAttribute((const void*)mha_fused, hipFuncAttributeMaxDynamicSharedMemorySize, L_BYTES);
  hipLaunchKernelGGL(mha_fused, dim3(NWG), dim3(NTHR), L_BYTES, stream, (const unsigned short*)d_in[0], (const unsigned short*)d_in[1],
                     (const unsigned short*)d_in[2], (const unsigned short*)d_in[3], (const unsigned short*)d_in[4],
                     (unsigned short*)d_out, (unsigned char*)d_ws);
}
```

```cpp
#include <hip/hip_runtime.h>
#include <hip/hip_bf16.h>

#define GAS __attribute__((address_space(1)))
#define LAS __attribute__((address_space(3)))
typedef unsigned v4u __attribute__((ext_vector_type(4)));
typedef unsigned v2u __attribute__((ext_vector_type(2)));
typedef float f32x4 __attribute__((ext_vector_type(4)));
typedef __bf16 bf16x8 __attribute__((ext_vector_type(8)));
typedef __bf16 bf16x2 __attribute__((ext_vector_type(2)));
typedef float f32x2 __attribute__((ext_vector_type(2)));

#define AUX_W 0
#define AUX_OUT 0
#define DESYNC 1
#define BMODE 3
#define BBAR 0
#define WQ_ROT 1
#define PACE 1
#define PACE_C 448
#define PACE_D 0
#define PACE_A 16
#define PACE_S0 4
#define PACE_B4T 0
#define SNAP_EARLY 1
#define PACE_B4 42
#define PACE_DZ 32
#define PACE_TC 100
#define PACE_DZ2 20
#define PACE_V0 1
#define PACE_END 14
#define PACE_MAX 500
#define PACE_SP 3
#define PACE_P0 1
#define ROT_H 4
#define ROT_BP 1
#define ROT_BETA 8
#define FIRSTLOOK 1
#define POLL_FIRST 0
#define PROBE 1
#define PROBE_AFTER 0
#define POLL2_FIRST 8
#define POLL2_SLEEP 2
#define POLL_SLEEP 2
#define SKIP_POS 2
#define WO_T0 1
#define TOUCH 1

namespace {

constexpr int NB = 16, SQ = 8, DM = 1024, NH = 32, SK = 2048, DH = 128;
constexpr int NWG = 256, NTHR = 512;
constexpr int NT = 16;
constexpr float CEXP = 0.08838834764831845f * 1.4426950408889634f;
constexpr int AUX_NT = 2, AUX_SC1 = 16;
constexpr int RSRC_W3 = 0x00020000;
constexpr unsigned SENT = 0xFFFFFFFFu;
constexpr unsigned kSpinCap = 1u << 16;

constexpr int WS_EPOCH = 0;
constexpr int WS_FLAG = 4096;
constexpr int WS_PACE = WS_FLAG + 32768;
constexpr int WS_O = 131072;
constexpr int WS_P = WS_O + 8 * 128 * 1024;
constexpr int WS_BYTES = WS_P + 8 * 8 * 16 * 2048;

constexpr int L_WAVE = 16384;
constexpr int L_QS = 8 * L_WAVE;
constexpr int QS_STRIDE = 272;
constexpr int L_ML = L_QS + 16 * QS_STRIDE;
constexpr int L_PC = L_ML + 1024;
constexpr int L_BYTES = L_PC + 16;
constexpr int QP_STRIDE = 528;
constexpr int L_X = 9216, X_ODD = 2064;
constexpr int L_C = 8192;

__device__ __forceinline__ f32x4 mfma16(v4u a, v4u b, f32x4 c) {
  return __builtin_amdgcn_mfma_f32_16x16x32_bf16(__builtin_bit_cast(bf16x8, a), __builtin_bit_cast(bf16x8, b), c, 0, 0, 0);
}
__device__ __forceinline__ unsigned pack_lo(unsigned a, unsigned b) { return __builtin_amdgcn_perm(b, a, 0x05040100u); }
__device__ __forceinline__ unsigned pack_hi(unsigned a, unsigned b) { return __builtin_amdgcn_perm(b, a, 0x07060302u); }
__device__ __forceinline__ unsigned cvt_pk(float lo, float hi) {
  f32x2 v = {lo, hi};
  return __builtin_bit_cast(unsigned, __builtin_convertvector(v, bf16x2));
}
__device__ __forceinline__ v4u tr_frag(const v4u (&R)[8], int e) {
  v4u f;
#pragma unroll
  for (int jj = 0; jj < 4; ++jj)
    f[jj] = (e & 1) ? pack_hi(R[2 * jj][e >> 1], R[2 * jj + 1][e >> 1]) : pack_lo(R[2 * jj][e >> 1], R[2 * jj + 1][e >> 1]);
  return f;
}
template <int CTRL> __device__ __forceinline__ int dppi(int x) { return __builtin_amdgcn_mov_dpp(x, CTRL, 0xf, 0xf, true); }
__device__ __forceinline__ float ror8(float x) {
  return __builtin_bit_cast(float, dppi<0x128>(__builtin_bit_cast(int, x)));
}
__device__ __forceinline__ float bsel(unsigned mask, float a, float b) {
  return __uint_as_float((__float_as_uint(a) & mask) | (__float_as_uint(b) & ~mask));
}
__device__ __forceinline__ float xg_max(float x) {
  auto s = __builtin_amdgcn_permlane16_swap(__float_as_uint(x), __float_as_uint(x), false, false);
  x = fmaxf(__uint_as_float(s[0]), __uint_as_float(s[1]));
  auto t = __builtin_amdgcn_permlane32_swap(__float_as_uint(x), __float_as_uint(x), false, false);
  return fmaxf(__uint_as_float(t[0]), __uint_as_float(t[1]));
}
__device__ __forceinline__ float xg_sum(float x) {
  auto s = __builtin_amdgcn_permlane16_swap(__float_as_uint(x), __float_as_uint(x), false, false);
  x = __uint_as_float(s[0]) + __uint_as_float(s[1]);
  auto t = __builtin_amdgcn_permlane32_swap(__float_as_uint(x), __float_as_uint(x), false, false);
  return __uint_as_float(t[0]) + __uint_as_float(t[1]);
}
__device__ __forceinline__ unsigned umax4(v4u v) { return max(max(v[0], v[1]), max(v[2], v[3])); }
__device__ __forceinline__ float rs32(float a, float b) {
  auto t = __builtin_amdgcn_permlane32_swap(__float_as_uint(a), __float_as_uint(b), false, false);
  return __uint_as_float(t[0]) + __uint_as_float(t[1]);
}
__device__ __forceinline__ float rs16(float a, float b) {
  auto t = __builtin_amdgcn_permlane16_swap(__float_as_uint(a), __float_as_uint(b), false, false);
  return __uint_as_float(t[0]) + __uint_as_float(t[1]);
}
#define SB() __builtin_amdgcn_sched_barrier(0)
#define BAR() __builtin_amdgcn_s_barrier()
#define MEMFENCE() asm volatile("" ::: "memory")
#define LDS_BAR() { asm volatile("s_waitcnt lgkmcnt(0)" ::: "memory"); BAR(); MEMFENCE(); }
#define STAMP(i)
#define TSTAMP(i)

template <int V> struct IC { static constexpr int value = V; };
template <int I, int N, class F> __device__ __forceinline__ void static_for(F&& f) {
  if constexpr (I < N) { f(IC<I>{}); static_for<I + 1, N>(f); }
}

__global__ void __launch_bounds__(NTHR, 2)
mha_fused(const unsigned short* __restrict__ x, const unsigned short* __restrict__ kc, const unsigned short* __restrict__ vc,
          const unsigned short* __restrict__ wq, const unsigned short* __restrict__ wo, unsigned short* out, unsigned char* ws) {
  extern __shared__ __attribute__((aligned(16))) unsigned char smem[];
  LAS unsigned char* L = (LAS unsigned char*)smem;
  const int tid = threadIdx.x, lane = tid & 63, w = __builtin_amdgcn_readfirstlane(tid >> 6);
  const int r = lane & 15, g = lane >> 4;
  const int wg = blockIdx.x, idx = wg >> 3, xg = wg & 7, i4 = idx & 3;
  const int h = 4 * xg + i4, bp = idx >> 2;
  const int kq = xg >> 1, ct = 4 * (xg & 1) + ((i4 + 2) & 3);
  const int beta = w >> 2, ws4 = w & 3;
  const int bh = (2 * bp + beta) * NH + h;
  int rot = (ROT_H * h + ROT_BP * bp + ROT_BETA * beta) & (NT - 1);
  LAS unsigned char* LW = L + w * L_WAVE;
  STAMP(0);

  asm volatile("" : "+s"(out));
  const __amdgpu_buffer_rsrc_t rk = __builtin_amdgcn_make_buffer_rsrc((void*)(kc + (size_t)bh * SK * DH), 0, SK * DH * 2, RSRC_W3);
  const __amdgpu_buffer_rsrc_t rv = __builtin_amdgcn_make_buffer_rsrc((void*)(vc + (size_t)bh * SK * DH), 0, SK * DH * 2, RSRC_W3);
  const __amdgpu_buffer_rsrc_t rws = __builtin_amdgcn_make_buffer_rsrc((void*)ws, 0, WS_BYTES, RSRC_W3);
  const __amdgpu_buffer_rsrc_t rw0 = __builtin_amdgcn_make_buffer_rsrc((void*)ws, 0, w == 0 ? WS_BYTES : 0, RSRC_W3);
  const __amdgpu_buffer_rsrc_t rwq = __builtin_amdgcn_make_buffer_rsrc((void*)wq, 0, DM * NH * DH * 2, RSRC_W3);
  const __amdgpu_buffer_rsrc_t rwo = __builtin_amdgcn_make_buffer_rsrc((void*)wo, 0, NH * DH * DM * 2, RSRC_W3);
  const __amdgpu_buffer_rsrc_t rx = __builtin_amdgcn_make_buffer_rsrc((void*)x, 0, NB * SQ * DM * 2, RSRC_W3);
  const __amdgpu_buffer_rsrc_t rout = __builtin_amdgcn_make_buffer_rsrc((void*)out, 0, NB * SQ * DM * 2, RSRC_W3);

#define ITEM_AT(pos) ((pos) == 2 * NT - 3 ? 2 * NT - 2 : (pos) == 2 * NT - 2 ? 2 * NT - 3 : (pos))
#define NEXT_AFTER(pos) ((pos) < SKIP_POS ? (pos) + 3 : (pos) == SKIP_POS || (pos) + 2 >= 2 * NT ? -1 : ITEM_AT((pos) + 2))
#define NEXT_K(t) NEXT_AFTER(2 * (t))
#define NEXT_V(t) NEXT_AFTER(2 * (t) + 1)
  const int koff = (4 * ((r >> 1) & 1) + (r >> 2)) * 256 + (2 * g + (r & 1)) * 16;
  const int voff = lane * 16;
  v4u item[2 * NT][8];
#define TILE_OFF(t) (((((t) + rot) & (NT - 1)) * 4 + ws4) * 8192)
#define LOAD_ITEM(n) { const int so_ = TILE_OFF((n) >> 1); _Pragma("unroll") for (int i_ = 0; i_ < 8; ++i_) \
    item[n][i_] = ((n) & 1) ? __builtin_amdgcn_raw_buffer_load_b128(rv, voff, so_ + i_ * 1024, AUX_NT) \
                            : __builtin_amdgcn_raw_buffer_load_b128(rk, koff, so_ + (i_ >> 1) * 2048 + (i_ & 1) * 128, AUX_NT); }

  const int o_slot = WS_O + (bp * 128 + 4 * h) * 1024;
  const int p_base = WS_P + (bp * 8 + ct) * (16 * 2048);
  const int out_off = ((16 * bp + 2 * w) * DM + 128 * ct + 32 * kq) * 2;

  unsigned tch[6];
  if (TOUCH) {
    typedef const __attribute__((address_space(4))) unsigned* cptr;
    tch[0] = *(cptr)(kc + (size_t)bh * SK * DH);
    tch[1] = *(cptr)(vc + (size_t)bh * SK * DH);
    tch[2] = *(cptr)(wo + (size_t)(1024 * kq + 128 * w) * DM);
    tch[3] = *(cptr)(ws + o_slot);
    tch[4] = *(cptr)(ws + p_base);
    tch[5] = *(cptr)((const unsigned char*)out + (out_off & ~63));
    SB();
  }
  unsigned e_lo, e_hi;
  {
#define KBLK(s) ((((4 * beta + (s) + WQ_ROT * bp) & 7) << 2) | ws4)
    const int wqoff = (8 * g) * (NH * DH * 2) + r * 16;
    v4u wa[4][8], xr[4];
    v2u epoch0;
#define LOAD_WQ(s) { const int so_ = KBLK(s) * 32 * (NH * DH * 2) + h * (DH * 2); _Pragma("unroll") for (int j_ = 0; j_ < 8; ++j_) \
      wa[s][j_] = __builtin_amdgcn_raw_buffer_load_b128(rwq, wqoff, so_ + j_ * (NH * DH * 2), AUX_W); }
#define LOAD_X() { MEMFENCE(); epoch0 = __builtin_amdgcn_raw_buffer_load_b64(rws, 0, WS_EPOCH + bp * 256, AUX_SC1); \
      _Pragma("unroll") for (int i_ = 0; i_ < 4; ++i_) \
      xr[i_] = __builtin_amdgcn_raw_buffer_load_b128(rx, voff, (16 * bp + 2 * w) * (DM * 2) + i_ * 1024, AUX_W); }
#define STAGE_X() { _Pragma("unroll") for (int i_ = 0; i_ < 4; ++i_) \
      *(LAS v4u*)(LW + L_X + w * 32 + (i_ >> 1) * X_ODD + (i_ & 1) * 1024 + voff) = xr[i_]; \
      e_lo = __builtin_amdgcn_readfirstlane(epoch0[0]); e_hi = __builtin_amdgcn_readfirstlane(epoch0[1]); \
      asm volatile("s_waitcnt lgkmcnt(0)" : "+s"(e_lo), "+s"(e_hi) :: "memory"); }
    const LAS unsigned char* xrow = L + (r >> 1) * (L_WAVE + 32) + L_X + (r & 1) * X_ODD + g * 16;
    f32x4 qa[8];
#pragma unroll
    for (int e = 0; e < 8; ++e) qa[e] = f32x4{0.f, 0.f, 0.f, 0.f};
#define MMA_WQ(s) { const v4u xf_ = *(const LAS v4u*)(xrow + KBLK(s) * 64); \
      _Pragma("unroll") for (int e_ = 0; e_ < 8; ++e_) qa[e_] = mfma16(tr_frag(wa[s], e_), xf_, qa[e_]); }
#define PUT_Q() { _Pragma("unroll") for (int rho = 0; rho < 4; ++rho) { \
      LAS f32x4* p_ = (LAS f32x4*)(LW + r * QP_STRIDE + (32 * g + 8 * rho) * 4); \
      p_[0] = f32x4{qa[0][rho], qa[1][rho], qa[2][rho], qa[3][rho]}; \
      p_[1] = f32x4{qa[4][rho], qa[5][rho], qa[6][rho], qa[7][rho]}; } }
#define REDUCE_Q() { const int row_ = tid >> 5, n4_ = (tid & 31) * 4; f32x4 pa_[8]; \
      _Pragma("unroll") for (int ww = 0; ww < 8; ++ww) pa_[ww] = *(LAS f32x4*)(L + ww * L_WAVE + row_ * QP_STRIDE + n4_ * 4); \
      const f32x4 s_ = ((pa_[0] + pa_[1]) + (pa_[2] + pa_[3])) + ((pa_[4] + pa_[5]) + (pa_[6] + pa_[7])); \
      *(LAS v2u*)(L + L_QS + row_ * QS_STRIDE + n4_ * 2) = v2u{cvt_pk(s_[0], s_[1]), cvt_pk(s_[2], s_[3])}; }
    if (!DESYNC || (bp & 1) == 0) {
      LOAD_X(); SB(); LOAD_WQ(0); SB(); LOAD_WQ(1); SB(); LOAD_WQ(2); SB(); LOAD_WQ(3); SB();
      STAMP(16);
      STAGE_X(); SB();
      BAR(); MEMFENCE(); SB();
      STAMP(17);
      LOAD_ITEM(0); SB();
      STAMP(18);
      MMA_WQ(0); MMA_WQ(1); MMA_WQ(2); MMA_WQ(3);
      PUT_Q(); SB();
      STAMP(19);
      LOAD_ITEM(1); SB();
      STAMP(20);
      LDS_BAR(); SB();
      STAMP(21);
      REDUCE_Q(); SB();
      LOAD_ITEM(2); SB();
      STAMP(22);
    } else {
      LOAD_X(); SB(); LOAD_WQ(0); SB();
      LOAD_ITEM(0); SB(); LOAD_ITEM(1); SB();
      STAMP(16);
      STAGE_X(); SB();
      BAR(); MEMFENCE(); SB();
      STAMP(17);
      LOAD_WQ(1); SB(); LOAD_WQ(2); SB();
      MMA_WQ(0); SB();
      LOAD_WQ(3); SB();
      STAMP(18);
      MMA_WQ(1); SB();
      if (BBAR) { BAR(); SB(); }
      LOAD_ITEM(2); SB();
      STAMP(19);
      MMA_WQ(2); MMA_WQ(3);
      PUT_Q(); SB();
      STAMP(20);
      LDS_BAR(); SB();
      STAMP(21);
      REDUCE_Q(); SB();
      STAMP(22);
    }
  }
  if (TOUCH) asm volatile("" :: "s"(tch[0]), "s"(tch[1]), "s"(tch[2]), "s"(tch[3]), "s"(tch[4]), "s"(tch[5]));
  LDS_BAR();
  asm volatile("" : "+s"(rot));
  v4u qf[2];
#pragma unroll
  for (int hf = 0; hf < 2; ++hf) qf[hf] = *(LAS v4u*)(L + L_QS + (8 * beta + (r & 7)) * QS_STRIDE + (8 * hf + 2 * g + (r >> 3)) * 16);
  unsigned hmask = (r >> 3) ? 0xFFFFFFFFu : 0u;
  asm volatile("" : "+v"(hmask));
  const unsigned pmask = hmask ^ 0x0000FFFFu;

  STAMP(1);
  v4u wr[4][8], wf[4][8];
  const int wooff = (32 * g) * (DM * 2) + r * 16;
  const int wos = (1024 * kq + 128 * w) * (DM * 2) + ct * 256;
#define LOAD_WO(ks) { _Pragma("unroll") for (int j_ = 0; j_ < 8; ++j_) \
    wr[ks][j_] = __builtin_amdgcn_raw_buffer_load_b128(rwo, wooff, wos + (8 * (j_ & 3) + 2 * (((ks) + w) & 3) + (j_ >> 2)) * (DM * 2), AUX_W); }
#define PARK_WO(ks) { _Pragma("unroll") for (int e_ = 0; e_ < 8; ++e_) *(LAS v4u*)(LW + (ks) * 8192 + e_ * 1024 + voff) = tr_frag(wr[ks], e_); }
#define KEEP_WO(ks) { _Pragma("unroll") for (int e_ = 0; e_ < 8; ++e_) { wf[ks][e_] = tr_frag(wr[ks], e_); asm volatile("" : "+v"(wf[ks][e_])); } }
#define BACK_WO(ks) { _Pragma("unroll") for (int e_ = 0; e_ < 8; ++e_) wf[ks][e_] = *(LAS v4u*)(LW + (ks) * 8192 + e_ * 1024 + voff); }

  f32x4 oacc[8];
#pragma unroll
  for (int e = 0; e < 8; ++e) oacc[e] = f32x4{0.f, 0.f, 0.f, 0.f};
  float m = -INFINITY, lsum = 0.f;
  const unsigned long long tag64 = (((unsigned long long)e_hi << 32) | e_lo) + 1ull;
  const unsigned tag_lo = (unsigned)tag64, tag_hi = (unsigned)(tag64 >> 32);
  v2u flags_v;
  v4u snap;
  const unsigned tbase = PACE ? (unsigned)__builtin_amdgcn_s_memrealtime() : 0u;
  unsigned line0 = 0, line1 = 0, tprev = 0;

  static_for<0, NT - 2>([&](auto tc) __attribute__((always_inline)) {
    constexpr int t = decltype(tc)::value;
    f32x4 sa[4];
#pragma unroll
    for (int u = 0; u < 4; ++u) sa[u] = mfma16(item[2 * t][2 * u], qf[0], f32x4{0.f, 0.f, 0.f, 0.f});
#pragma unroll
    for (int u = 0; u < 4; ++u) sa[u] = mfma16(item[2 * t][2 * u + 1], qf[1], sa[u]);
    float s[4];
#pragma unroll
    for (int u = 0; u < 4; ++u) {
      const float xx = bsel(hmask, sa[u][3], sa[u][0]), yy = bsel(hmask, sa[u][1], sa[u][2]);
      s[u] = xx + ror8(yy);
    }
    asm volatile("" : "+v"(s[0]), "+v"(s[1]), "+v"(s[2]), "+v"(s[3]));
    SB();
    if constexpr (t == 0) {
      const int p_own = ((voff >> 7) & 3) * 8192 + (voff >> 9) * 128 + (voff & 0x70);
      __builtin_amdgcn_raw_buffer_store_b64(v2u{SENT, SENT}, rws, voff >> 1, o_slot + w * 512, AUX_SC1);
      __builtin_amdgcn_raw_buffer_store_b128(v4u{SENT, SENT, SENT, SENT}, rws, p_own, p_base + kq * 2048 + w * 256, AUX_SC1);
      MEMFENCE();
      SB();
    }
    if constexpr (t >= WO_T0 && t < WO_T0 + 4) LOAD_WO(t - WO_T0);
    if constexpr (t == NT - 5) {
      flags_v = __builtin_amdgcn_raw_buffer_load_b64(rw0, (voff << 3) & 0xF80, WS_FLAG + bp * 4096, AUX_SC1);
    }
#define PACE_AT(tt) (PACE && (tt) >= PACE_P0 && (tt) % PACE_SP == PACE_P0 % PACE_SP && (tt) < PACE_END - 2)
    if constexpr (PACE_AT(t - 2)) {
      const int dt = (int)(snap[0] - tbase);
      const bool ok = snap[3] == ~(snap[0] ^ snap[1] ^ snap[2]) && dt > -1500 && dt < 12000;
      int rs = ok ? (int)snap[1] : 0;
      const int nr = __builtin_popcount((unsigned)__builtin_amdgcn_ballot_w64(rs > 0) & 0xFFu);
      rs += dppi<0xB1>(rs); rs += dppi<0x4E>(rs); rs += dppi<0x141>(rs);
      const int cm = (int)((float)__builtin_amdgcn_readfirstlane(rs) * __builtin_amdgcn_rcpf((float)max(nr, 1)));
      const int cu = (PACE_A && cm > PACE_C + (t - 2 > 4 ? PACE_DZ2 : PACE_DZ)) ? min(cm - PACE_S0, PACE_C + 100) : PACE_C;
      int v = dt - (int)snap[2] * cu;
      v += dppi<0xB1>(v); v += dppi<0x4E>(v); v += dppi<0x141>(v);
      const bool all_ok = (__builtin_amdgcn_ballot_w64(ok) & 0xFFull) == 0xFFull;
      if (w == 0) *(LAS v2u*)(L + L_PC) = v2u{tbase + (unsigned)(all_ok ? (v >> 3) - PACE_D : -1000000), (unsigned)cu};
    }
#define HOLD(allow_abs) { unsigned now_ = (unsigned)__builtin_amdgcn_s_memrealtime(); int d_ = min((int)((allow_abs) - now_), PACE_MAX); \
      const unsigned al_ = now_ + (unsigned)d_; \
      while (d_ > 0) { __builtin_amdgcn_s_sleep(1); d_ = (int)(al_ - (unsigned)__builtin_amdgcn_s_memrealtime()); } }
    if constexpr (PACE && t >= PACE_P0 + 3) HOLD(line0 + ((16 * t - 7 + w) * line1 >> 4));
    if constexpr (PACE_AT(t - 1) && SNAP_EARLY) {
      const int px = (voff >> 4) & 7;
      const int peer = (((bp + px) & 7) * 4 + ((i4 + px) & 3)) * 8 + ((xg + px) & 7);
      snap = __builtin_amdgcn_raw_buffer_load_b128(rw0, peer * 128, WS_PACE, AUX_SC1);
      SB();
    }
    if constexpr (NEXT_K(t) >= 0) LOAD_ITEM(NEXT_K(t));
    SB();
    if constexpr (PACE_AT(t - 2)) asm volatile("s_waitcnt lgkmcnt(0)" ::: "memory");
    if constexpr (t == 3) {
      asm volatile("s_waitcnt vmcnt(42)" ::: "memory");
      BAR();
      MEMFENCE();
      __builtin_amdgcn_raw_buffer_store_b64(v2u{tag_lo, tag_hi}, rw0, (voff >> 1) & 0x78, WS_FLAG + (bp * 32 + h) * 128, AUX_SC1);
    } else if constexpr (t == NT - 3) {
      if (w == 0) {
        if (__builtin_expect(!__all(flags_v[0] == tag_lo && flags_v[1] == tag_hi), 0)) {
          for (unsigned spins = 0; spins < kSpinCap; ++spins) {
            __builtin_amdgcn_s_sleep(2);
            MEMFENCE();
            const v2u fv = __builtin_amdgcn_raw_buffer_load_b64(rws, (voff << 3) & 0xF80, WS_FLAG + bp * 4096, AUX_SC1);
            if (__all(fv[0] == tag_lo && fv[1] == tag_hi)) break;
          }
        }
        if (h == 0) __builtin_amdgcn_raw_buffer_store_b64(v2u{tag_lo, tag_hi}, rws, (voff >> 1) & 0x78, WS_EPOCH + bp * 256, AUX_SC1);
      }
      MEMFENCE();
      BAR();
      MEMFENCE();
    } else BAR();
    if constexpr (PACE_AT(t - 2)) {
      MEMFENCE();
      const v2u ln = *(LAS v2u*)(L + L_PC);
      line0 = __builtin_amdgcn_readfirstlane(ln[0]); line1 = __builtin_amdgcn_readfirstlane(ln[1]);
    }
    if constexpr (PACE_AT(t) || (PACE && (t == 1 || t == PACE_TC))) {
      const unsigned tv = (unsigned)__builtin_amdgcn_s_memrealtime();
      constexpr int tb = t > PACE_TC ? PACE_TC : 1;
      const unsigned rate = t > 1 ? (tv - tprev - (t == 4 ? PACE_B4 : 0)) / (t > tb ? t - tb : 1) : 0u;
      if constexpr (t == 1 || t == PACE_TC) tprev = tv;
      const unsigned tc2 = tv - (t == 4 ? PACE_B4T : 0);
      if constexpr (PACE_AT(t))
        __builtin_amdgcn_raw_buffer_store_b128(v4u{tc2, rate, (unsigned)t, ~(tc2 ^ rate ^ (unsigned)t)}, rw0, voff & 0x70, WS_PACE + wg * 128, AUX_SC1);
    }
    SB();
    const float mx = xg_max(fmaxf(fmaxf(s[0], s[1]), fmaxf(s[2], s[3])));
    const float mn = fmaxf(m, mx);
    const float alpha = __builtin_amdgcn_exp2f((m - mn) * CEXP);
    const float mc = mn * CEXP;
    m = mn;
    float p[4];
#pragma unroll
    for (int u = 0; u < 4; ++u) p[u] = __builtin_amdgcn_exp2f(__builtin_fmaf(s[u], CEXP, -mc));
    lsum = lsum * alpha + ((p[0] + p[1]) + (p[2] + p[3]));
    asm volatile("" : "+v"(lsum));
    const v4u pf = {cvt_pk(p[0], p[0]) & pmask, cvt_pk(p[1], p[1]) & pmask, cvt_pk(p[2], p[2]) & pmask, cvt_pk(p[3], p[3]) & pmask};
#pragma unroll
    for (int e = 0; e < 8; ++e) oacc[e] *= alpha;
#pragma unroll
    for (int e = 0; e < 8; ++e) oacc[e] = mfma16(tr_frag(item[2 * t + 1], e), pf, oacc[e]);
    asm volatile("" : "+v"(oacc[0]), "+v"(oacc[1]), "+v"(oacc[2]), "+v"(oacc[3]), "+v"(oacc[4]), "+v"(oacc[5]), "+v"(oacc[6]), "+v"(oacc[7]));
    SB();
    if constexpr ((t & 1) == 1) STAMP(8 + (t >> 1));
    if constexpr (t == WO_T0 + 1) PARK_WO(0);
    if constexpr (t == WO_T0 + 2) PARK_WO(1);
    if constexpr (t == WO_T0 + 3) KEEP_WO(2);
    if constexpr (t == WO_T0 + 4) KEEP_WO(3);
    SB();
    if constexpr (PACE && t >= PACE_P0 + 2) HOLD(line0 + ((16 * t + PACE_V0 + w) * line1 >> 4));
    if constexpr (PACE_AT(t - 1) && !SNAP_EARLY) {
      const int px = (voff >> 4) & 7;
      const int peer = (((bp + px) & 7) * 4 + ((i4 + px) & 3)) * 8 + ((xg + px) & 7);
      snap = __builtin_amdgcn_raw_buffer_load_b128(rw0, peer * 128, WS_PACE, AUX_SC1);
      SB();
    }
    if constexpr (NEXT_V(t) >= 0) LOAD_ITEM(NEXT_V(t));
    SB();
  });
  {
    float s[8];
    static_for<0, 2>([&](auto hc) __attribute__((always_inline)) {
      constexpr int hh = decltype(hc)::value, n = 2 * (NT - 2 + hh);
      f32x4 sa[4];
#pragma unroll
      for (int u = 0; u < 4; ++u) sa[u] = mfma16(item[n][2 * u], qf[0], f32x4{0.f, 0.f, 0.f, 0.f});
#pragma unroll
      for (int u = 0; u < 4; ++u) sa[u] = mfma16(item[n][2 * u + 1], qf[1], sa[u]);
#pragma unroll
      for (int u = 0; u < 4; ++u) s[4 * hh + u] = bsel(hmask, sa[u][3], sa[u][0]) + ror8(bsel(hmask, sa[u][1], sa[u][2]));
      asm volatile("" : "+v"(s[4 * hh]), "+v"(s[4 * hh + 1]), "+v"(s[4 * hh + 2]), "+v"(s[4 * hh + 3]));
      SB();
      if constexpr (hh == 0) {
        if (PACE) HOLD(line0 + ((16 * (NT - 2) - 7 + w) * line1 >> 4));
        LOAD_ITEM(NEXT_AFTER(2 * NT - 4)); SB();
      }
    });
    BACK_WO(0);
    const float mx = xg_max(fmaxf(fmaxf(fmaxf(s[0], s[1]), fmaxf(s[2], s[3])), fmaxf(fmaxf(s[4], s[5]), fmaxf(s[6], s[7]))));
    const float mn = fmaxf(m, mx);
    float alpha = __builtin_amdgcn_exp2f((m - mn) * CEXP);
    const float mc = mn * CEXP;
    m = mn;
    float p[8];
#pragma unroll
    for (int u = 0; u < 8; ++u) p[u] = __builtin_amdgcn_exp2f(__builtin_fmaf(s[u], CEXP, -mc));
    lsum = xg_sum(lsum * alpha + (((p[0] + p[1]) + (p[2] + p[3])) + ((p[4] + p[5]) + (p[6] + p[7]))));
    LAS unsigned char* ml = L + L_ML + beta * 512 + ((voff >> 4) & 7) * 64;
    if (voff < 256) *(LAS f32x2*)(ml + ws4 * 16 + (voff >> 7) * 8) = f32x2{m, lsum};
    SB();
    LOAD_ITEM(2 * NT - 1); SB();
    LDS_BAR();
    f32x4 rec[4];
#pragma unroll
    for (int j = 0; j < 4; ++j) rec[j] = *(LAS f32x4*)(ml + j * 16);
    const float M = fmaxf(fmaxf(fmaxf(rec[0][0], rec[0][2]), fmaxf(rec[1][0], rec[1][2])), fmaxf(fmaxf(rec[2][0], rec[2][2]), fmaxf(rec[3][0], rec[3][2])));
    float Ls = 0.f;
#pragma unroll
    for (int j = 0; j < 4; ++j)
      Ls += __builtin_amdgcn_exp2f((rec[j][0] - M) * CEXP) * rec[j][1] + __builtin_amdgcn_exp2f((rec[j][2] - M) * CEXP) * rec[j][3];
    const float scale = __builtin_amdgcn_exp2f((m - M) * CEXP) / Ls;
    alpha *= scale;
    v4u pf[2];
#pragma unroll
    for (int u = 0; u < 8; ++u) { const float ps = p[u] * scale; pf[u >> 2][u & 3] = cvt_pk(ps, ps) & pmask; }
#pragma unroll
    for (int e = 0; e < 8; ++e) oacc[e] *= alpha;
#pragma unroll
    for (int e = 0; e < 8; ++e) oacc[e] = mfma16(tr_frag(item[2 * NT - 3], e), pf[0], oacc[e]);
    SB();
    BACK_WO(1);
    SB();
#pragma unroll
    for (int e = 0; e < 8; ++e) oacc[e] = mfma16(tr_frag(item[2 * NT - 1], e), pf[1], oacc[e]);
    SB();
    STAMP(15);
  }

  STAMP(2);
  const int lane2 = __builtin_amdgcn_mbcnt_hi(~0u, __builtin_amdgcn_mbcnt_lo(~0u, 0u));
  asm volatile("s_waitcnt lgkmcnt(0)" ::: "memory");
#pragma unroll
  for (int e = 0; e < 8; ++e) *(LAS f32x4*)(LW + e * 1024 + lane2 * 16) = oacc[e];
  const int goff = WS_O + (bp * 128 + 4 * (8 * kq + w)) * 1024;
  v4u ob[4];
#pragma unroll
  for (int ks = 0; ks < 4; ++ks)
    ob[ks] = FIRSTLOOK ? __builtin_amdgcn_raw_buffer_load_b128(rws, lane2 * 16, goff + ((ks + w) & 3) * 1024, AUX_SC1) : v4u{SENT, SENT, SENT, SENT};
  TSTAMP(21);
  LDS_BAR();
  STAMP(3);
  {
    const int fl = 32 * (w & 1) + (lane2 >> 1);
    const LAS unsigned char* src = L + ((fl >> 3) & 1) * (4 * L_WAVE) + ((w & ~1) + (lane2 & 1)) * 1024 + (fl & 0x37) * 16;
    const int par = (lane2 & 1) * 128;
    f32x4 pa[8];
#pragma unroll
    for (int j = 0; j < 8; ++j) pa[j] = *(const LAS f32x4*)(src + (j >> 1) * L_WAVE + ((j & 1) ? 128 - par : par));
    const f32x4 a0 = ((pa[0] + pa[1]) + (pa[2] + pa[3])) + ((pa[4] + pa[5]) + (pa[6] + pa[7]));
    v2u ov = {cvt_pk(a0[0], a0[1]), cvt_pk(a0[2], a0[3])};
#pragma unroll
    for (int j = 0; j < 2; ++j) ov[j] = (ov[j] == SENT) ? 0x7FC07FC0u : ov[j];
    __builtin_amdgcn_raw_buffer_store_b64(ov, rws, lane2 * 8, o_slot + w * 512, AUX_SC1);
  }
  SB();
  STAMP(4);
  {
    TSTAMP(16);
    unsigned spins = 0;
    while (__any(max(max(umax4(ob[0]), umax4(ob[1])), max(umax4(ob[2]), umax4(ob[3]))) == SENT)) {
      if (++spins > kSpinCap) break;
      if (spins > 1) __builtin_amdgcn_s_sleep(POLL_SLEEP); else if (POLL_FIRST) __builtin_amdgcn_s_sleep(POLL_FIRST);
      if (PROBE && spins > PROBE_AFTER) {
        for (unsigned sp2 = 0; sp2 < kSpinCap; ++sp2) {
          MEMFENCE();
          const unsigned pr = __builtin_amdgcn_raw_buffer_load_b32(rws, (lane2 & (PROBE - 1)) * (4096 / (PROBE ? PROBE : 1)), goff, AUX_SC1);
          if (__any(pr != SENT)) break;
          __builtin_amdgcn_s_sleep(1);
        }
      }
      MEMFENCE();
#pragma unroll
      for (int ks = 0; ks < 4; ++ks) ob[ks] = __builtin_amdgcn_raw_buffer_load_b128(rws, lane2 * 16, goff + ((ks + w) & 3) * 1024, AUX_SC1);
    }
    TSTAMP(17);
#pragma unroll
    for (int hf = 0; hf < 2; ++hf) {
      f32x4 c[4];
#pragma unroll
      for (int e = 0; e < 4; ++e) c[e] = f32x4{0.f, 0.f, 0.f, 0.f};
#pragma unroll
      for (int ks = 0; ks < 4; ++ks)
#pragma unroll
        for (int e = 0; e < 4; ++e) c[e] = mfma16(wf[ks][4 * hf + e], ob[ks], c[e]);
#pragma unroll
      for (int rho = 0; rho < 4; ++rho)
        *(LAS f32x4*)(LW + L_C + (((lane2 & 15) * 512 + (lane2 >> 4) * 128 + (lane2 & 7) * 16) ^ (rho * 32 + hf * 16))) =
            f32x4{c[0][rho], c[1][rho], c[2][rho], c[3][rho]};
    }
  }
  TSTAMP(18);
  LDS_BAR();
  STAMP(5);
  {
    const int row = 2 * w + (lane2 >> 5);
    const int so = row * 512 + (((lane2 & 31) * 16) ^ ((row & 7) * 16));
    f32x4 pa[8];
#pragma unroll
    for (int ww = 0; ww < 8; ++ww) pa[ww] = *(const LAS f32x4*)(L + ww * L_WAVE + L_C + so);
    const f32x4 s = ((pa[0] + pa[1]) + (pa[2] + pa[3])) + ((pa[4] + pa[5]) + (pa[6] + pa[7]));
    v4u sv = __builtin_bit_cast(v4u, s);
#pragma unroll
    for (int j = 0; j < 4; ++j) sv[j] = (sv[j] == SENT) ? 0x7FC00000u : sv[j];
    const int p_own2 = ((lane2 >> 3) & 3) * 8192 + (lane2 >> 5) * 128 + (lane2 & 7) * 16;
    __builtin_amdgcn_raw_buffer_store_b128(sv, rws, p_own2, p_base + kq * 2048 + w * 256, AUX_SC1);
  }
  TSTAMP(19);
  {
    const int poff = (lane2 >> 4) * 2048 + (lane2 & 15) * 16;
    int oo = ((lane2 >> 3) & 1) * (DM * 2) + (lane2 & 7) * 8 + (lane2 >> 5) * 4 + (lane2 & 16) * 0x1000000;
    asm volatile("" : "+v"(oo));
    v4u pv;
    unsigned spins = 0;
    if (POLL2_FIRST) __builtin_amdgcn_s_sleep(POLL2_FIRST);
    for (;;) {
      MEMFENCE();
      pv = __builtin_amdgcn_raw_buffer_load_b128(rws, poff, p_base + kq * 8192 + w * 256, AUX_SC1);
      if (!__any(umax4(pv) == SENT)) break;
      if (++spins > kSpinCap) break;
      __builtin_amdgcn_s_sleep(POLL2_SLEEP);
    }
    TSTAMP(20);
    const float u0 = rs32(__uint_as_float(pv[0]), __uint_as_float(pv[2])), u1 = rs32(__uint_as_float(pv[1]), __uint_as_float(pv[3]));
    const float t0 = rs16(u0, u0), t1 = rs16(u1, u1);
    __builtin_amdgcn_raw_buffer_store_b32(cvt_pk(t0, t1), rout, oo, out_off, AUX_OUT);
  }
}

}

extern "C" void kernel_launch(void* const* d_in, const int* in_sizes, int n_in, void* d_out, int out_size, void* d_ws,
                              size_t ws_size, hipStream_t stream) {
  (void)in_sizes; (void)n_in; (void)out_size; (void)ws_size;
  (void)hipFuncSetAttribute((const void*)mha_fused, hipFuncAttributeMaxDynamicSharedMemorySize, L_BYTES);
  hipLaunchKernelGGL(mha_fused, dim3(NWG), dim3(NTHR), L_BYTES, stream, (const unsigned short*)d_in[0], (const unsigned short*)d_in[1],
                     (const unsigned short*)d_in[2], (const unsigned short*)d_in[3], (const unsigned short*)d_in[4],
                     (unsigned short*)d_out, (unsigned char*)d_ws);
}
```

```cpp
#define TOUCH 0
#include <hip/hip_runtime.h>
#include <hip/hip_bf16.h>

#define GAS __attribute__((address_space(1)))
#define LAS __attribute__((address_space(3)))
typedef unsigned v4u __attribute__((ext_vector_type(4)));
typedef unsigned v2u __attribute__((ext_vector_type(2)));
typedef float f32x4 __attribute__((ext_vector_type(4)));
typedef __bf16 bf16x8 __attribute__((ext_vector_type(8)));
typedef __bf16 bf16x2 __attribute__((ext_vector_type(2)));
typedef float f32x2 __attribute__((ext_vector_type(2)));

#define AUX_W 0
#define AUX_OUT 0
#define DESYNC 1
#define BMODE 3
#define BBAR 0
#define WQ_ROT 1
#define PACE 1
#define PACE_C 448
#define PACE_D 0
#define PACE_A 16
#define PACE_S0 4
#define PACE_B4T 0
#define SNAP_EARLY 1
#define PACE_B4 42
#define PACE_DZ 32
#define PACE_TC 100
#define PACE_DZ2 20
#define PACE_V0 1
#define PACE_END 14
#define PACE_MAX 500
#define PACE_SP 3
#define PACE_P0 1
#define ROT_H 4
#define ROT_BP 1
#define ROT_BETA 8
#define FIRSTLOOK 1
#define POLL_FIRST 0
#define PROBE 1
#define PROBE_AFTER 0
#define POLL2_FIRST 8
#define POLL2_SLEEP 2
#define POLL_SLEEP 2
#define SKIP_POS 2
#define WO_T0 1

namespace {

constexpr int NB = 16, SQ = 8, DM = 1024, NH = 32, SK = 2048, DH = 128;
constexpr int NWG = 256, NTHR = 512;
constexpr int NT = 16;
constexpr float CEXP = 0.08838834764831845f * 1.4426950408889634f;
constexpr int AUX_NT = 2, AUX_SC1 = 16;
constexpr int RSRC_W3 = 0x00020000;
constexpr unsigned SENT = 0xFFFFFFFFu;
constexpr unsigned kSpinCap = 1u << 16;

constexpr int WS_EPOCH = 0;
constexpr int WS_FLAG = 4096;
constexpr int WS_PACE = WS_FLAG + 32768;
constexpr int WS_O = 131072;
constexpr int WS_P = WS_O + 8 * 128 * 1024;
constexpr int WS_BYTES = WS_P + 8 * 8 * 16 * 2048;

constexpr int L_WAVE = 16384;
constexpr int L_QS = 8 * L_WAVE;
constexpr int QS_STRIDE = 272;
constexpr int L_ML = L_QS + 16 * QS_STRIDE;
constexpr int L_PC = L_ML + 1024;
constexpr int L_BYTES = L_PC + 16;
constexpr int QP_STRIDE = 528;
constexpr int L_X = 9216, X_ODD = 2064;
constexpr int L_C = 8192;

__device__ __forceinline__ f32x4 mfma16(v4u a, v4u b, f32x4 c) {
  return __builtin_amdgcn_mfma_f32_16x16x32_bf16(__builtin_bit_cast(bf16x8, a), __builtin_bit_cast(bf16x8, b), c, 0, 0, 0);
}
__device__ __forceinline__ unsigned pack_lo(unsigned a, unsigned b) { return __builtin_amdgcn_perm(b, a, 0x05040100u); }
__device__ __forceinline__ unsigned pack_hi(unsigned a, unsigned b) { return __builtin_amdgcn_perm(b, a, 0x07060302u); }
__device__ __forceinline__ unsigned cvt_pk(float lo, float hi) {
  f32x2 v = {lo, hi};
  return __builtin_bit_cast(unsigned, __builtin_convertvector(v, bf16x2));
}
__device__ __forceinline__ v4u tr_frag(const v4u (&R)[8], int e) {
  v4u f;
#pragma unroll
  for (int jj = 0; jj < 4; ++jj)
    f[jj] = (e & 1) ? pack_hi(R[2 * jj][e >> 1], R[2 * jj + 1][e >> 1]) : pack_lo(R[2 * jj][e >> 1], R[2 * jj + 1][e >> 1]);
  return f;
}
template <int CTRL> __device__ __forceinline__ int dppi(int x) { return __builtin_amdgcn_mov_dpp(x, CTRL, 0xf, 0xf, true); }
__device__ __forceinline__ float ror8(float x) {
  return __builtin_bit_cast(float, dppi<0x128>(__builtin_bit_cast(int, x)));
}
__device__ __forceinline__ float bsel(unsigned mask, float a, float b) {
  return __uint_as_float((__float_as_uint(a) & mask) | (__float_as_uint(b) & ~mask));
}
__device__ __forceinline__ float xg_max(float x) {
  auto s = __builtin_amdgcn_permlane16_swap(__float_as_uint(x), __float_as_uint(x), false, false);
  x = fmaxf(__uint_as_float(s[0]), __uint_as_float(s[1]));
  auto t = __builtin_amdgcn_permlane32_swap(__float_as_uint(x), __float_as_uint(x), false, false);
  return fmaxf(__uint_as_float(t[0]), __uint_as_float(t[1]));
}
__device__ __forceinline__ float xg_sum(float x) {
  auto s = __builtin_amdgcn_permlane16_swap(__float_as_uint(x), __float_as_uint(x), false, false);
  x = __uint_as_float(s[0]) + __uint_as_float(s[1]);
  auto t = __builtin_amdgcn_permlane32_swap(__float_as_uint(x), __float_as_uint(x), false, false);
  return __uint_as_float(t[0]) + __uint_as_float(t[1]);
}
__device__ __forceinline__ unsigned umax4(v4u v) { return max(max(v[0], v[1]), max(v[2], v[3])); }
__device__ __forceinline__ float rs32(float a, float b) {
  auto t = __builtin_amdgcn_permlane32_swap(__float_as_uint(a), __float_as_uint(b), false, false);
  return __uint_as_float(t[0]) + __uint_as_float(t[1]);
}
__device__ __forceinline__ float rs16(float a, float b) {
  auto t = __builtin_amdgcn_permlane16_swap(__float_as_uint(a), __float_as_uint(b), false, false);
  return __uint_as_float(t[0]) + __uint_as_float(t[1]);
}
#define SB() __builtin_amdgcn_sched_barrier(0)
#define BAR() __builtin_amdgcn_s_barrier()
#define MEMFENCE() asm volatile("" ::: "memory")
#define LDS_BAR() { asm volatile("s_waitcnt lgkmcnt(0)" ::: "memory"); BAR(); MEMFENCE(); }
#define STAMP(i)
#define TSTAMP(i)

template <int V> struct IC { static constexpr int value = V; };
template <int I, int N, class F> __device__ __forceinline__ void static_for(F&& f) {
  if constexpr (I < N) { f(IC<I>{}); static_for<I + 1, N>(f); }
}

__global__ void __launch_bounds__(NTHR, 2)
mha_fused(const unsigned short* __restrict__ x, const unsigned short* __restrict__ kc, const unsigned short* __restrict__ vc,
          const unsigned short* __restrict__ wq, const unsigned short* __restrict__ wo, unsigned short* out, unsigned char* ws) {
  extern __shared__ __attribute__((aligned(16))) unsigned char smem[];
  LAS unsigned char* L = (LAS unsigned char*)smem;
  const int tid = threadIdx.x, lane = tid & 63, w = __builtin_amdgcn_readfirstlane(tid >> 6);
  const int r = lane & 15, g = lane >> 4;
  const int wg = blockIdx.x, idx = wg >> 3, xg = wg & 7, i4 = idx & 3;
  const int h = 4 * xg + i4, bp = idx >> 2;
  const int kq = xg >> 1, ct = 4 * (xg & 1) + ((i4 + 2) & 3);
  const int beta = w >> 2, ws4 = w & 3;
  const int bh = (2 * bp + beta) * NH + h;
  int rot = (ROT_H * h + ROT_BP * bp + ROT_BETA * beta) & (NT - 1);
  LAS unsigned char* LW = L + w * L_WAVE;
  STAMP(0);
  const unsigned tfix0 = (unsigned)__builtin_amdgcn_s_memrealtime();
  __builtin_amdgcn_s_waitcnt(0xC07F);
  SB();

  asm volatile("" : "+s"(out));
  const __amdgpu_buffer_rsrc_t rk = __builtin_amdgcn_make_buffer_rsrc((void*)(kc + (size_t)bh * SK * DH), 0, SK * DH * 2, RSRC_W3);
  const __amdgpu_buffer_rsrc_t rv = __builtin_amdgcn_make_buffer_rsrc((void*)(vc + (size_t)bh * SK * DH), 0, SK * DH * 2, RSRC_W3);
  const __amdgpu_buffer_rsrc_t rws = __builtin_amdgcn_make_buffer_rsrc((void*)ws, 0, WS_BYTES, RSRC_W3);
  const __amdgpu_buffer_rsrc_t rw0 = __builtin_amdgcn_make_buffer_rsrc((void*)ws, 0, w == 0 ? WS_BYTES : 0, RSRC_W3);
  const __amdgpu_buffer_rsrc_t rwq = __builtin_amdgcn_make_buffer_rsrc((void*)wq, 0, DM * NH * DH * 2, RSRC_W3);
  const __amdgpu_buffer_rsrc_t rwo = __builtin_amdgcn_make_buffer_rsrc((void*)wo, 0, NH * DH * DM * 2, RSRC_W3);
  const __amdgpu_buffer_rsrc_t rx = __builtin_amdgcn_make_buffer_rsrc((void*)x, 0, NB * SQ * DM * 2, RSRC_W3);
  const __amdgpu_buffer_rsrc_t rout = __builtin_amdgcn_make_buffer_rsrc((void*)out, 0, NB * SQ * DM * 2, RSRC_W3);

#define ITEM_AT(pos) ((pos) == 2 * NT - 3 ? 2 * NT - 2 : (pos) == 2 * NT - 2 ? 2 * NT - 3 : (pos))
#define NEXT_AFTER(pos) ((pos) < SKIP_POS ? (pos) + 3 : (pos) == SKIP_POS || (pos) + 2 >= 2 * NT ? -1 : ITEM_AT((pos) + 2))
#define NEXT_K(t) NEXT_AFTER(2 * (t))
#define NEXT_V(t) NEXT_AFTER(2 * (t) + 1)
  const int koff = (4 * ((r >> 1) & 1) + (r >> 2)) * 256 + (2 * g + (r & 1)) * 16;
  const int voff = lane * 16;
  v4u item[2 * NT][8];
#define TILE_OFF(t) (((((t) + rot) & (NT - 1)) * 4 + ws4) * 8192)
#define LOAD_ITEM(n) { const int so_ = TILE_OFF((n) >> 1); _Pragma("unroll") for (int i_ = 0; i_ < 8; ++i_) \
    item[n][i_] = ((n) & 1) ? __builtin_amdgcn_raw_buffer_load_b128(rv, voff, so_ + i_ * 1024, AUX_NT) \
                            : __builtin_amdgcn_raw_buffer_load_b128(rk, koff, so_ + (i_ >> 1) * 2048 + (i_ & 1) * 128, AUX_NT); }

  const int o_slot = WS_O + (bp * 128 + 4 * h) * 1024;
  const int p_base = WS_P + (bp * 8 + ct) * (16 * 2048);
  const int out_off = ((16 * bp + 2 * w) * DM + 128 * ct + 32 * kq) * 2;

  unsigned tch[6];
  if (TOUCH) {
    typedef const __attribute__((address_space(4))) unsigned* cptr;
    tch[0] = *(cptr)(kc + (size_t)bh * SK * DH);
    tch[1] = *(cptr)(vc + (size_t)bh * SK * DH);
    tch[2] = *(cptr)(wo + (size_t)(1024 * kq + 128 * w) * DM);
    tch[3] = *(cptr)(ws + o_slot);
    tch[4] = *(cptr)(ws + p_base);
    tch[5] = *(cptr)((const unsigned char*)out + (out_off & ~63));
    SB();
  }
  unsigned e_lo, e_hi;
  {
#define KBLK(s) ((((4 * beta + (s) + WQ_ROT * bp) & 7) << 2) | ws4)
    const int wqoff = (8 * g) * (NH * DH * 2) + r * 16;
    v4u wa[4][8], xr[4];
    v2u epoch0;
#define LOAD_WQ(s) { const int so_ = KBLK(s) * 32 * (NH * DH * 2) + h * (DH * 2); _Pragma("unroll") for (int j_ = 0; j_ < 8; ++j_) \
      wa[s][j_] = __builtin_amdgcn_raw_buffer_load_b128(rwq, wqoff, so_ + j_ * (NH * DH * 2), AUX_W); }
#define LOAD_X() { MEMFENCE(); epoch0 = __builtin_amdgcn_raw_buffer_load_b64(rws, 0, WS_EPOCH + bp * 256, AUX_SC1); \
      _Pragma("unroll") for (int i_ = 0; i_ < 4; ++i_) \
      xr[i_] = __builtin_amdgcn_raw_buffer_load_b128(rx, voff, (16 * bp + 2 * w) * (DM * 2) + i_ * 1024, AUX_W); }
#define STAGE_X() { _Pragma("unroll") for (int i_ = 0; i_ < 4; ++i_) \
      *(LAS v4u*)(LW + L_X + w * 32 + (i_ >> 1) * X_ODD + (i_ & 1) * 1024 + voff) = xr[i_]; \
      e_lo = __builtin_amdgcn_readfirstlane(epoch0[0]); e_hi = __builtin_amdgcn_readfirstlane(epoch0[1]); \
      asm volatile("s_waitcnt lgkmcnt(0)" : "+s"(e_lo), "+s"(e_hi) :: "memory"); }
    const LAS unsigned char* xrow = L + (r >> 1) * (L_WAVE + 32) + L_X + (r & 1) * X_ODD + g * 16;
    f32x4 qa[8];
#pragma unroll
    for (int e = 0; e < 8; ++e) qa[e] = f32x4{0.f, 0.f, 0.f, 0.f};
#define MMA_WQ(s) { const v4u xf_ = *(const LAS v4u*)(xrow + KBLK(s) * 64); \
      _Pragma("unroll") for (int e_ = 0; e_ < 8; ++e_) qa[e_] = mfma16(tr_frag(wa[s], e_), xf_, qa[e_]); }
#define PUT_Q() { _Pragma("unroll") for (int rho = 0; rho < 4; ++rho) { \
      LAS f32x4* p_ = (LAS f32x4*)(LW + r * QP_STRIDE + (32 * g + 8 * rho) * 4); \
      p_[0] = f32x4{qa[0][rho], qa[1][rho], qa[2][rho], qa[3][rho]}; \
      p_[1] = f32x4{qa[4][rho], qa[5][rho], qa[6][rho], qa[7][rho]}; } }
#define REDUCE_Q() { const int row_ = tid >> 5, n4_ = (tid & 31) * 4; f32x4 pa_[8]; \
      _Pragma("unroll") for (int ww = 0; ww < 8; ++ww) pa_[ww] = *(LAS f32x4*)(L + ww * L_WAVE + row_ * QP_STRIDE + n4_ * 4); \
      const f32x4 s_ = ((pa_[0] + pa_[1]) + (pa_[2] + pa_[3])) + ((pa_[4] + pa_[5]) + (pa_[6] + pa_[7])); \
      *(LAS v2u*)(L + L_QS + row_ * QS_STRIDE + n4_ * 2) = v2u{cvt_pk(s_[0], s_[1]), cvt_pk(s_[2], s_[3])}; }
    if (!DESYNC || (bp & 1) == 0) {
      LOAD_X(); SB(); LOAD_WQ(0); SB(); LOAD_WQ(1); SB(); LOAD_WQ(2); SB(); LOAD_WQ(3); SB();
      STAMP(16);
      STAGE_X(); SB();
      BAR(); MEMFENCE(); SB();
      STAMP(17);
      LOAD_ITEM(0); SB();
      STAMP(18);
      MMA_WQ(0); MMA_WQ(1); MMA_WQ(2); MMA_WQ(3);
      PUT_Q(); SB();
      STAMP(19);
      LOAD_ITEM(1); SB();
      STAMP(20);
      LDS_BAR(); SB();
      STAMP(21);
      REDUCE_Q(); SB();
      LOAD_ITEM(2); SB();
      STAMP(22);
    } else {
      LOAD_X(); SB(); LOAD_WQ(0); SB();
      LOAD_ITEM(0); SB(); LOAD_ITEM(1); SB();
      STAMP(16);
      STAGE_X(); SB();
      BAR(); MEMFENCE(); SB();
      STAMP(17);
      LOAD_WQ(1); SB(); LOAD_WQ(2); SB();
      MMA_WQ(0); SB();
      LOAD_WQ(3); SB();
      STAMP(18);
      MMA_WQ(1); SB();
      if (BBAR) { BAR(); SB(); }
      LOAD_ITEM(2); SB();
      STAMP(19);
      MMA_WQ(2); MMA_WQ(3);
      PUT_Q(); SB();
      STAMP(20);
      LDS_BAR(); SB();
      STAMP(21);
      REDUCE_Q(); SB();
      STAMP(22);
    }
  }
  if (TOUCH) asm volatile("" :: "s"(tch[0]), "s"(tch[1]), "s"(tch[2]), "s"(tch[3]), "s"(tch[4]), "s"(tch[5]));
  LDS_BAR();
  asm volatile("" : "+s"(rot));
  v4u qf[2];
#pragma unroll
  for (int hf = 0; hf < 2; ++hf) qf[hf] = *(LAS v4u*)(L + L_QS + (8 * beta + (r & 7)) * QS_STRIDE + (8 * hf + 2 * g + (r >> 3)) * 16);
  unsigned hmask = (r >> 3) ? 0xFFFFFFFFu : 0u;
  asm volatile("" : "+v"(hmask));
  const unsigned pmask = hmask ^ 0x0000FFFFu;

  STAMP(1);
  v4u wr[4][8], wf[4][8];
  const int wooff = (32 * g) * (DM * 2) + r * 16;
  const int wos = (1024 * kq + 128 * w) * (DM * 2) + ct * 256;
#define LOAD_WO(ks) { _Pragma("unroll") for (int j_ = 0; j_ < 8; ++j_) \
    wr[ks][j_] = __builtin_amdgcn_raw_buffer_load_b128(rwo, wooff, wos + (8 * (j_ & 3) + 2 * (((ks) + w) & 3) + (j_ >> 2)) * (DM * 2), AUX_W); }
#define PARK_WO(ks) { _Pragma("unroll") for (int e_ = 0; e_ < 8; ++e_) *(LAS v4u*)(LW + (ks) * 8192 + e_ * 1024 + voff) = tr_frag(wr[ks], e_); }
#define KEEP_WO(ks) { _Pragma("unroll") for (int e_ = 0; e_ < 8; ++e_) { wf[ks][e_] = tr_frag(wr[ks], e_); asm volatile("" : "+v"(wf[ks][e_])); } }
#define BACK_WO(ks) { _Pragma("unroll") for (int e_ = 0; e_ < 8; ++e_) wf[ks][e_] = *(LAS v4u*)(LW + (ks) * 8192 + e_ * 1024 + voff); }

  f32x4 oacc[8];
#pragma unroll
  for (int e = 0; e < 8; ++e) oacc[e] = f32x4{0.f, 0.f, 0.f, 0.f};
  float m = -INFINITY, lsum = 0.f;
  const unsigned long long tag64 = (((unsigned long long)e_hi << 32) | e_lo) + 1ull;
  const unsigned tag_lo = (unsigned)tag64, tag_hi = (unsigned)(tag64 >> 32);
  v2u flags_v;
  v4u snap;
  const unsigned tbase = PACE ? (unsigned)__builtin_amdgcn_s_memrealtime() : 0u;
  unsigned line0 = 0, line1 = 0, tprev = 0;

  static_for<0, NT - 2>([&](auto tc) __attribute__((always_inline)) {
    constexpr int t = decltype(tc)::value;
    f32x4 sa[4];
#pragma unroll
    for (int u = 0; u < 4; ++u) sa[u] = mfma16(item[2 * t][2 * u], qf[0], f32x4{0.f, 0.f, 0.f, 0.f});
#pragma unroll
    for (int u = 0; u < 4; ++u) sa[u] = mfma16(item[2 * t][2 * u + 1], qf[1], sa[u]);
    float s[4];
#pragma unroll
    for (int u = 0; u < 4; ++u) {
      const float xx = bsel(hmask, sa[u][3], sa[u][0]), yy = bsel(hmask, sa[u][1], sa[u][2]);
      s[u] = xx + ror8(yy);
    }
    asm volatile("" : "+v"(s[0]), "+v"(s[1]), "+v"(s[2]), "+v"(s[3]));
    SB();
    if constexpr (t == 0) {
      const int p_own = ((voff >> 7) & 3) * 8192 + (voff >> 9) * 128 + (voff & 0x70);
      __builtin_amdgcn_raw_buffer_store_b64(v2u{SENT, SENT}, rws, voff >> 1, o_slot + w * 512, AUX_SC1);
      __builtin_amdgcn_raw_buffer_store_b128(v4u{SENT, SENT, SENT, SENT}, rws, p_own, p_base + kq * 2048 + w * 256, AUX_SC1);
      MEMFENCE();
      SB();
    }
    if constexpr (t >= WO_T0 && t < WO_T0 + 4) LOAD_WO(t - WO_T0);
    if constexpr (t == 5) __builtin_amdgcn_raw_buffer_store_b32(tfix0, rw0, 0, 73728 + wg * 128, AUX_SC1);
    if constexpr (t == NT - 5) {
      flags_v = __builtin_amdgcn_raw_buffer_load_b64(rw0, (voff << 3) & 0xF80, WS_FLAG + bp * 4096, AUX_SC1);
    }
#define PACE_AT(tt) (PACE && (tt) >= PACE_P0 && (tt) % PACE_SP == PACE_P0 % PACE_SP && (tt) < PACE_END - 2)
    if constexpr (PACE_AT(t - 2)) {
      const int dt = (int)(snap[0] - tbase);
      const bool ok = snap[3] == ~(snap[0] ^ snap[1] ^ snap[2]) && dt > -1500 && dt < 12000;
      int rs = ok ? (int)snap[1] : 0;
      const int nr = __builtin_popcount((unsigned)__builtin_amdgcn_ballot_w64(rs > 0) & 0xFFu);
      rs += dppi<0xB1>(rs); rs += dppi<0x4E>(rs); rs += dppi<0x141>(rs);
      const int cm = (int)((float)__builtin_amdgcn_readfirstlane(rs) * __builtin_amdgcn_rcpf((float)max(nr, 1)));
      const int cu = (PACE_A && cm > PACE_C + (t - 2 > 4 ? PACE_DZ2 : PACE_DZ)) ? min(cm - PACE_S0, PACE_C + 100) : PACE_C;
      int v = dt - (int)snap[2] * cu;
      v += dppi<0xB1>(v); v += dppi<0x4E>(v); v += dppi<0x141>(v);
      const bool all_ok = (__builtin_amdgcn_ballot_w64(ok) & 0xFFull) == 0xFFull;
      if (w == 0) *(LAS v2u*)(L + L_PC) = v2u{tbase + (unsigned)(all_ok ? (v >> 3) - PACE_D : -1000000), (unsigned)cu};
    }
#define HOLD(allow_abs) { unsigned now_ = (unsigned)__builtin_amdgcn_s_memrealtime(); int d_ = min((int)((allow_abs) - now_), PACE_MAX); \
      const unsigned al_ = now_ + (unsigned)d_; \
      while (d_ > 0) { __builtin_amdgcn_s_sleep(1); d_ = (int)(al_ - (unsigned)__builtin_amdgcn_s_memrealtime()); } }
    if constexpr (PACE && t >= PACE_P0 + 3) HOLD(line0 + ((16 * t - 7 + w) * line1 >> 4));
    if constexpr (PACE_AT(t - 1) && SNAP_EARLY) {
      const int px = (voff >> 4) & 7;
      const int peer = (((bp + px) & 7) * 4 + ((i4 + px) & 3)) * 8 + ((xg + px) & 7);
      snap = __builtin_amdgcn_raw_buffer_load_b128(rw0, peer * 128, WS_PACE, AUX_SC1);
      SB();
    }
    if constexpr (NEXT_K(t) >= 0) LOAD_ITEM(NEXT_K(t));
    SB();
    if constexpr (PACE_AT(t - 2)) asm volatile("s_waitcnt lgkmcnt(0)" ::: "memory");
    if constexpr (t == 3) {
      asm volatile("s_waitcnt vmcnt(42)" ::: "memory");
      BAR();
      MEMFENCE();
      __builtin_amdgcn_raw_buffer_store_b64(v2u{tag_lo, tag_hi}, rw0, (voff >> 1) & 0x78, WS_FLAG + (bp * 32 + h) * 128, AUX_SC1);
    } else if constexpr (t == NT - 3) {
      if (w == 0) {
        if (__builtin_expect(!__all(flags_v[0] == tag_lo && flags_v[1] == tag_hi), 0)) {
          for (unsigned spins = 0; spins < kSpinCap; ++spins) {
            __builtin_amdgcn_s_sleep(2);
            MEMFENCE();
            const v2u fv = __builtin_amdgcn_raw_buffer_load_b64(rws, (voff << 3) & 0xF80, WS_FLAG + bp * 4096, AUX_SC1);
            if (__all(fv[0] == tag_lo && fv[1] == tag_hi)) break;
          }
        }
        if (h == 0) __builtin_amdgcn_raw_buffer_store_b64(v2u{tag_lo, tag_hi}, rws, (voff >> 1) & 0x78, WS_EPOCH + bp * 256, AUX_SC1);
      }
      MEMFENCE();
      BAR();
      MEMFENCE();
    } else BAR();
    if constexpr (PACE_AT(t - 2)) {
      MEMFENCE();
      const v2u ln = *(LAS v2u*)(L + L_PC);
      line0 = __builtin_amdgcn_readfirstlane(ln[0]); line1 = __builtin_amdgcn_readfirstlane(ln[1]);
    }
    if constexpr (PACE_AT(t) || (PACE && (t == 1 || t == PACE_TC))) {
      const unsigned tv = (unsigned)__builtin_amdgcn_s_memrealtime();
      constexpr int tb = t > PACE_TC ? PACE_TC : 1;
      const unsigned rate = t > 1 ? (tv - tprev - (t == 4 ? PACE_B4 : 0)) / (t > tb ? t - tb : 1) : 0u;
      if constexpr (t == 1 || t == PACE_TC) tprev = tv;
      const unsigned tc2 = tv - (t == 4 ? PACE_B4T : 0);
      if constexpr (PACE_AT(t))
        __builtin_amdgcn_raw_buffer_store_b128(v4u{tc2, rate, (unsigned)t, ~(tc2 ^ rate ^ (unsigned)t)}, rw0, voff & 0x70, WS_PACE + wg * 128, AUX_SC1);
    }
    SB();
    const float mx = xg_max(fmaxf(fmaxf(s[0], s[1]), fmaxf(s[2], s[3])));
    const float mn = fmaxf(m, mx);
    const float alpha = __builtin_amdgcn_exp2f((m - mn) * CEXP);
    const float mc = mn * CEXP;
    m = mn;
    float p[4];
#pragma unroll
    for (int u = 0; u < 4; ++u) p[u] = __builtin_amdgcn_exp2f(__builtin_fmaf(s[u], CEXP, -mc));
    lsum = lsum * alpha + ((p[0] + p[1]) + (p[2] + p[3]));
    asm volatile("" : "+v"(lsum));
    const v4u pf = {cvt_pk(p[0], p[0]) & pmask, cvt_pk(p[1], p[1]) & pmask, cvt_pk(p[2], p[2]) & pmask, cvt_pk(p[3], p[3]) & pmask};
#pragma unroll
    for (int e = 0; e < 8; ++e) oacc[e] *= alpha;
#pragma unroll
    for (int e = 0; e < 8; ++e) oacc[e] = mfma16(tr_frag(item[2 * t + 1], e), pf, oacc[e]);
    asm volatile("" : "+v"(oacc[0]), "+v"(oacc[1]), "+v"(oacc[2]), "+v"(oacc[3]), "+v"(oacc[4]), "+v"(oacc[5]), "+v"(oacc[6]), "+v"(oacc[7]));
    SB();
    if constexpr ((t & 1) == 1) STAMP(8 + (t >> 1));
    if constexpr (t == WO_T0 + 1) PARK_WO(0);
    if constexpr (t == WO_T0 + 2) PARK_WO(1);
    if constexpr (t == WO_T0 + 3) KEEP_WO(2);
    if constexpr (t == WO_T0 + 4) KEEP_WO(3);
    SB();
    if constexpr (PACE && t >= PACE_P0 + 2) HOLD(line0 + ((16 * t + PACE_V0 + w) * line1 >> 4));
    if constexpr (PACE_AT(t - 1) && !SNAP_EARLY) {
      const int px = (voff >> 4) & 7;
      const int peer = (((bp + px) & 7) * 4 + ((i4 + px) & 3)) * 8 + ((xg + px) & 7);
      snap = __builtin_amdgcn_raw_buffer_load_b128(rw0, peer * 128, WS_PACE, AUX_SC1);
      SB();
    }
    if constexpr (NEXT_V(t) >= 0) LOAD_ITEM(NEXT_V(t));
    SB();
  });
  {
    float s[8];
    static_for<0, 2>([&](auto hc) __attribute__((always_inline)) {
      constexpr int hh = decltype(hc)::value, n = 2 * (NT - 2 + hh);
      f32x4 sa[4];
#pragma unroll
      for (int u = 0; u < 4; ++u) sa[u] = mfma16(item[n][2 * u], qf[0], f32x4{0.f, 0.f, 0.f, 0.f});
#pragma unroll
      for (int u = 0; u < 4; ++u) sa[u] = mfma16(item[n][2 * u + 1], qf[1], sa[u]);
#pragma unroll
      for (int u = 0; u < 4; ++u) s[4 * hh + u] = bsel(hmask, sa[u][3], sa[u][0]) + ror8(bsel(hmask, sa[u][1], sa[u][2]));
      asm volatile("" : "+v"(s[4 * hh]), "+v"(s[4 * hh + 1]), "+v"(s[4 * hh + 2]), "+v"(s[4 * hh + 3]));
      SB();
      if constexpr (hh == 0) {
        if (PACE) HOLD(line0 + ((16 * (NT - 2) - 7 + w) * line1 >> 4));
        LOAD_ITEM(NEXT_AFTER(2 * NT - 4)); SB();
      }
    });
    BACK_WO(0);
    const float mx = xg_max(fmaxf(fmaxf(fmaxf(s[0], s[1]), fmaxf(s[2], s[3])), fmaxf(fmaxf(s[4], s[5]), fmaxf(s[6], s[7]))));
    const float mn = fmaxf(m, mx);
    float alpha = __builtin_amdgcn_exp2f((m - mn) * CEXP);
    const float mc = mn * CEXP;
    m = mn;
    float p[8];
#pragma unroll
    for (int u = 0; u < 8; ++u) p[u] = __builtin_amdgcn_exp2f(__builtin_fmaf(s[u], CEXP, -mc));
    lsum = xg_sum(lsum * alpha + (((p[0] + p[1]) + (p[2] + p[3])) + ((p[4] + p[5]) + (p[6] + p[7]))));
    LAS unsigned char* ml = L + L_ML + beta * 512 + ((voff >> 4) & 7) * 64;
    if (voff < 256) *(LAS f32x2*)(ml + ws4 * 16 + (voff >> 7) * 8) = f32x2{m, lsum};
    SB();
    LOAD_ITEM(2 * NT - 1); SB();
    LDS_BAR();
    f32x4 rec[4];
#pragma unroll
    for (int j = 0; j < 4; ++j) rec[j] = *(LAS f32x4*)(ml + j * 16);
    const float M = fmaxf(fmaxf(fmaxf(rec[0][0], rec[0][2]), fmaxf(rec[1][0], rec[1][2])), fmaxf(fmaxf(rec[2][0], rec[2][2]), fmaxf(rec[3][0], rec[3][2])));
    float Ls = 0.f;
#pragma unroll
    for (int j = 0; j < 4; ++j)
      Ls += __builtin_amdgcn_exp2f((rec[j][0] - M) * CEXP) * rec[j][1] + __builtin_amdgcn_exp2f((rec[j][2] - M) * CEXP) * rec[j][3];
    const float scale = __builtin_amdgcn_exp2f((m - M) * CEXP) / Ls;
    alpha *= scale;
    v4u pf[2];
#pragma unroll
    for (int u = 0; u < 8; ++u) { const float ps = p[u] * scale; pf[u >> 2][u & 3] = cvt_pk(ps, ps) & pmask; }
#pragma unroll
    for (int e = 0; e < 8; ++e) oacc[e] *= alpha;
#pragma unroll
    for (int e = 0; e < 8; ++e) oacc[e] = mfma16(tr_frag(item[2 * NT - 3], e), pf[0], oacc[e]);
    SB();
    BACK_WO(1);
    SB();
#pragma unroll
    for (int e = 0; e < 8; ++e) oacc[e] = mfma16(tr_frag(item[2 * NT - 1], e), pf[1], oacc[e]);
    SB();
    STAMP(15);
  }

  STAMP(2);
  const int lane2 = __builtin_amdgcn_mbcnt_hi(~0u, __builtin_amdgcn_mbcnt_lo(~0u, 0u));
  asm volatile("s_waitcnt lgkmcnt(0)" ::: "memory");
#pragma unroll
  for (int e = 0; e < 8; ++e) *(LAS f32x4*)(LW + e * 1024 + lane2 * 16) = oacc[e];
  const int goff = WS_O + (bp * 128 + 4 * (8 * kq + w)) * 1024;
  v4u ob[4];
#pragma unroll
  for (int ks = 0; ks < 4; ++ks)
    ob[ks] = FIRSTLOOK ? __builtin_amdgcn_raw_buffer_load_b128(rws, lane2 * 16, goff + ((ks + w) & 3) * 1024, AUX_SC1) : v4u{SENT, SENT, SENT, SENT};
  TSTAMP(21);
  LDS_BAR();
  STAMP(3);
  {
    const int fl = 32 * (w & 1) + (lane2 >> 1);
    const LAS unsigned char* src = L + ((fl >> 3) & 1) * (4 * L_WAVE) + ((w & ~1) + (lane2 & 1)) * 1024 + (fl & 0x37) * 16;
    const int par = (lane2 & 1) * 128;
    f32x4 pa[8];
#pragma unroll
    for (int j = 0; j < 8; ++j) pa[j] = *(const LAS f32x4*)(src + (j >> 1) * L_WAVE + ((j & 1) ? 128 - par : par));
    const f32x4 a0 = ((pa[0] + pa[1]) + (pa[2] + pa[3])) + ((pa[4] + pa[5]) + (pa[6] + pa[7]));
    v2u ov = {cvt_pk(a0[0], a0[1]), cvt_pk(a0[2], a0[3])};
#pragma unroll
    for (int j = 0; j < 2; ++j) ov[j] = (ov[j] == SENT) ? 0x7FC07FC0u : ov[j];
    __builtin_amdgcn_raw_buffer_store_b64(ov, rws, lane2 * 8, o_slot + w * 512, AUX_SC1);
  }
  SB();
  STAMP(4);
  {
    TSTAMP(16);
    unsigned spins = 0;
    while (__any(max(max(umax4(ob[0]), umax4(ob[1])), max(umax4(ob[2]), umax4(ob[3]))) == SENT)) {
      if (++spins > kSpinCap) break;
      if (spins > 1) __builtin_amdgcn_s_sleep(POLL_SLEEP); else if (POLL_FIRST) __builtin_amdgcn_s_sleep(POLL_FIRST);
      if (PROBE && spins > PROBE_AFTER) {
        for (unsigned sp2 = 0; sp2 < kSpinCap; ++sp2) {
          MEMFENCE();
          const unsigned pr = __builtin_amdgcn_raw_buffer_load_b32(rws, (lane2 & (PROBE - 1)) * (4096 / (PROBE ? PROBE : 1)), goff, AUX_SC1);
          if (__any(pr != SENT)) break;
          __builtin_amdgcn_s_sleep(1);
        }
      }
      MEMFENCE();
#pragma unroll
      for (int ks = 0; ks < 4; ++ks) ob[ks] = __builtin_amdgcn_raw_buffer_load_b128(rws, lane2 * 16, goff + ((ks + w) & 3) * 1024, AUX_SC1);
    }
    TSTAMP(17);
#pragma unroll
    for (int hf = 0; hf < 2; ++hf) {
      f32x4 c[4];
#pragma unroll
      for (int e = 0; e < 4; ++e) c[e] = f32x4{0.f, 0.f, 0.f, 0.f};
#pragma unroll
      for (int ks = 0; ks < 4; ++ks)
#pragma unroll
        for (int e = 0; e < 4; ++e) c[e] = mfma16(wf[ks][4 * hf + e], ob[ks], c[e]);
#pragma unroll
      for (int rho = 0; rho < 4; ++rho)
        *(LAS f32x4*)(LW + L_C + (((lane2 & 15) * 512 + (lane2 >> 4) * 128 + (lane2 & 7) * 16) ^ (rho * 32 + hf * 16))) =
            f32x4{c[0][rho], c[1][rho], c[2][rho], c[3][rho]};
    }
  }
  TSTAMP(18);
  LDS_BAR();
  STAMP(5);
  {
    const int row = 2 * w + (lane2 >> 5);
    const int so = row * 512 + (((lane2 & 31) * 16) ^ ((row & 7) * 16));
    f32x4 pa[8];
#pragma unroll
    for (int ww = 0; ww < 8; ++ww) pa[ww] = *(const LAS f32x4*)(L + ww * L_WAVE + L_C + so);
    const f32x4 s = ((pa[0] + pa[1]) + (pa[2] + pa[3])) + ((pa[4] + pa[5]) + (pa[6] + pa[7]));
    v4u sv = __builtin_bit_cast(v4u, s);
#pragma unroll
    for (int j = 0; j < 4; ++j) sv[j] = (sv[j] == SENT) ? 0x7FC00000u : sv[j];
    const int p_own2 = ((lane2 >> 3) & 3) * 8192 + (lane2 >> 5) * 128 + (lane2 & 7) * 16;
    __builtin_amdgcn_raw_buffer_store_b128(sv, rws, p_own2, p_base + kq * 2048 + w * 256, AUX_SC1);
  }
  TSTAMP(19);
  {
    const int poff = (lane2 >> 4) * 2048 + (lane2 & 15) * 16;
    int oo = ((lane2 >> 3) & 1) * (DM * 2) + (lane2 & 7) * 8 + (lane2 >> 5) * 4 + (lane2 & 16) * 0x1000000;
    asm volatile("" : "+v"(oo));
    v4u pv;
    unsigned spins = 0;
    if (POLL2_FIRST) __builtin_amdgcn_s_sleep(POLL2_FIRST);
    for (;;) {
      MEMFENCE();
      pv = __builtin_amdgcn_raw_buffer_load_b128(rws, poff, p_base + kq * 8192 + w * 256, AUX_SC1);
      if (!__any(umax4(pv) == SENT)) break;
      if (++spins > kSpinCap) break;
      __builtin_amdgcn_s_sleep(POLL2_SLEEP);
    }
    TSTAMP(20);
    const float u0 = rs32(__uint_as_float(pv[0]), __uint_as_float(pv[2])), u1 = rs32(__uint_as_float(pv[1]), __uint_as_float(pv[3]));
    const float t0 = rs16(u0, u0), t1 = rs16(u1, u1);
    __builtin_amdgcn_raw_buffer_store_b32(cvt_pk(t0, t1), rout, oo, out_off, AUX_OUT);
  }
  {
    while ((int)((unsigned)__builtin_amdgcn_s_memrealtime() - tfix0) < 10000) __builtin_amdgcn_s_sleep(8);
    int v = 0;
    for (int j = 0; j < 4; ++j) { const int d = (int)(__builtin_amdgcn_raw_buffer_load_b32(rws, lane2 * 128, 73728 + j * 8192, AUX_SC1) - tfix0); v = j ? min(v, d) : d; }
    for (int o = 1; o < 64; o <<= 1) v = min(v, __shfl_xor(v, o));
    v = __builtin_amdgcn_readfirstlane(v);
    v = max(min(v, 3000), -3000);
    while ((int)((unsigned)__builtin_amdgcn_s_memrealtime() - tfix0) < 12000 + v) __builtin_amdgcn_s_sleep(1);
  }
}

}

extern "C" void kernel_launch(void* const* d_in, const int* in_sizes, int n_in, void* d_out, int out_size, void* d_ws,
                              size_t ws_size, hipStream_t stream) {
  (void)in_sizes; (void)n_in; (void)out_size; (void)ws_size;
  (void)hipFuncSetAttribute((const void*)mha_fused, hipFuncAttributeMaxDynamicSharedMemorySize, L_BYTES);
  hipLaunchKernelGGL(mha_fused, dim3(NWG), dim3(NTHR), L_BYTES, stream, (const unsigned short*)d_in[0], (const unsigned short*)d_in[1],
                     (const unsigned short*)d_in[2], (const unsigned short*)d_in[3], (const unsigned short*)d_in[4],
                     (unsigned short*)d_out, (unsigned char*)d_ws);
}
```

```cpp
#include <hip/hip_runtime.h>
#include <hip/hip_bf16.h>

#define LAS __attribute__((address_space(3)))
typedef unsigned v4u __attribute__((ext_vector_type(4)));
typedef unsigned v2u __attribute__((ext_vector_type(2)));
typedef float f32x4 __attribute__((ext_vector_type(4)));
typedef __bf16 bf16x8 __attribute__((ext_vector_type(8)));
typedef __bf16 bf16x2 __attribute__((ext_vector_type(2)));
typedef float f32x2 __attribute__((ext_vector_type(2)));

namespace {

constexpr int NB = 16, SQ = 8, DM = 1024, NH = 32, SK = 2048, DH = 128;
constexpr int NWG = 256, NTHR = 512;
constexpr int NT = 16;
constexpr float CEXP = 0.08838834764831845f * 1.4426950408889634f;
constexpr int AUX_NT = 2, AUX_SC1 = 16;
constexpr int RSRC_W3 = 0x00020000;
constexpr unsigned SENT = 0xFFFFFFFFu;
constexpr unsigned kSpinCap = 1u << 16;

constexpr int PACE_C = 448;
constexpr int PACE_DZ1 = 32, PACE_DZ2 = 20;
constexpr int PACE_S0 = 4, PACE_B4 = 42, PACE_MAX = 500;

constexpr int WS_EPOCH = 0;
constexpr int WS_SPARE = 2048;
constexpr int WS_FLAG = 4096;
constexpr int WS_PACE = WS_FLAG + 32768;
constexpr int WS_O = 131072;
constexpr int WS_P = WS_O + 8 * 128 * 1024;
constexpr int WS_BYTES = WS_P + 8 * 8 * 16 * 2048;

constexpr int L_WAVE = 16384;
constexpr int L_QS = 8 * L_WAVE;
constexpr int QS_STRIDE = 272;
constexpr int L_ML = L_QS + 16 * QS_STRIDE;
constexpr int L_PC = L_ML + 1024;
constexpr int L_BYTES = L_PC + 16;
constexpr int QP_STRIDE = 528;
constexpr int L_X = 9216, X_ODD = 2064;
constexpr int L_C = 8192;

__device__ __forceinline__ f32x4 mfma16(v4u a, v4u b, f32x4 c) {
  return __builtin_amdgcn_mfma_f32_16x16x32_bf16(__builtin_bit_cast(bf16x8, a), __builtin_bit_cast(bf16x8, b), c, 0, 0, 0);
}
__device__ __forceinline__ unsigned pack_lo(unsigned a, unsigned b) { return __builtin_amdgcn_perm(b, a, 0x05040100u); }
__device__ __forceinline__ unsigned pack_hi(unsigned a, unsigned b) { return __builtin_amdgcn_perm(b, a, 0x07060302u); }
__device__ __forceinline__ unsigned cvt_pk(float lo, float hi) {
  f32x2 v = {lo, hi};
  return __builtin_bit_cast(unsigned, __builtin_convertvector(v, bf16x2));
}
__device__ __forceinline__ v4u tr_frag(const v4u (&R)[8], int e) {
  v4u f;
#pragma unroll
  for (int jj = 0; jj < 4; ++jj)
    f[jj] = (e & 1) ? pack_hi(R[2 * jj][e >> 1], R[2 * jj + 1][e >> 1]) : pack_lo(R[2 * jj][e >> 1], R[2 * jj + 1][e >> 1]);
  return f;
}
template <int CTRL> __device__ __forceinline__ int dppi(int x) { return __builtin_amdgcn_mov_dpp(x, CTRL, 0xf, 0xf, true); }
__device__ __forceinline__ int sum8(int v) { v += dppi<0xB1>(v); v += dppi<0x4E>(v); return v + dppi<0x141>(v); }
__device__ __forceinline__ float ror8(float x) {
  return __builtin_bit_cast(float, dppi<0x128>(__builtin_bit_cast(int, x)));
}
__device__ __forceinline__ float bsel(unsigned mask, float a, float b) {
  return __uint_as_float((__float_as_uint(a) & mask) | (__float_as_uint(b) & ~mask));
}
__device__ __forceinline__ float xg_max(float x) {
  auto s = __builtin_amdgcn_permlane16_swap(__float_as_uint(x), __float_as_uint(x), false, false);
  x = fmaxf(__uint_as_float(s[0]), __uint_as_float(s[1]));
  auto t = __builtin_amdgcn_permlane32_swap(__float_as_uint(x), __float_as_uint(x), false, false);
  return fmaxf(__uint_as_float(t[0]), __uint_as_float(t[1]));
}
__device__ __forceinline__ float rs32(float a, float b) {
  auto t = __builtin_amdgcn_permlane32_swap(__float_as_uint(a), __float_as_uint(b), false, false);
  return __uint_as_float(t[0]) + __uint_as_float(t[1]);
}
__device__ __forceinline__ float rs16(float a, float b) {
  auto t = __builtin_amdgcn_permlane16_swap(__float_as_uint(a), __float_as_uint(b), false, false);
  return __uint_as_float(t[0]) + __uint_as_float(t[1]);
}
__device__ __forceinline__ float xg_sum(float x) { x = rs16(x, x); return rs32(x, x); }
__device__ __forceinline__ unsigned umax4(v4u v) { return max(max(v[0], v[1]), max(v[2], v[3])); }
__device__ __forceinline__ unsigned now32() { return (unsigned)__builtin_amdgcn_s_memrealtime(); }
__device__ __forceinline__ void hold_until(unsigned t_abs) {
  const unsigned now = now32();
  int d = min((int)(t_abs - now), PACE_MAX);
  const unsigned lim = now + (unsigned)d;
  while (d > 0) { __builtin_amdgcn_s_sleep(1); d = (int)(lim - now32()); }
}
#define SB() __builtin_amdgcn_sched_barrier(0)
#define BAR() __builtin_amdgcn_s_barrier()
#define MEMFENCE() asm volatile("" ::: "memory")
#define LDS_BAR() { asm volatile("s_waitcnt lgkmcnt(0)" ::: "memory"); BAR(); MEMFENCE(); }

template <int V> struct IC { static constexpr int value = V; };
template <int I, int N, class F> __device__ __forceinline__ void static_for(F&& f) {
  if constexpr (I < N) { f(IC<I>{}); static_for<I + 1, N>(f); }
}
constexpr int item_at(int pos) { return pos == 2 * NT - 3 ? 2 * NT - 2 : pos == 2 * NT - 2 ? 2 * NT - 3 : pos; }
constexpr int next_after(int pos) { return pos < 2 ? pos + 3 : (pos == 2 || pos + 2 >= 2 * NT) ? -1 : item_at(pos + 2); }
constexpr bool pace_at(int t) { return t == 1 || t == 4 || t == 7 || t == 10; }

__global__ void __launch_bounds__(NTHR, 2)
mha_fused(const unsigned short* __restrict__ x, const unsigned short* __restrict__ kc, const unsigned short* __restrict__ vc,
          const unsigned short* __restrict__ wq, const unsigned short* __restrict__ wo, unsigned short* out, unsigned char* ws) {
  extern __shared__ __attribute__((aligned(16))) unsigned char smem[];
  LAS unsigned char* L = (LAS unsigned char*)smem;
  const int tid = threadIdx.x, lane = tid & 63, w = __builtin_amdgcn_readfirstlane(tid >> 6);
  const int r = lane & 15, g = lane >> 4;
  const int wg = blockIdx.x, idx = wg >> 3, xg = wg & 7, i4 = idx & 3;
  const int h = 4 * xg + i4, bp = idx >> 2;
  const int kq = xg >> 1, ct = 4 * (xg & 1) + ((i4 + 2) & 3);
  const int beta = w >> 2, ws4 = w & 3;
  const int bh = (2 * bp + beta) * NH + h;
  int rot = (4 * h + bp + 8 * beta) & (NT - 1);
  LAS unsigned char* LW = L + w * L_WAVE;

  asm volatile("" : "+s"(out));
  const __amdgpu_buffer_rsrc_t rk = __builtin_amdgcn_make_buffer_rsrc((void*)(kc + (size_t)bh * SK * DH), 0, SK * DH * 2, RSRC_W3);
  const __amdgpu_buffer_rsrc_t rv = __builtin_amdgcn_make_buffer_rsrc((void*)(vc + (size_t)bh * SK * DH), 0, SK * DH * 2, RSRC_W3);
  const __amdgpu_buffer_rsrc_t rws = __builtin_amdgcn_make_buffer_rsrc((void*)ws, 0, WS_BYTES, RSRC_W3);
  const __amdgpu_buffer_rsrc_t rw0 = __builtin_amdgcn_make_buffer_rsrc((void*)ws, 0, w == 0 ? WS_BYTES : 0, RSRC_W3);
  const __amdgpu_buffer_rsrc_t rwq = __builtin_amdgcn_make_buffer_rsrc((void*)wq, 0, DM * NH * DH * 2, RSRC_W3);
  const __amdgpu_buffer_rsrc_t rwo = __builtin_amdgcn_make_buffer_rsrc((void*)wo, 0, NH * DH * DM * 2, RSRC_W3);
  const __amdgpu_buffer_rsrc_t rx = __builtin_amdgcn_make_buffer_rsrc((void*)x, 0, NB * SQ * DM * 2, RSRC_W3);
  const __amdgpu_buffer_rsrc_t rout = __builtin_amdgcn_make_buffer_rsrc((void*)out, 0, NB * SQ * DM * 2, RSRC_W3);

  const int koff = (4 * ((r >> 1) & 1) + (r >> 2)) * 256 + (2 * g + (r & 1)) * 16;
  const int voff = lane * 16;
  v4u item[2 * NT][8];
#define LOAD_ITEM(n) { const int so_ = (((((n) >> 1) + rot) & (NT - 1)) * 4 + ws4) * 8192; _Pragma("unroll") for (int i_ = 0; i_ < 8; ++i_) \
    item[n][i_] = ((n) & 1) ? __builtin_amdgcn_raw_buffer_load_b128(rv, voff, so_ + i_ * 1024, AUX_NT) \
                            : __builtin_amdgcn_raw_buffer_load_b128(rk, koff, so_ + (i_ >> 1) * 2048 + (i_ & 1) * 128, AUX_NT); }

  const int o_slot = WS_O + (bp * 128 + 4 * h) * 1024;
  const int p_base = WS_P + (bp * 8 + ct) * (16 * 2048);
  const int out_off = ((16 * bp + 2 * w) * DM + 128 * ct + 32 * kq) * 2;

  unsigned tch[5];
  {
    typedef const __attribute__((address_space(4))) unsigned* cptr;
    tch[0] = *(cptr)(kc + (size_t)bh * SK * DH);
    tch[1] = *(cptr)(vc + (size_t)bh * SK * DH);
    tch[2] = *(cptr)(wo + (size_t)(1024 * kq + 128 * w) * DM);
    tch[3] = *(cptr)(ws + WS_SPARE + w * 128);
    tch[4] = *(cptr)(ws + WS_BYTES + w * 128);
    SB();
  }
  {
#define KBLK(s) ((((4 * beta + (s) + bp) & 7) << 2) | ws4)
    const int wqoff = (8 * g) * (NH * DH * 2) + r * 16;
    v4u wa[4][8], xr[4];
#define LOAD_WQ(s) { const int so_ = KBLK(s) * 32 * (NH * DH * 2) + h * (DH * 2); _Pragma("unroll") for (int j_ = 0; j_ < 8; ++j_) \
      wa[s][j_] = __builtin_amdgcn_raw_buffer_load_b128(rwq, wqoff, so_ + j_ * (NH * DH * 2), 0); }
#define LOAD_X() { _Pragma("unroll") for (int i_ = 0; i_ < 4; ++i_) \
      xr[i_] = __builtin_amdgcn_raw_buffer_load_b128(rx, voff, (16 * bp + 2 * w) * (DM * 2) + i_ * 1024, 0); }
#define STAGE_X() { _Pragma("unroll") for (int i_ = 0; i_ < 4; ++i_) \
      *(LAS v4u*)(LW + L_X + w * 32 + (i_ >> 1) * X_ODD + (i_ & 1) * 1024 + voff) = xr[i_]; }
    const LAS unsigned char* xrow = L + (r >> 1) * (L_WAVE + 32) + L_X + (r & 1) * X_ODD + g * 16;
    f32x4 qa[8];
#pragma unroll
    for (int e = 0; e < 8; ++e) qa[e] = f32x4{0.f, 0.f, 0.f, 0.f};
#define MMA_WQ(s) { const v4u xf_ = *(const LAS v4u*)(xrow + KBLK(s) * 64); \
      _Pragma("unroll") for (int e_ = 0; e_ < 8; ++e_) qa[e_] = mfma16(tr_frag(wa[s], e_), xf_, qa[e_]); }
#define PUT_Q() { _Pragma("unroll") for (int rho = 0; rho < 4; ++rho) { \
      LAS f32x4* p_ = (LAS f32x4*)(LW + r * QP_STRIDE + (32 * g + 8 * rho) * 4); \
      p_[0] = f32x4{qa[0][rho], qa[1][rho], qa[2][rho], qa[3][rho]}; \
      p_[1] = f32x4{qa[4][rho], qa[5][rho], qa[6][rho], qa[7][rho]}; } }
#define REDUCE_Q() { const int row_ = tid >> 5, n4_ = (tid & 31) * 4; f32x4 pa_[8]; \
      _Pragma("unroll") for (int ww = 0; ww < 8; ++ww) pa_[ww] = *(LAS f32x4*)(L + ww * L_WAVE + row_ * QP_STRIDE + n4_ * 4); \
      const f32x4 s_ = ((pa_[0] + pa_[1]) + (pa_[2] + pa_[3])) + ((pa_[4] + pa_[5]) + (pa_[6] + pa_[7])); \
      *(LAS v2u*)(L + L_QS + row_ * QS_STRIDE + n4_ * 2) = v2u{cvt_pk(s_[0], s_[1]), cvt_pk(s_[2], s_[3])}; }
    if ((bp & 1) == 0) {
      asm volatile("; path A" ::: "memory");
      LOAD_X(); SB(); LOAD_WQ(0); SB(); LOAD_WQ(1); SB(); LOAD_WQ(2); SB(); LOAD_WQ(3); SB();
      STAGE_X(); SB();
      LDS_BAR(); SB();
      LOAD_ITEM(0); SB();
      MMA_WQ(0); MMA_WQ(1); MMA_WQ(2); MMA_WQ(3);
      PUT_Q(); SB();
      LOAD_ITEM(1); SB();
      LDS_BAR(); SB();
      REDUCE_Q(); SB();
      LOAD_ITEM(2); SB();
    } else {
      asm volatile("; path B" ::: "memory");
      LOAD_X(); SB(); LOAD_WQ(0); SB();
      LOAD_ITEM(0); SB(); LOAD_ITEM(1); SB();
      STAGE_X(); SB();
      LDS_BAR(); SB();
      LOAD_WQ(1); SB(); LOAD_WQ(2); SB();
      MMA_WQ(0); SB();
      LOAD_WQ(3); SB();
      MMA_WQ(1); SB();
      LOAD_ITEM(2); SB();
      MMA_WQ(2); MMA_WQ(3);
      PUT_Q(); SB();
      LDS_BAR(); SB();
      REDUCE_Q(); SB();
    }
  }
  asm volatile("" :: "s"(tch[0]), "s"(tch[1]), "s"(tch[2]), "s"(tch[3]), "s"(tch[4]));
  LDS_BAR();
  asm volatile("" : "+s"(rot));
  v4u qf[2];
#pragma unroll
  for (int hf = 0; hf < 2; ++hf) qf[hf] = *(LAS v4u*)(L + L_QS + (8 * beta + (r & 7)) * QS_STRIDE + (8 * hf + 2 * g + (r >> 3)) * 16);
  unsigned hmask = (r >> 3) ? 0xFFFFFFFFu : 0u;
  asm volatile("" : "+v"(hmask));
  const unsigned pmask = hmask ^ 0x0000FFFFu;

  v4u wr[4][8], wf[4][8];
  const int wooff = (32 * g) * (DM * 2) + r * 16;
  const int wos = (1024 * kq + 128 * w) * (DM * 2) + ct * 256;
#define LOAD_WO(ks) { _Pragma("unroll") for (int j_ = 0; j_ < 8; ++j_) \
    wr[ks][j_] = __builtin_amdgcn_raw_buffer_load_b128(rwo, wooff, wos + (8 * (j_ & 3) + 2 * (((ks) + w) & 3) + (j_ >> 2)) * (DM * 2), 0); }
#define PARK_WO(ks) { _Pragma("unroll") for (int e_ = 0; e_ < 8; ++e_) *(LAS v4u*)(LW + (ks) * 8192 + e_ * 1024 + voff) = tr_frag(wr[ks], e_); }
#define KEEP_WO(ks) { _Pragma("unroll") for (int e_ = 0; e_ < 8; ++e_) { wf[ks][e_] = tr_frag(wr[ks], e_); asm volatile("" : "+v"(wf[ks][e_])); } }
#define BACK_WO(ks) { _Pragma("unroll") for (int e_ = 0; e_ < 8; ++e_) wf[ks][e_] = *(LAS v4u*)(LW + (ks) * 8192 + e_ * 1024 + voff); }
#define SCORES(n, s) { f32x4 sa_[4]; \
    _Pragma("unroll") for (int u = 0; u < 4; ++u) sa_[u] = mfma16(item[n][2 * u], qf[0], f32x4{0.f, 0.f, 0.f, 0.f}); \
    _Pragma("unroll") for (int u = 0; u < 4; ++u) sa_[u] = mfma16(item[n][2 * u + 1], qf[1], sa_[u]); \
    _Pragma("unroll") for (int u = 0; u < 4; ++u) (s)[u] = bsel(hmask, sa_[u][3], sa_[u][0]) + ror8(bsel(hmask, sa_[u][1], sa_[u][2])); \
    asm volatile("" : "+v"((s)[0]), "+v"((s)[1]), "+v"((s)[2]), "+v"((s)[3])); }

  f32x4 oacc[8];
#pragma unroll
  for (int e = 0; e < 8; ++e) oacc[e] = f32x4{0.f, 0.f, 0.f, 0.f};
  float m = -INFINITY, lsum = 0.f;
  v2u epoch0, flags_v;
  unsigned tag_lo = 0, tag_hi = 0;
  v4u snap;
  const unsigned tbase = now32();
  unsigned line0 = 0, line1 = 0, tprev = 0;

  static_for<0, NT - 2>([&](auto tc) __attribute__((always_inline)) {
    constexpr int t = decltype(tc)::value;
    float s[4];
    SCORES(2 * t, s);
    SB();
    if constexpr (t == 0) {
      epoch0 = __builtin_amdgcn_raw_buffer_load_b64(rw0, 0, WS_EPOCH + bp * 256, AUX_SC1);
      const int p_own = ((voff >> 7) & 3) * 8192 + (voff >> 9) * 128 + (voff & 0x70);
      __builtin_amdgcn_raw_buffer_store_b64(v2u{SENT, SENT}, rws, voff >> 1, o_slot + w * 512, AUX_SC1);
      __builtin_amdgcn_raw_buffer_store_b128(v4u{SENT, SENT, SENT, SENT}, rws, p_own, p_base + kq * 2048 + w * 256, AUX_SC1);
      MEMFENCE();
      SB();
    }
    if constexpr (t >= 1 && t <= 4) LOAD_WO(t - 1);
    if constexpr (t == NT - 5) flags_v = __builtin_amdgcn_raw_buffer_load_b64(rw0, (voff << 3) & 0xF80, WS_FLAG + bp * 4096, AUX_SC1);
    if constexpr (pace_at(t - 2)) {
      const int dt = (int)(snap[0] - tbase);
      const bool ok = snap[3] == ~(snap[0] ^ snap[1] ^ snap[2]) && dt > -1500 && dt < 12000;
      const int rs = ok ? (int)snap[1] : 0;
      const int nr = __builtin_popcount((unsigned)__builtin_amdgcn_ballot_w64(rs > 0) & 0xFFu);
      const int cm = (int)((float)__builtin_amdgcn_readfirstlane(sum8(rs)) * __builtin_amdgcn_rcpf((float)max(nr, 1)));
      const int cu = cm > PACE_C + (t - 2 > 4 ? PACE_DZ2 : PACE_DZ1) ? min(cm - PACE_S0, PACE_C + 100) : PACE_C;
      const int v = sum8(dt - (int)snap[2] * cu) >> 3;
      const bool all_ok = (__builtin_amdgcn_ballot_w64(ok) & 0xFFull) == 0xFFull;
      if (w == 0) *(LAS v2u*)(L + L_PC) = v2u{tbase + (unsigned)(all_ok ? v : -1000000), (unsigned)cu};
    }
    if constexpr (t >= 4) hold_until(line0 + ((16 * t - 7 + w) * line1 >> 4));
    if constexpr (pace_at(t - 1)) {
      const int px = (voff >> 4) & 7;
      const int peer = (((bp + px) & 7) * 4 + ((i4 + px) & 3)) * 8 + ((xg + px) & 7);
      snap = __builtin_amdgcn_raw_buffer_load_b128(rw0, peer * 128, WS_PACE, AUX_SC1);
      SB();
    }
    if constexpr (next_after(2 * t) >= 0) LOAD_ITEM(next_after(2 * t));
    SB();
    if constexpr (pace_at(t - 2)) asm volatile("s_waitcnt lgkmcnt(0)" ::: "memory");
    if constexpr (t == 3) {
      const unsigned long long tag64 = (((unsigned long long)(unsigned)__builtin_amdgcn_readfirstlane(epoch0[1]) << 32) |
                                        (unsigned)__builtin_amdgcn_readfirstlane(epoch0[0])) + 1ull;
      tag_lo = (unsigned)tag64; tag_hi = (unsigned)(tag64 >> 32);
      asm volatile("s_waitcnt vmcnt(42)" ::: "memory");
      BAR();
      MEMFENCE();
      __builtin_amdgcn_raw_buffer_store_b64(v2u{tag_lo, tag_hi}, rw0, (voff >> 1) & 0x78, WS_FLAG + (bp * 32 + h) * 128, AUX_SC1);
    } else if constexpr (t == NT - 3) {
      if (w == 0) {
        if (__builtin_expect(!__all(flags_v[0] == tag_lo && flags_v[1] == tag_hi), 0)) {
          for (unsigned spins = 0; spins < kSpinCap; ++spins) {
            __builtin_amdgcn_s_sleep(2);
            MEMFENCE();
            const v2u fv = __builtin_amdgcn_raw_buffer_load_b64(rws, (voff << 3) & 0xF80, WS_FLAG + bp * 4096, AUX_SC1);
            if (__all(fv[0] == tag_lo && fv[1] == tag_hi)) break;
          }
        }
        if (h == 0) __builtin_amdgcn_raw_buffer_store_b64(v2u{tag_lo, tag_hi}, rws, (voff >> 1) & 0x78, WS_EPOCH + bp * 256, AUX_SC1);
      }
      MEMFENCE();
      BAR();
      MEMFENCE();
    } else BAR();
    if constexpr (pace_at(t - 2)) {
      MEMFENCE();
      const v2u ln = *(LAS v2u*)(L + L_PC);
      line0 = __builtin_amdgcn_readfirstlane(ln[0]); line1 = __builtin_amdgcn_readfirstlane(ln[1]);
    }
    if constexpr (pace_at(t)) {
      const unsigned tv = now32();
      const unsigned rate = t > 1 ? (tv - tprev - (t == 4 ? PACE_B4 : 0)) / (t > 1 ? t - 1 : 1) : 0u;
      if constexpr (t == 1) tprev = tv;
      __builtin_amdgcn_raw_buffer_store_b128(v4u{tv, rate, (unsigned)t, ~(tv ^ rate ^ (unsigned)t)}, rw0, voff & 0x70, WS_PACE + wg * 128, AUX_SC1);
    }
    SB();
    const float mx = xg_max(fmaxf(fmaxf(s[0], s[1]), fmaxf(s[2], s[3])));
    const float mn = fmaxf(m, mx);
    const float alpha = __builtin_amdgcn_exp2f((m - mn) * CEXP);
    const float mc = mn * CEXP;
    m = mn;
    float p[4];
#pragma unroll
    for (int u = 0; u < 4; ++u) p[u] = __builtin_amdgcn_exp2f(__builtin_fmaf(s[u], CEXP, -mc));
    lsum = lsum * alpha + ((p[0] + p[1]) + (p[2] + p[3]));
    asm volatile("" : "+v"(lsum));
    const v4u pf = {cvt_pk(p[0], p[0]) & pmask, cvt_pk(p[1], p[1]) & pmask, cvt_pk(p[2], p[2]) & pmask, cvt_pk(p[3], p[3]) & pmask};
#pragma unroll
    for (int e = 0; e < 8; ++e) oacc[e] *= alpha;
#pragma unroll
    for (int e = 0; e < 8; ++e) oacc[e] = mfma16(tr_frag(item[2 * t + 1], e), pf, oacc[e]);
    asm volatile("" : "+v"(oacc[0]), "+v"(oacc[1]), "+v"(oacc[2]), "+v"(oacc[3]), "+v"(oacc[4]), "+v"(oacc[5]), "+v"(oacc[6]), "+v"(oacc[7]));
    SB();
    if constexpr (t == 2) PARK_WO(0);
    if constexpr (t == 3) PARK_WO(1);
    if constexpr (t == 4) KEEP_WO(2);
    if constexpr (t == 5) KEEP_WO(3);
    SB();
    if constexpr (t >= 3) hold_until(line0 + ((16 * t + 1 + w) * line1 >> 4));
    if constexpr (next_after(2 * t + 1) >= 0) LOAD_ITEM(next_after(2 * t + 1));
    SB();
  });
  {
    float s[8];
    SCORES(2 * NT - 4, s);
    SB();
    hold_until(line0 + ((16 * (NT - 2) - 7 + w) * line1 >> 4));
    LOAD_ITEM(2 * NT - 3); SB();
    SCORES(2 * NT - 2, s + 4);
    SB();
    BACK_WO(0);
    const float mx = xg_max(fmaxf(fmaxf(fmaxf(s[0], s[1]), fmaxf(s[2], s[3])), fmaxf(fmaxf(s[4], s[5]), fmaxf(s[6], s[7]))));
    const float mn = fmaxf(m, mx);
    float alpha = __builtin_amdgcn_exp2f((m - mn) * CEXP);
    const float mc = mn * CEXP;
    m = mn;
    float p[8];
#pragma unroll
    for (int u = 0; u < 8; ++u) p[u] = __builtin_amdgcn_exp2f(__builtin_fmaf(s[u], CEXP, -mc));
    lsum = xg_sum(lsum * alpha + (((p[0] + p[1]) + (p[2] + p[3])) + ((p[4] + p[5]) + (p[6] + p[7]))));
    LAS unsigned char* ml = L + L_ML + beta * 512 + ((voff >> 4) & 7) * 64;
    if (voff < 256) *(LAS f32x2*)(ml + ws4 * 16 + (voff >> 7) * 8) = f32x2{m, lsum};
    SB();
    LOAD_ITEM(2 * NT - 1); SB();
    LDS_BAR();
    f32x4 rec[4];
#pragma unroll
    for (int j = 0; j < 4; ++j) rec[j] = *(LAS f32x4*)(ml + j * 16);
    const float M = fmaxf(fmaxf(fmaxf(rec[0][0], rec[0][2]), fmaxf(rec[1][0], rec[1][2])), fmaxf(fmaxf(rec[2][0], rec[2][2]), fmaxf(rec[3][0], rec[3][2])));
    float Ls = 0.f;
#pragma unroll
    for (int j = 0; j < 4; ++j)
      Ls += __builtin_amdgcn_exp2f((rec[j][0] - M) * CEXP) * rec[j][1] + __builtin_amdgcn_exp2f((rec[j][2] - M) * CEXP) * rec[j][3];
    const float scale = __builtin_amdgcn_exp2f((m - M) * CEXP) / Ls;
    alpha *= scale;
    v4u pf[2];
#pragma unroll
    for (int u = 0; u < 8; ++u) { const float ps = p[u] * scale; pf[u >> 2][u & 3] = cvt_pk(ps, ps) & pmask; }
#pragma unroll
    for (int e = 0; e < 8; ++e) oacc[e] *= alpha;
#pragma unroll
    for (int e = 0; e < 8; ++e) oacc[e] = mfma16(tr_frag(item[2 * NT - 3], e), pf[0], oacc[e]);
    SB();
    BACK_WO(1);
    SB();
#pragma unroll
    for (int e = 0; e < 8; ++e) oacc[e] = mfma16(tr_frag(item[2 * NT - 1], e), pf[1], oacc[e]);
    SB();
  }

  const int lane2 = __builtin_amdgcn_mbcnt_hi(~0u, __builtin_amdgcn_mbcnt_lo(~0u, 0u));
  asm volatile("s_waitcnt lgkmcnt(0)" ::: "memory");
#pragma unroll
  for (int e = 0; e < 8; ++e) *(LAS f32x4*)(LW + e * 1024 + lane2 * 16) = oacc[e];
  LDS_BAR();
  {
    const int fl = 32 * (w & 1) + (lane2 >> 1);
    const LAS unsigned char* src = L + ((fl >> 3) & 1) * (4 * L_WAVE) + ((w & ~1) + (lane2 & 1)) * 1024 + (fl & 0x37) * 16;
    const int par = (lane2 & 1) * 128;
    f32x4 pa[8];
#pragma unroll
    for (int j = 0; j < 8; ++j) pa[j] = *(const LAS f32x4*)(src + (j >> 1) * L_WAVE + ((j & 1) ? 128 - par : par));
    const f32x4 a0 = ((pa[0] + pa[1]) + (pa[2] + pa[3])) + ((pa[4] + pa[5]) + (pa[6] + pa[7]));
    v2u ov = {cvt_pk(a0[0], a0[1]), cvt_pk(a0[2], a0[3])};
#pragma unroll
    for (int j = 0; j < 2; ++j) ov[j] = (ov[j] == SENT) ? 0x7FC07FC0u : ov[j];
    __builtin_amdgcn_raw_buffer_store_b64(ov, rws, lane2 * 8, o_slot + w * 512, AUX_SC1);
  }
  SB();
  {
    const int goff = WS_O + (bp * 128 + 4 * (8 * kq + w)) * 1024;
    unsigned spins = 0;
#pragma unroll 1
    for (; spins < kSpinCap; ++spins) {
      MEMFENCE();
      if (__any(__builtin_amdgcn_raw_buffer_load_b32(rws, 0, goff, AUX_SC1) != SENT)) break;
      __builtin_amdgcn_s_sleep(1);
    }
    v4u ob[4];
    for (;; ++spins) {
      MEMFENCE();
#pragma unroll
      for (int ks = 0; ks < 4; ++ks) ob[ks] = __builtin_amdgcn_raw_buffer_load_b128(rws, lane2 * 16, goff + ((ks + w) & 3) * 1024, AUX_SC1);
      if (!__any(max(max(umax4(ob[0]), umax4(ob[1])), max(umax4(ob[2]), umax4(ob[3]))) == SENT) || spins >= kSpinCap) break;
      __builtin_amdgcn_s_sleep(2);
    }
#pragma unroll
    for (int hf = 0; hf < 2; ++hf) {
      f32x4 c[4];
#pragma unroll
      for (int e = 0; e < 4; ++e) c[e] = f32x4{0.f, 0.f, 0.f, 0.f};
#pragma unroll
      for (int ks = 0; ks < 4; ++ks)
#pragma unroll
        for (int e = 0; e < 4; ++e) c[e] = mfma16(wf[ks][4 * hf + e], ob[ks], c[e]);
#pragma unroll
      for (int rho = 0; rho < 4; ++rho)
        *(LAS f32x4*)(LW + L_C + (((lane2 & 15) * 512 + (lane2 >> 4) * 128 + (lane2 & 7) * 16) ^ (rho * 32 + hf * 16))) =
            f32x4{c[0][rho], c[1][rho], c[2][rho], c[3][rho]};
    }
  }
  LDS_BAR();
  {
    const int row = 2 * w + (lane2 >> 5);
    const int so = row * 512 + (((lane2 & 31) * 16) ^ ((row & 7) * 16));
    f32x4 pa[8];
#pragma unroll
    for (int ww = 0; ww < 8; ++ww) pa[ww] = *(const LAS f32x4*)(L + ww * L_WAVE + L_C + so);
    const f32x4 s = ((pa[0] + pa[1]) + (pa[2] + pa[3])) + ((pa[4] + pa[5]) + (pa[6] + pa[7]));
    v4u sv = __builtin_bit_cast(v4u, s);
#pragma unroll
    for (int j = 0; j < 4; ++j) sv[j] = (sv[j] == SENT) ? 0x7FC00000u : sv[j];
    const int p_own2 = ((lane2 >> 3) & 3) * 8192 + (lane2 >> 5) * 128 + (lane2 & 7) * 16;
    __builtin_amdgcn_raw_buffer_store_b128(sv, rws, p_own2, p_base + kq * 2048 + w * 256, AUX_SC1);
  }
  {
    const int poff = (lane2 >> 4) * 2048 + (lane2 & 15) * 16;
    int oo = ((lane2 >> 3) & 1) * (DM * 2) + (lane2 & 7) * 8 + (lane2 >> 5) * 4 + (lane2 & 16) * 0x1000000;
    asm volatile("" : "+v"(oo));
    v4u pv;
    __builtin_amdgcn_s_sleep(8);
    for (unsigned spins = 0;; ++spins) {
      MEMFENCE();
      pv = __builtin_amdgcn_raw_buffer_load_b128(rws, poff, p_base + kq * 8192 + w * 256, AUX_SC1);
      if (!__any(umax4(pv) == SENT) || spins >= kSpinCap) break;
      __builtin_amdgcn_s_sleep(2);
    }
    const float u0 = rs32(__uint_as_float(pv[0]), __uint_as_float(pv[2])), u1 = rs32(__uint_as_float(pv[1]), __uint_as_float(pv[3]));
    const float t0 = rs16(u0, u0), t1 = rs16(u1, u1);
    __builtin_amdgcn_raw_buffer_store_b32(cvt_pk(t0, t1), rout, oo, out_off, 0);
  }
}

}

extern "C" void kernel_launch(void* const* d_in, const int* in_sizes, int n_in, void* d_out, int out_size, void* d_ws,
                              size_t ws_size, hipStream_t stream) {
  (void)in_sizes; (void)n_in; (void)out_size; (void)ws_size;
  (void)hipFuncSetAttribute((const void*)mha_fused, hipFuncAttributeMaxDynamicSharedMemorySize, L_BYTES);
  hipLaunchKernelGGL(mha_fused, dim3(NWG), dim3(NTHR), L_BYTES, stream, (const unsigned short*)d_in[0], (const unsigned short*)d_in[1],
                     (const unsigned short*)d_in[2], (const unsigned short*)d_in[3], (const unsigned short*)d_in[4],
                     (unsigned short*)d_out, (unsigned char*)d_ws);
}
```

```cpp
#include <hip/hip_runtime.h>
#include <hip/hip_bf16.h>

#define LAS __attribute__((address_space(3)))
typedef unsigned v4u __attribute__((ext_vector_type(4)));
typedef unsigned v2u __attribute__((ext_vector_type(2)));
typedef float f32x4 __attribute__((ext_vector_type(4)));
typedef __bf16 bf16x8 __attribute__((ext_vector_type(8)));
typedef __bf16 bf16x2 __attribute__((ext_vector_type(2)));
typedef float f32x2 __attribute__((ext_vector_type(2)));

namespace {

constexpr int NB = 16, SQ = 8, DM = 1024, NH = 32, SK = 2048, DH = 128;
constexpr int NWG = 256, NTHR = 512;
constexpr int NT = 16;
constexpr float CEXP = 0.08838834764831845f * 1.4426950408889634f;
constexpr int AUX_NT = 2, AUX_SC1 = 16;
constexpr int RSRC_W3 = 0x00020000;
constexpr unsigned SENT = 0xFFFFFFFFu;
constexpr unsigned kSpinCap = 1u << 16;

constexpr int PACE_C = 449;
constexpr int PACE_DZ = 16, PACE_S0 = 4;
constexpr int PACE_WO = 150;
constexpr int PACE_MAX = 500;
constexpr int WO_S0 = 2;

constexpr int WS_EPOCH = 0;
constexpr int WS_SPARE = 2048;
constexpr int WS_FLAG = 4096;
constexpr int WS_PACE = WS_FLAG + 32768;
constexpr int WS_O = 131072;
constexpr int WS_P = WS_O + 8 * 128 * 1024;
constexpr int WS_BYTES = WS_P + 8 * 8 * 16 * 2048;

constexpr int L_WAVE = 16384;
constexpr int L_QS = 8 * L_WAVE;
constexpr int QS_STRIDE = 272;
constexpr int L_ML = L_QS + 16 * QS_STRIDE;
constexpr int L_PC = L_ML + 1024;
constexpr int L_BYTES = L_PC + 16;
constexpr int QP_STRIDE = 528;
constexpr int L_X = 9216, X_ODD = 2064;
constexpr int L_C = 8192;

__device__ __forceinline__ f32x4 mfma16(v4u a, v4u b, f32x4 c) {
  return __builtin_amdgcn_mfma_f32_16x16x32_bf16(__builtin_bit_cast(bf16x8, a), __builtin_bit_cast(bf16x8, b), c, 0, 0, 0);
}
__device__ __forceinline__ unsigned pack_lo(unsigned a, unsigned b) { return __builtin_amdgcn_perm(b, a, 0x05040100u); }
__device__ __forceinline__ unsigned pack_hi(unsigned a, unsigned b) { return __builtin_amdgcn_perm(b, a, 0x07060302u); }
__device__ __forceinline__ unsigned cvt_pk(float lo, float hi) {
  f32x2 v = {lo, hi};
  return __builtin_bit_cast(unsigned, __builtin_convertvector(v, bf16x2));
}
__device__ __forceinline__ v4u tr_frag(const v4u (&R)[8], int e) {
  v4u f;
#pragma unroll
  for (int jj = 0; jj < 4; ++jj)
    f[jj] = (e & 1) ? pack_hi(R[2 * jj][e >> 1], R[2 * jj + 1][e >> 1]) : pack_lo(R[2 * jj][e >> 1], R[2 * jj + 1][e >> 1]);
  return f;
}
template <int CTRL> __device__ __forceinline__ int dppi(int x) { return __builtin_amdgcn_mov_dpp(x, CTRL, 0xf, 0xf, true); }
__device__ __forceinline__ int sum8(int v) { v += dppi<0xB1>(v); v += dppi<0x4E>(v); return v + dppi<0x141>(v); }
__device__ __forceinline__ float ror8(float x) {
  return __builtin_bit_cast(float, dppi<0x128>(__builtin_bit_cast(int, x)));
}
__device__ __forceinline__ float bsel(unsigned mask, float a, float b) {
  return __uint_as_float((__float_as_uint(a) & mask) | (__float_as_uint(b) & ~mask));
}
__device__ __forceinline__ float xg_max(float x) {
  auto s = __builtin_amdgcn_permlane16_swap(__float_as_uint(x), __float_as_uint(x), false, false);
  x = fmaxf(__uint_as_float(s[0]), __uint_as_float(s[1]));
  auto t = __builtin_amdgcn_permlane32_swap(__float_as_uint(x), __float_as_uint(x), false, false);
  return fmaxf(__uint_as_float(t[0]), __uint_as_float(t[1]));
}
__device__ __forceinline__ float rs32(float a, float b) {
  auto t = __builtin_amdgcn_permlane32_swap(__float_as_uint(a), __float_as_uint(b), false, false);
  return __uint_as_float(t[0]) + __uint_as_float(t[1]);
}
__device__ __forceinline__ float rs16(float a, float b) {
  auto t = __builtin_amdgcn_permlane16_swap(__float_as_uint(a), __float_as_uint(b), false, false);
  return __uint_as_float(t[0]) + __uint_as_float(t[1]);
}
__device__ __forceinline__ float xg_sum(float x) { x = rs16(x, x); return rs32(x, x); }
__device__ __forceinline__ unsigned umax4(v4u v) { return max(max(v[0], v[1]), max(v[2], v[3])); }
__device__ __forceinline__ unsigned now32() { return (unsigned)__builtin_amdgcn_s_memrealtime(); }
__device__ __forceinline__ void hold_until(unsigned t_abs) {
  const unsigned now = now32();
  int d = min((int)(t_abs - now), PACE_MAX);
  const unsigned lim = now + (unsigned)d;
  while (d > 0) { __builtin_amdgcn_s_sleep(1); d = (int)(lim - now32()); }
}
__device__ __forceinline__ void store128_sc1(v4u v, __amdgpu_buffer_rsrc_t rs, int voffset, int soffset) {
  asm volatile("" : "+v"(v));
  __builtin_amdgcn_raw_buffer_store_b128(v, rs, voffset, soffset, 16);
  __builtin_amdgcn_sched_barrier(0);
  asm volatile("s_nop 2" :: "v"(v));
  __builtin_amdgcn_sched_barrier(0);
}
#define SB() __builtin_amdgcn_sched_barrier(0)
#define BAR() __builtin_amdgcn_s_barrier()
#define MEMFENCE() asm volatile("" ::: "memory")
#define LDS_BAR() { asm volatile("s_waitcnt lgkmcnt(0)" ::: "memory"); BAR(); MEMFENCE(); }

template <int V> struct IC { static constexpr int value = V; };
template <int I, int N, class F> __device__ __forceinline__ void static_for(F&& f) {
  if constexpr (I < N) { f(IC<I>{}); static_for<I + 1, N>(f); }
}
constexpr int next_after(int n) { return n < 2 ? n + 3 : (n == 2 || n + 2 >= 2 * NT) ? -1 : n + 2; }
constexpr bool pace_at(int t) { return t == 1 || t == 4 || t == 7 || t == 10; }

__global__ void __launch_bounds__(NTHR, 2)
mha_fused(const unsigned short* __restrict__ x, const unsigned short* __restrict__ kc, const unsigned short* __restrict__ vc,
          const unsigned short* __restrict__ wq, const unsigned short* __restrict__ wo, unsigned short* out, unsigned char* ws) {
  extern __shared__ __attribute__((aligned(16))) unsigned char smem[];
  LAS unsigned char* L = (LAS unsigned char*)smem;
  const int tid = threadIdx.x, lane = tid & 63, w = __builtin_amdgcn_readfirstlane(tid >> 6);
  const int r = lane & 15, g = lane >> 4;
  const int wg = blockIdx.x, idx = wg >> 3, xg = wg & 7, i4 = idx & 3;
  const int h = 4 * xg + i4, bp = idx >> 2;
  const int kq = xg >> 1, ct = 4 * (xg & 1) + ((i4 + 2) & 3);
  const int beta = w >> 2, ws4 = w & 3;
  const int bh = (2 * bp + beta) * NH + h;
  int rot = (4 * h + bp + 8 * beta) & (NT - 1);
  int rotv = rot;
  asm volatile("" : "+s"(rotv));
  LAS unsigned char* LW = L + w * L_WAVE;

  asm volatile("" : "+s"(out));
  const __amdgpu_buffer_rsrc_t rk = __builtin_amdgcn_make_buffer_rsrc((void*)(kc + (size_t)bh * SK * DH), 0, SK * DH * 2, RSRC_W3);
  const __amdgpu_buffer_rsrc_t rv = __builtin_amdgcn_make_buffer_rsrc((void*)(vc + (size_t)bh * SK * DH), 0, SK * DH * 2, RSRC_W3);
  const __amdgpu_buffer_rsrc_t rws = __builtin_amdgcn_make_buffer_rsrc((void*)ws, 0, WS_BYTES, RSRC_W3);
  const __amdgpu_buffer_rsrc_t rw0 = __builtin_amdgcn_make_buffer_rsrc((void*)ws, 0, w == 0 ? WS_BYTES : 0, RSRC_W3);
  const __amdgpu_buffer_rsrc_t rwq = __builtin_amdgcn_make_buffer_rsrc((void*)wq, 0, DM * NH * DH * 2, RSRC_W3);
  const __amdgpu_buffer_rsrc_t rwo = __builtin_amdgcn_make_buffer_rsrc((void*)wo, 0, NH * DH * DM * 2, RSRC_W3);
  const __amdgpu_buffer_rsrc_t rx = __builtin_amdgcn_make_buffer_rsrc((void*)x, 0, NB * SQ * DM * 2, RSRC_W3);
  const __amdgpu_buffer_rsrc_t rout = __builtin_amdgcn_make_buffer_rsrc((void*)out, 0, NB * SQ * DM * 2, RSRC_W3);

  const int koff = (4 * ((r >> 1) & 1) + (r >> 2)) * 256 + (2 * g + (r & 1)) * 16;
  const int voff = lane * 16;
  v4u item[2 * NT][8];
#define LOAD_ITEM(n) { const int so_ = ((((n) + ((n) >= NT ? rotv : rot)) & (NT - 1)) * 4 + ws4) * 8192; _Pragma("unroll") for (int i_ = 0; i_ < 8; ++i_) \
    item[n][i_] = ((n) >= NT) ? __builtin_amdgcn_raw_buffer_load_b128(rv, voff, so_ + i_ * 1024, AUX_NT) \
                            : __builtin_amdgcn_raw_buffer_load_b128(rk, koff, so_ + (i_ >> 1) * 2048 + (i_ & 1) * 128, AUX_NT); }

  const int o_slot = WS_O + (bp * 128 + 4 * h) * 1024;
  const int p_base = WS_P + (bp * 8 + ct) * (16 * 2048);
  const int out_off = ((16 * bp + 2 * w) * DM + 128 * ct + 32 * kq) * 2;

  unsigned tch[5];
  {
    typedef const __attribute__((address_space(4))) unsigned* cptr;
    tch[0] = *(cptr)(kc + (size_t)bh * SK * DH);
    tch[1] = *(cptr)(vc + (size_t)bh * SK * DH);
    tch[2] = *(cptr)(wo + (size_t)(1024 * kq + 128 * w) * DM);
    tch[3] = *(cptr)(ws + WS_SPARE + w * 128);
    tch[4] = *(cptr)(ws + WS_BYTES + w * 128);
    SB();
  }
  {
#define KBLK(s) ((((4 * beta + (s) + bp) & 7) << 2) | ws4)
    const int wqoff = (8 * g) * (NH * DH * 2) + r * 16;
    v4u wa[4][8], xr[4];
#define LOAD_WQ(s) { const int so_ = KBLK(s) * 32 * (NH * DH * 2) + h * (DH * 2); _Pragma("unroll") for (int j_ = 0; j_ < 8; ++j_) \
      wa[s][j_] = __builtin_amdgcn_raw_buffer_load_b128(rwq, wqoff, so_ + j_ * (NH * DH * 2), 0); }
#define LOAD_X() { _Pragma("unroll") for (int i_ = 0; i_ < 4; ++i_) \
      xr[i_] = __builtin_amdgcn_raw_buffer_load_b128(rx, voff, (16 * bp + 2 * w) * (DM * 2) + i_ * 1024, 0); }
#define STAGE_X() { _Pragma("unroll") for (int i_ = 0; i_ < 4; ++i_) \
      *(LAS v4u*)(LW + L_X + w * 32 + (i_ >> 1) * X_ODD + (i_ & 1) * 1024 + voff) = xr[i_]; }
    const LAS unsigned char* xrow = L + (r >> 1) * (L_WAVE + 32) + L_X + (r & 1) * X_ODD + g * 16;
    f32x4 qa[8];
#pragma unroll
    for (int e = 0; e < 8; ++e) qa[e] = f32x4{0.f, 0.f, 0.f, 0.f};
#define MMA_WQ(s) { const v4u xf_ = *(const LAS v4u*)(xrow + KBLK(s) * 64); \
      _Pragma("unroll") for (int e_ = 0; e_ < 8; ++e_) qa[e_] = mfma16(tr_frag(wa[s], e_), xf_, qa[e_]); }
#define PUT_Q() { _Pragma("unroll") for (int rho = 0; rho < 4; ++rho) { \
      LAS f32x4* p_ = (LAS f32x4*)(LW + r * QP_STRIDE + (32 * g + 8 * rho) * 4); \
      p_[0] = f32x4{qa[0][rho], qa[1][rho], qa[2][rho], qa[3][rho]}; \
      p_[1] = f32x4{qa[4][rho], qa[5][rho], qa[6][rho], qa[7][rho]}; } }
#define REDUCE_Q() { const int row_ = tid >> 5, n4_ = (tid & 31) * 4; f32x4 pa_[8]; \
      _Pragma("unroll") for (int ww = 0; ww < 8; ++ww) pa_[ww] = *(LAS f32x4*)(L + ww * L_WAVE + row_ * QP_STRIDE + n4_ * 4); \
      const f32x4 s_ = ((pa_[0] + pa_[1]) + (pa_[2] + pa_[3])) + ((pa_[4] + pa_[5]) + (pa_[6] + pa_[7])); \
      *(LAS v2u*)(L + L_QS + row_ * QS_STRIDE + n4_ * 2) = v2u{cvt_pk(s_[0], s_[1]), cvt_pk(s_[2], s_[3])}; }
    if ((bp & 1) == 0) {
      asm volatile("; path A" ::: "memory");
      LOAD_X(); SB(); LOAD_WQ(0); SB(); LOAD_WQ(1); SB(); LOAD_WQ(2); SB(); LOAD_WQ(3); SB();
      STAGE_X(); SB();
      LDS_BAR(); SB();
      LOAD_ITEM(0); SB();
      MMA_WQ(0); MMA_WQ(1); MMA_WQ(2); MMA_WQ(3);
      PUT_Q(); SB();
      LOAD_ITEM(1); SB();
      LDS_BAR(); SB();
      REDUCE_Q(); SB();
      LOAD_ITEM(2); SB();
    } else {
      asm volatile("; path B" ::: "memory");
      LOAD_X(); SB(); LOAD_WQ(0); SB();
      LOAD_ITEM(0); SB(); LOAD_ITEM(1); SB();
      STAGE_X(); SB();
      LDS_BAR(); SB();
      LOAD_WQ(1); SB(); LOAD_WQ(2); SB();
      MMA_WQ(0); SB();
      LOAD_WQ(3); SB();
      MMA_WQ(1); SB();
      LOAD_ITEM(2); SB();
      MMA_WQ(2); MMA_WQ(3);
      PUT_Q(); SB();
      LDS_BAR(); SB();
      REDUCE_Q(); SB();
    }
  }
  asm volatile("" :: "s"(tch[0]), "s"(tch[1]), "s"(tch[2]), "s"(tch[3]), "s"(tch[4]));
  LDS_BAR();
  asm volatile("" : "+s"(rot));
  v4u qf[2];
#pragma unroll
  for (int hf = 0; hf < 2; ++hf) qf[hf] = *(LAS v4u*)(L + L_QS + (8 * beta + (r & 7)) * QS_STRIDE + (8 * hf + 2 * g + (r >> 3)) * 16);
  unsigned hmask = (r >> 3) ? 0xFFFFFFFFu : 0u;
  asm volatile("" : "+v"(hmask));
  const unsigned psel0 = (hmask & 0x01000C0Cu) | (~hmask & 0x0C0C0100u), psel1 = (hmask & 0x03020C0Cu) | (~hmask & 0x0C0C0302u);

  v4u wr[4][8], wf[4][8];
  const int wooff = (32 * g) * (DM * 2) + r * 16;
  const int wos = (1024 * kq + 128 * w) * (DM * 2) + ct * 256;
#define LOAD_WO(ks) { _Pragma("unroll") for (int j_ = 0; j_ < 8; ++j_) \
    wr[ks][j_] = __builtin_amdgcn_raw_buffer_load_b128(rwo, wooff, wos + (8 * (j_ & 3) + 2 * (((ks) + w) & 3) + (j_ >> 2)) * (DM * 2), 0); }
#define PARK_WO(ks) { _Pragma("unroll") for (int e_ = 0; e_ < 8; ++e_) *(LAS v4u*)(LW + (ks) * 8192 + e_ * 1024 + voff) = tr_frag(wr[ks], e_); }
#define KEEP_WO(ks) { _Pragma("unroll") for (int e_ = 0; e_ < 8; ++e_) { wf[ks][e_] = tr_frag(wr[ks], e_); asm volatile("" : "+v"(wf[ks][e_])); } }
#define BACK_WO(ks) { _Pragma("unroll") for (int e_ = 0; e_ < 8; ++e_) wf[ks][e_] = *(LAS v4u*)(LW + (ks) * 8192 + e_ * 1024 + voff); }
#define SCORES(n, s) { f32x4 sa_[4]; \
    _Pragma("unroll") for (int u = 0; u < 4; ++u) sa_[u] = mfma16(item[n][2 * u], qf[0], f32x4{0.f, 0.f, 0.f, 0.f}); \
    _Pragma("unroll") for (int u = 0; u < 4; ++u) sa_[u] = mfma16(item[n][2 * u + 1], qf[1], sa_[u]); \
    _Pragma("unroll") for (int u = 0; u < 4; ++u) (s)[u] = bsel(hmask, sa_[u][3], sa_[u][0]) + ror8(bsel(hmask, sa_[u][1], sa_[u][2])); \
    asm volatile("" : "+v"((s)[0]), "+v"((s)[1]), "+v"((s)[2]), "+v"((s)[3])); }

  f32x4 oacc[8];
  float sc[NT][4];
  unsigned pk[NT][2];
  v2u epoch0, flags_v;
  unsigned tag_lo = 0, tag_hi = 0;
  v4u snap;
  const unsigned tbase = now32();
  unsigned line0 = 0, line1 = 0, tprev = 0;
#define PV(n) { const v4u pf_ = {__builtin_amdgcn_perm(0u, pk[(n) - NT][0], psel0), __builtin_amdgcn_perm(0u, pk[(n) - NT][0], psel1), \
                                 __builtin_amdgcn_perm(0u, pk[(n) - NT][1], psel0), __builtin_amdgcn_perm(0u, pk[(n) - NT][1], psel1)}; \
    _Pragma("unroll") for (int e = 0; e < 8; ++e) oacc[e] = mfma16(tr_frag(item[n], e), pf_, (n) == NT ? f32x4{0.f, 0.f, 0.f, 0.f} : oacc[e]); \
    asm volatile("" : "+v"(oacc[0]), "+v"(oacc[1]), "+v"(oacc[2]), "+v"(oacc[3]), "+v"(oacc[4]), "+v"(oacc[5]), "+v"(oacc[6]), "+v"(oacc[7])); }
#define CONSUME(n) { if constexpr ((n) < NT) SCORES(n, sc[(n) < NT ? (n) : 0]) else PV((n) < NT ? NT : (n)) }

  static_for<0, NT>([&](auto tc) __attribute__((always_inline)) {
    constexpr int t = decltype(tc)::value;
    CONSUME(2 * t);
    SB();
    if constexpr (t == 0) {
      epoch0 = __builtin_amdgcn_raw_buffer_load_b64(rw0, 0, WS_EPOCH + bp * 256, AUX_SC1);
      const int p_own = ((voff >> 7) & 3) * 8192 + (voff >> 9) * 128 + (voff & 0x70);
      __builtin_amdgcn_raw_buffer_store_b64(v2u{SENT, SENT}, rws, voff >> 1, o_slot + w * 512, AUX_SC1);
      store128_sc1(v4u{SENT, SENT, SENT, SENT}, rws, p_own, p_base + kq * 2048 + w * 256);
      MEMFENCE();
      SB();
    }
    if constexpr (t == NT - 5) flags_v = __builtin_amdgcn_raw_buffer_load_b64(rw0, (voff << 3) & 0xF80, WS_FLAG + bp * 4096, AUX_SC1);
    if constexpr (pace_at(t - 2)) {
      const int dt = (int)(snap[0] - tbase);
      const bool ok = snap[3] == ~(snap[0] ^ snap[1] ^ snap[2]) && dt > -1500 && dt < 12000;
      const int rs = ok ? (int)snap[1] : 0;
      const int nr = __builtin_popcount((unsigned)__builtin_amdgcn_ballot_w64(rs > 0) & 0xFFu);
      const int cm = (int)((float)__builtin_amdgcn_readfirstlane(sum8(rs)) * __builtin_amdgcn_rcpf((float)max(nr, 1)));
      const int cu = cm > PACE_C + PACE_DZ ? min(cm - PACE_S0, PACE_C + 100) : PACE_C;
      const int v = sum8(dt - (int)snap[2] * cu) >> 3;
      const bool all_ok = (__builtin_amdgcn_ballot_w64(ok) & 0xFFull) == 0xFFull;
      if (w == 0) *(LAS v2u*)(L + L_PC) = v2u{tbase + (unsigned)(all_ok ? v : -1000000), (unsigned)cu};
    }
    if constexpr (t >= 4 && next_after(2 * t) >= 0) hold_until(line0 + ((16 * t - 7 + w) * line1 >> 4));
    if constexpr (t >= WO_S0 && t < WO_S0 + 4) LOAD_WO(t - WO_S0);
    if constexpr (pace_at(t - 1)) {
      const int px = (voff >> 4) & 7;
      const int peer = (((bp + px) & 7) * 4 + ((i4 + px) & 3)) * 8 + ((xg + px) & 7);
      snap = __builtin_amdgcn_raw_buffer_load_b128(rw0, peer * 128, WS_PACE, AUX_SC1);
      SB();
    }
    if constexpr (next_after(2 * t) >= 0) LOAD_ITEM(next_after(2 * t));
    SB();
    if constexpr (pace_at(t - 2)) asm volatile("s_waitcnt lgkmcnt(0)" ::: "memory");
    if constexpr (t == NT - 3) {
      if (w == 0) {
        if (__builtin_expect(!__all(flags_v[0] == tag_lo && flags_v[1] == tag_hi), 0)) {
          for (unsigned spins = 0; spins < kSpinCap; ++spins) {
            __builtin_amdgcn_s_sleep(2);
            MEMFENCE();
            const v2u fv = __builtin_amdgcn_raw_buffer_load_b64(rws, (voff << 3) & 0xF80, WS_FLAG + bp * 4096, AUX_SC1);
            if (__all(fv[0] == tag_lo && fv[1] == tag_hi)) break;
          }
        }
        if (h == 0) __builtin_amdgcn_raw_buffer_store_b64(v2u{tag_lo, tag_hi}, rws, (voff >> 1) & 0x78, WS_EPOCH + bp * 256, AUX_SC1);
      }
      MEMFENCE();
      BAR();
      MEMFENCE();
    } else if constexpr (t < NT - 2) BAR();
    if constexpr (pace_at(t - 2)) {
      MEMFENCE();
      const v2u ln = *(LAS v2u*)(L + L_PC);
      line0 = __builtin_amdgcn_readfirstlane(ln[0]); line1 = __builtin_amdgcn_readfirstlane(ln[1]);
    }
    if constexpr (pace_at(t)) {
      const unsigned tv = now32();
      const unsigned rate = t >= 7 ? (tv - tprev - PACE_WO) / (unsigned)(t > 1 ? t - 1 : 1) : 0u;
      if constexpr (t == 1) tprev = tv;
      store128_sc1(v4u{tv, rate, (unsigned)t, ~(tv ^ rate ^ (unsigned)t)}, rw0, voff & 0x70, WS_PACE + wg * 128);
    }
    if constexpr (t == NT - 1) BACK_WO(1);
    SB();
    CONSUME(2 * t + 1);
    SB();
    if constexpr (t == WO_S0 + 1) PARK_WO(0);
    if constexpr (t == WO_S0 + 2) PARK_WO(1);
    if constexpr (t == WO_S0 + 3) KEEP_WO(2);
    if constexpr (t == WO_S0 + 4) KEEP_WO(3);
    SB();
    if constexpr (t >= 3 && t < NT - 1) hold_until(line0 + ((16 * t + 1 + w) * line1 >> 4));
    if constexpr (next_after(2 * t + 1) >= 0) LOAD_ITEM(next_after(2 * t + 1));
    SB();
    if constexpr (t == NT - 2) { BACK_WO(0); SB(); }
    if constexpr (t == NT / 2 - 1) {
      float m = fmaxf(fmaxf(sc[0][0], sc[0][1]), fmaxf(sc[0][2], sc[0][3]));
#pragma unroll
      for (int k = 1; k < NT; ++k) m = fmaxf(fmaxf(fmaxf(m, sc[k][0]), sc[k][1]), fmaxf(sc[k][2], sc[k][3]));
      m = xg_max(m);
      const float mc = m * CEXP;
      float l0 = 0.f, l1 = 0.f;
#pragma unroll
      for (int k = 0; k < NT; ++k) {
#pragma unroll
        for (int u = 0; u < 4; ++u) sc[k][u] = __builtin_amdgcn_exp2f(__builtin_fmaf(sc[k][u], CEXP, -mc));
        l0 += sc[k][0] + sc[k][1]; l1 += sc[k][2] + sc[k][3];
      }
      const float lsum = xg_sum(l0 + l1);
      LAS unsigned char* ml = L + L_ML + beta * 512 + ((voff >> 4) & 7) * 64;
      if (voff < 256) *(LAS f32x2*)(ml + ws4 * 16 + (voff >> 7) * 8) = f32x2{mc, lsum};
      asm volatile("s_waitcnt vmcnt(0)" ::: "memory");
      LDS_BAR();
      {
        const unsigned long long tag64 = (((unsigned long long)(unsigned)__builtin_amdgcn_readfirstlane(epoch0[1]) << 32) |
                                          (unsigned)__builtin_amdgcn_readfirstlane(epoch0[0])) + 1ull;
        tag_lo = (unsigned)tag64; tag_hi = (unsigned)(tag64 >> 32);
        __builtin_amdgcn_raw_buffer_store_b64(v2u{tag_lo, tag_hi}, rw0, (voff >> 1) & 0x78, WS_FLAG + (bp * 32 + h) * 128, AUX_SC1);
      }
      f32x4 rec[4];
#pragma unroll
      for (int j = 0; j < 4; ++j) rec[j] = *(LAS f32x4*)(ml + j * 16);
      const float M = fmaxf(fmaxf(fmaxf(rec[0][0], rec[0][2]), fmaxf(rec[1][0], rec[1][2])), fmaxf(fmaxf(rec[2][0], rec[2][2]), fmaxf(rec[3][0], rec[3][2])));
      float Ls = 0.f;
#pragma unroll
      for (int j = 0; j < 4; ++j) Ls += __builtin_amdgcn_exp2f(rec[j][0] - M) * rec[j][1] + __builtin_amdgcn_exp2f(rec[j][2] - M) * rec[j][3];
      const float scale = __builtin_amdgcn_exp2f(mc - M) / Ls;
#pragma unroll
      for (int k = 0; k < NT; ++k) {
        pk[k][0] = cvt_pk(sc[k][0] * scale, sc[k][1] * scale); pk[k][1] = cvt_pk(sc[k][2] * scale, sc[k][3] * scale);
        asm volatile("" : "+v"(pk[k][0]), "+v"(pk[k][1]));
      }
      SB();
    }
  });
  const int lane2 = __builtin_amdgcn_mbcnt_hi(~0u, __builtin_amdgcn_mbcnt_lo(~0u, 0u));
  asm volatile("s_waitcnt lgkmcnt(0)" ::: "memory");
#pragma unroll
  for (int e = 0; e < 8; ++e) *(LAS f32x4*)(LW + e * 1024 + lane2 * 16) = oacc[e];
  LDS_BAR();
  {
    const int fl = 32 * (w & 1) + (lane2 >> 1);
    const LAS unsigned char* src = L + ((fl >> 3) & 1) * (4 * L_WAVE) + ((w & ~1) + (lane2 & 1)) * 1024 + (fl & 0x37) * 16;
    const int par = (lane2 & 1) * 128;
    f32x4 pa[8];
#pragma unroll
    for (int j = 0; j < 8; ++j) pa[j] = *(const LAS f32x4*)(src + (j >> 1) * L_WAVE + ((j & 1) ? 128 - par : par));
    const f32x4 a0 = ((pa[0] + pa[1]) + (pa[2] + pa[3])) + ((pa[4] + pa[5]) + (pa[6] + pa[7]));
    v2u ov = {cvt_pk(a0[0], a0[1]), cvt_pk(a0[2], a0[3])};
#pragma unroll
    for (int j = 0; j < 2; ++j) ov[j] = (ov[j] == SENT) ? 0x7FC07FC0u : ov[j];
    __builtin_amdgcn_raw_buffer_store_b64(ov, rws, lane2 * 8, o_slot + w * 512, AUX_SC1);
  }
  SB();
  {
    const int goff = WS_O + (bp * 128 + 4 * (8 * kq + w)) * 1024;
    unsigned spins = 0;
#pragma unroll 1
    for (; spins < kSpinCap; ++spins) {
      MEMFENCE();
      if (__any(__builtin_amdgcn_raw_buffer_load_b32(rws, 0, goff, AUX_SC1) != SENT)) break;
      __builtin_amdgcn_s_sleep(1);
    }
    v4u ob[4];
    for (;; ++spins) {
      MEMFENCE();
#pragma unroll
      for (int ks = 0; ks < 4; ++ks) ob[ks] = __builtin_amdgcn_raw_buffer_load_b128(rws, lane2 * 16, goff + ((ks + w) & 3) * 1024, AUX_SC1);
      if (!__any(max(max(umax4(ob[0]), umax4(ob[1])), max(umax4(ob[2]), umax4(ob[3]))) == SENT) || spins >= kSpinCap) break;
      __builtin_amdgcn_s_sleep(2);
    }
#pragma unroll
    for (int hf = 0; hf < 2; ++hf) {
      f32x4 c[4];
#pragma unroll
      for (int e = 0; e < 4; ++e) c[e] = f32x4{0.f, 0.f, 0.f, 0.f};
#pragma unroll
      for (int ks = 0; ks < 4; ++ks)
#pragma unroll
        for (int e = 0; e < 4; ++e) c[e] = mfma16(wf[ks][4 * hf + e], ob[ks], c[e]);
#pragma unroll
      for (int rho = 0; rho < 4; ++rho)
        *(LAS f32x4*)(LW + L_C + (((lane2 & 15) * 512 + (lane2 >> 4) * 128 + (lane2 & 7) * 16) ^ (rho * 32 + hf * 16))) =
            f32x4{c[0][rho], c[1][rho], c[2][rho], c[3][rho]};
    }
  }
  LDS_BAR();
  {
    const int row = 2 * w + (lane2 >> 5);
    const int so = row * 512 + (((lane2 & 31) * 16) ^ ((row & 7) * 16));
    f32x4 pa[8];
#pragma unroll
    for (int ww = 0; ww < 8; ++ww) pa[ww] = *(const LAS f32x4*)(L + ww * L_WAVE + L_C + so);
    const f32x4 s = ((pa[0] + pa[1]) + (pa[2] + pa[3])) + ((pa[4] + pa[5]) + (pa[6] + pa[7]));
    v4u sv = __builtin_bit_cast(v4u, s);
#pragma unroll
    for (int j = 0; j < 4; ++j) sv[j] = (sv[j] == SENT) ? 0x7FC00000u : sv[j];
    const int p_own2 = ((lane2 >> 3) & 3) * 8192 + (lane2 >> 5) * 128 + (lane2 & 7) * 16;
    store128_sc1(sv, rws, p_own2, p_base + kq * 2048 + w * 256);
  }
  {
    const int poff = (lane2 >> 4) * 2048 + (lane2 & 15) * 16;
    int oo = ((lane2 >> 3) & 1) * (DM * 2) + (lane2 & 7) * 8 + (lane2 >> 5) * 4 + (lane2 & 16) * 0x1000000;
    asm volatile("" : "+v"(oo));
    v4u pv;
    __builtin_amdgcn_s_sleep(8);
    for (unsigned spins = 0;; ++spins) {
      MEMFENCE();
      pv = __builtin_amdgcn_raw_buffer_load_b128(rws, poff, p_base + kq * 8192 + w * 256, AUX_SC1);
      if (!__any(umax4(pv) == SENT) || spins >= kSpinCap) break;
      __builtin_amdgcn_s_sleep(2);
    }
    const float u0 = rs32(__uint_as_float(pv[0]), __uint_as_float(pv[2])), u1 = rs32(__uint_as_float(pv[1]), __uint_as_float(pv[3]));
    const float t0 = rs16(u0, u0), t1 = rs16(u1, u1);
    __builtin_amdgcn_raw_buffer_store_b32(cvt_pk(t0, t1), rout, oo, out_off, 0);
  }
}

}

extern "C" void kernel_launch(void* const* d_in, const int* in_sizes, int n_in, void* d_out, int out_size, void* d_ws,
                              size_t ws_size, hipStream_t stream) {
  (void)in_sizes; (void)n_in; (void)out_size; (void)ws_size;
  (void)hipFuncSetAttribute((const void*)mha_fused, hipFuncAttributeMaxDynamicSharedMemorySize, L_BYTES);
  hipLaunchKernelGGL(mha_fused, dim3(NWG), dim3(NTHR), L_BYTES, stream, (const unsigned short*)d_in[0], (const unsigned short*)d_in[1],
                     (const unsigned short*)d_in[2], (const unsigned short*)d_in[3], (const unsigned short*)d_in[4],
                     (unsigned short*)d_out, (unsigned char*)d_ws);
}
```

```cpp
#include <hip/hip_runtime.h>
#include <hip/hip_bf16.h>

#define LAS __attribute__((address_space(3)))
typedef unsigned v4u __attribute__((ext_vector_type(4)));
typedef unsigned v2u __attribute__((ext_vector_type(2)));
typedef float f32x4 __attribute__((ext_vector_type(4)));
typedef __bf16 bf16x8 __attribute__((ext_vector_type(8)));
typedef __bf16 bf16x2 __attribute__((ext_vector_type(2)));
typedef float f32x2 __attribute__((ext_vector_type(2)));

namespace {

constexpr int NB = 16, SQ = 8, DM = 1024, NH = 32, SK = 2048, DH = 128;
constexpr int NWG = 256, NTHR = 512;
constexpr int NT = 16;
constexpr float CEXP = 0.08838834764831845f * 1.4426950408889634f;
constexpr int AUX_NT = 2, AUX_SC1 = 16;
constexpr int RSRC_W3 = 0x00020000;
constexpr unsigned SENT = 0xFFFFFFFFu;
constexpr unsigned kSpinCap = 1u << 16;

constexpr int PACE_C = 449;
constexpr int PACE_DZ = 16, PACE_S0 = 4;
constexpr int PACE_WO = 60;
constexpr int PACE_MAX = 500;
constexpr int WO_S0 = 2;

constexpr int WS_EPOCH = 0;
constexpr int WS_SPARE = 2048;
constexpr int WS_FLAG = 4096;
constexpr int WS_PACE = WS_FLAG + 32768;
constexpr int WS_O = 131072;
constexpr int WS_P = WS_O + 8 * 128 * 1024;
constexpr int WS_BYTES = WS_P + 8 * 8 * 16 * 2048;

constexpr int L_WAVE = 16384;
constexpr int L_QS = 8 * L_WAVE;
constexpr int QS_STRIDE = 272;
constexpr int L_ML = L_QS + 16 * QS_STRIDE;
constexpr int L_PC = L_ML + 1024;
constexpr int L_BYTES = L_PC + 16;
constexpr int QP_STRIDE = 528;
constexpr int L_X = 9216, X_ODD = 2064;
constexpr int L_C = 8192;

__device__ __forceinline__ f32x4 mfma16(v4u a, v4u b, f32x4 c) {
  return __builtin_amdgcn_mfma_f32_16x16x32_bf16(__builtin_bit_cast(bf16x8, a), __builtin_bit_cast(bf16x8, b), c, 0, 0, 0);
}
__device__ __forceinline__ unsigned pack_lo(unsigned a, unsigned b) { return __builtin_amdgcn_perm(b, a, 0x05040100u); }
__device__ __forceinline__ unsigned pack_hi(unsigned a, unsigned b) { return __builtin_amdgcn_perm(b, a, 0x07060302u); }
__device__ __forceinline__ unsigned cvt_pk(float lo, float hi) {
  f32x2 v = {lo, hi};
  return __builtin_bit_cast(unsigned, __builtin_convertvector(v, bf16x2));
}
__device__ __forceinline__ v4u tr_frag(const v4u (&R)[8], int e) {
  v4u f;
#pragma unroll
  for (int jj = 0; jj < 4; ++jj)
    f[jj] = (e & 1) ? pack_hi(R[2 * jj][e >> 1], R[2 * jj + 1][e >> 1]) : pack_lo(R[2 * jj][e >> 1], R[2 * jj + 1][e >> 1]);
  return f;
}
template <int CTRL> __device__ __forceinline__ int dppi(int x) { return __builtin_amdgcn_mov_dpp(x, CTRL, 0xf, 0xf, true); }
__device__ __forceinline__ int sum8(int v) { v += dppi<0xB1>(v); v += dppi<0x4E>(v); return v + dppi<0x141>(v); }
__device__ __forceinline__ float ror8(float x) {
  return __builtin_bit_cast(float, dppi<0x128>(__builtin_bit_cast(int, x)));
}
__device__ __forceinline__ float bsel(unsigned mask, float a, float b) {
  return __uint_as_float((__float_as_uint(a) & mask) | (__float_as_uint(b) & ~mask));
}
__device__ __forceinline__ float xg_max(float x) {
  auto s = __builtin_amdgcn_permlane16_swap(__float_as_uint(x), __float_as_uint(x), false, false);
  x = fmaxf(__uint_as_float(s[0]), __uint_as_float(s[1]));
  auto t = __builtin_amdgcn_permlane32_swap(__float_as_uint(x), __float_as_uint(x), false, false);
  return fmaxf(__uint_as_float(t[0]), __uint_as_float(t[1]));
}
__device__ __forceinline__ float rs32(float a, float b) {
  auto t = __builtin_amdgcn_permlane32_swap(__float_as_uint(a), __float_as_uint(b), false, false);
  return __uint_as_float(t[0]) + __uint_as_float(t[1]);
}
__device__ __forceinline__ float rs16(float a, float b) {
  auto t = __builtin_amdgcn_permlane16_swap(__float_as_uint(a), __float_as_uint(b), false, false);
  return __uint_as_float(t[0]) + __uint_as_float(t[1]);
}
__device__ __forceinline__ float xg_sum(float x) { x = rs16(x, x); return rs32(x, x); }
__device__ __forceinline__ unsigned umax4(v4u v) { return max(max(v[0], v[1]), max(v[2], v[3])); }
__device__ __forceinline__ unsigned now32() { return (unsigned)__builtin_amdgcn_s_memrealtime(); }
__device__ __forceinline__ void hold_until(unsigned t_abs) {
  const unsigned now = now32();
  int d = min((int)(t_abs - now), PACE_MAX);
  const unsigned lim = now + (unsigned)d;
  while (d > 0) { __builtin_amdgcn_s_sleep(1); d = (int)(lim - now32()); }
}
__device__ __forceinline__ void store128_sc1(v4u v, __amdgpu_buffer_rsrc_t rs, int voffset, int soffset) {
  asm volatile("" : "+v"(v));
  __builtin_amdgcn_raw_buffer_store_b128(v, rs, voffset, soffset, 16);
  __builtin_amdgcn_sched_barrier(0);
  asm volatile("s_nop 2" :: "v"(v));
  __builtin_amdgcn_sched_barrier(0);
}
#define SB() __builtin_amdgcn_sched_barrier(0)
#define BAR() __builtin_amdgcn_s_barrier()
#define MEMFENCE() asm volatile("" ::: "memory")
#define LDS_BAR() { asm volatile("s_waitcnt lgkmcnt(0)" ::: "memory"); BAR(); MEMFENCE(); }

template <int V> struct IC { static constexpr int value = V; };
template <int I, int N, class F> __device__ __forceinline__ void static_for(F&& f) {
  if constexpr (I < N) { f(IC<I>{}); static_for<I + 1, N>(f); }
}
constexpr int next_after(int n) { return n < 2 ? n + 3 : (n == 2 || n + 2 >= 2 * NT) ? -1 : n + 2; }
constexpr bool pace_at(int t) { return t == 1 || t == 4 || t == 7 || t == 10; }

__global__ void __launch_bounds__(NTHR, 2)
mha_fused(const unsigned short* __restrict__ x, const unsigned short* __restrict__ kc, const unsigned short* __restrict__ vc,
          const unsigned short* __restrict__ wq, const unsigned short* __restrict__ wo, unsigned short* out, unsigned char* ws) {
  extern __shared__ __attribute__((aligned(16))) unsigned char smem[];
  LAS unsigned char* L = (LAS unsigned char*)smem;
  const int tid = threadIdx.x, lane = tid & 63, w = __builtin_amdgcn_readfirstlane(tid >> 6);
  const int r = lane & 15, g = lane >> 4;
  const int wg = blockIdx.x, idx = wg >> 3, xg = wg & 7, i4 = idx & 3;
  const int h = 4 * xg + i4, bp = idx >> 2;
  const int kq = xg >> 1, ct = 4 * (xg & 1) + ((i4 + 2) & 3);
  const int beta = w >> 2, ws4 = w & 3;
  const int bh = (2 * bp + beta) * NH + h;
  int rot = (4 * h + bp + 8 * beta) & (NT - 1);
  int rotv = rot;
  asm volatile("" : "+s"(rotv));
  LAS unsigned char* LW = L + w * L_WAVE;

  asm volatile("" : "+s"(out));
  const __amdgpu_buffer_rsrc_t rk = __builtin_amdgcn_make_buffer_rsrc((void*)(kc + (size_t)bh * SK * DH), 0, SK * DH * 2, RSRC_W3);
  const __amdgpu_buffer_rsrc_t rv = __builtin_amdgcn_make_buffer_rsrc((void*)(vc + (size_t)bh * SK * DH), 0, SK * DH * 2, RSRC_W3);
  const __amdgpu_buffer_rsrc_t rws = __builtin_amdgcn_make_buffer_rsrc((void*)ws, 0, WS_BYTES, RSRC_W3);
  const __amdgpu_buffer_rsrc_t rw0 = __builtin_amdgcn_make_buffer_rsrc((void*)ws, 0, w == 0 ? WS_BYTES : 0, RSRC_W3);
  const __amdgpu_buffer_rsrc_t rwq = __builtin_amdgcn_make_buffer_rsrc((void*)wq, 0, DM * NH * DH * 2, RSRC_W3);
  const __amdgpu_buffer_rsrc_t rwo = __builtin_amdgcn_make_buffer_rsrc((void*)wo, 0, NH * DH * DM * 2, RSRC_W3);
  const __amdgpu_buffer_rsrc_t rx = __builtin_amdgcn_make_buffer_rsrc((void*)x, 0, NB * SQ * DM * 2, RSRC_W3);
  const __amdgpu_buffer_rsrc_t rout = __builtin_amdgcn_make_buffer_rsrc((void*)out, 0, NB * SQ * DM * 2, RSRC_W3);

  const int koff = (4 * ((r >> 1) & 1) + (r >> 2)) * 256 + (2 * g + (r & 1)) * 16;
  const int voff = lane * 16;
  v4u item[2 * NT][8];
#define LOAD_ITEM(n) { const int so_ = ((((n) + ((n) >= NT ? rotv : rot)) & (NT - 1)) * 4 + ws4) * 8192; _Pragma("unroll") for (int i_ = 0; i_ < 8; ++i_) \
    item[n][i_] = ((n) >= NT) ? __builtin_amdgcn_raw_buffer_load_b128(rv, voff, so_ + i_ * 1024, AUX_NT) \
                            : __builtin_amdgcn_raw_buffer_load_b128(rk, koff, so_ + (i_ >> 1) * 2048 + (i_ & 1) * 128, AUX_NT); }

  const int o_slot = WS_O + (bp * 128 + 4 * h) * 1024;
  const int p_base = WS_P + (bp * 8 + ct) * (16 * 2048);
  const int out_off = ((16 * bp + 2 * w) * DM + 128 * ct + 32 * kq) * 2;

  unsigned tch[5];
  {
    typedef const __attribute__((address_space(4))) unsigned* cptr;
    tch[0] = *(cptr)(kc + (size_t)bh * SK * DH);
    tch[1] = *(cptr)(vc + (size_t)bh * SK * DH);
    tch[2] = *(cptr)(wo + (size_t)(1024 * kq + 128 * w) * DM);
    tch[3] = *(cptr)(ws + WS_SPARE + w * 128);
    tch[4] = *(cptr)(ws + WS_BYTES + w * 128);
    SB();
  }
  {
#define KBLK(s) ((((4 * beta + (s) + bp) & 7) << 2) | ws4)
    const int wqoff = (8 * g) * (NH * DH * 2) + r * 16;
    v4u wa[4][8], xr[4];
#define LOAD_WQ(s) { const int so_ = KBLK(s) * 32 * (NH * DH * 2) + h * (DH * 2); _Pragma("unroll") for (int j_ = 0; j_ < 8; ++j_) \
      wa[s][j_] = __builtin_amdgcn_raw_buffer_load_b128(rwq, wqoff, so_ + j_ * (NH * DH * 2), 0); }
#define LOAD_X() { _Pragma("unroll") for (int i_ = 0; i_ < 4; ++i_) \
      xr[i_] = __builtin_amdgcn_raw_buffer_load_b128(rx, voff, (16 * bp + 2 * w) * (DM * 2) + i_ * 1024, 0); }
#define STAGE_X() { _Pragma("unroll") for (int i_ = 0; i_ < 4; ++i_) \
      *(LAS v4u*)(LW + L_X + w * 32 + (i_ >> 1) * X_ODD + (i_ & 1) * 1024 + voff) = xr[i_]; }
    const LAS unsigned char* xrow = L + (r >> 1) * (L_WAVE + 32) + L_X + (r & 1) * X_ODD + g * 16;
    v4u pfw[4];
#define PF_WO() { const int so_ = (1024 * kq + 128 * w + bp) * (DM * 2) + ct * 256; _Pragma("unroll") for (int j_ = 0; j_ < 4; ++j_) \
      pfw[j_] = __builtin_amdgcn_raw_buffer_load_b128(rwo, (32 * g) * (DM * 2) + r * 16, so_ + 8 * j_ * (DM * 2), 0); }
#define PF_END() asm volatile("" :: "v"(pfw[0]), "v"(pfw[1]), "v"(pfw[2]), "v"(pfw[3]))
    f32x4 qa[8];
#pragma unroll
    for (int e = 0; e < 8; ++e) qa[e] = f32x4{0.f, 0.f, 0.f, 0.f};
#define MMA_WQ(s) { const v4u xf_ = *(const LAS v4u*)(xrow + KBLK(s) * 64); \
      _Pragma("unroll") for (int e_ = 0; e_ < 8; ++e_) qa[e_] = mfma16(tr_frag(wa[s], e_), xf_, qa[e_]); }
#define PUT_Q() { _Pragma("unroll") for (int rho = 0; rho < 4; ++rho) { \
      LAS f32x4* p_ = (LAS f32x4*)(LW + r * QP_STRIDE + (32 * g + 8 * rho) * 4); \
      p_[0] = f32x4{qa[0][rho], qa[1][rho], qa[2][rho], qa[3][rho]}; \
      p_[1] = f32x4{qa[4][rho], qa[5][rho], qa[6][rho], qa[7][rho]}; } }
#define REDUCE_Q() { const int row_ = tid >> 5, n4_ = (tid & 31) * 4; f32x4 pa_[8]; \
      _Pragma("unroll") for (int ww = 0; ww < 8; ++ww) pa_[ww] = *(LAS f32x4*)(L + ww * L_WAVE + row_ * QP_STRIDE + n4_ * 4); \
      const f32x4 s_ = ((pa_[0] + pa_[1]) + (pa_[2] + pa_[3])) + ((pa_[4] + pa_[5]) + (pa_[6] + pa_[7])); \
      *(LAS v2u*)(L + L_QS + row_ * QS_STRIDE + n4_ * 2) = v2u{cvt_pk(s_[0], s_[1]), cvt_pk(s_[2], s_[3])}; }
    if ((bp & 1) == 0) {
      asm volatile("; path A" ::: "memory");
      LOAD_X(); SB(); LOAD_WQ(0); SB(); LOAD_WQ(1); SB(); LOAD_WQ(2); SB(); LOAD_WQ(3); SB();
      PF_WO(); SB();
      STAGE_X(); SB();
      LDS_BAR(); SB();
      LOAD_ITEM(0); SB();
      MMA_WQ(0); MMA_WQ(1); MMA_WQ(2); MMA_WQ(3);
      PUT_Q(); SB();
      PF_END(); SB();
      LOAD_ITEM(1); SB();
      LDS_BAR(); SB();
      REDUCE_Q(); SB();
      LOAD_ITEM(2); SB();
    } else {
      asm volatile("; path B" ::: "memory");
      LOAD_X(); SB(); LOAD_WQ(0); SB();
      PF_WO(); SB();
      LOAD_ITEM(0); SB(); LOAD_ITEM(1); SB();
      STAGE_X(); SB();
      PF_END(); SB();
      LDS_BAR(); SB();
      LOAD_WQ(1); SB(); LOAD_WQ(2); SB();
      MMA_WQ(0); SB();
      LOAD_WQ(3); SB();
      MMA_WQ(1); SB();
      LOAD_ITEM(2); SB();
      MMA_WQ(2); MMA_WQ(3);
      PUT_Q(); SB();
      LDS_BAR(); SB();
      REDUCE_Q(); SB();
    }
  }
  asm volatile("" :: "s"(tch[0]), "s"(tch[1]), "s"(tch[2]), "s"(tch[3]), "s"(tch[4]));
  LDS_BAR();
  asm volatile("" : "+s"(rot));
  v4u qf[2];
#pragma unroll
  for (int hf = 0; hf < 2; ++hf) qf[hf] = *(LAS v4u*)(L + L_QS + (8 * beta + (r & 7)) * QS_STRIDE + (8 * hf + 2 * g + (r >> 3)) * 16);
  unsigned hmask = (r >> 3) ? 0xFFFFFFFFu : 0u;
  asm volatile("" : "+v"(hmask));
  const unsigned psel0 = (hmask & 0x01000C0Cu) | (~hmask & 0x0C0C0100u), psel1 = (hmask & 0x03020C0Cu) | (~hmask & 0x0C0C0302u);

  v4u wr[4][8], wf[4][8];
  const int wooff = (32 * g) * (DM * 2) + r * 16;
  const int wos = (1024 * kq + 128 * w) * (DM * 2) + ct * 256;
#define LOAD_WO(ks, j0) { _Pragma("unroll") for (int j_ = j0; j_ < j0 + 4; ++j_) \
    wr[ks][j_] = __builtin_amdgcn_raw_buffer_load_b128(rwo, wooff, wos + (8 * (j_ & 3) + 2 * (((ks) + w) & 3) + (j_ >> 2)) * (DM * 2), 0); }
#define PARK_WO(ks) { _Pragma("unroll") for (int e_ = 0; e_ < 8; ++e_) *(LAS v4u*)(LW + (ks) * 8192 + e_ * 1024 + voff) = tr_frag(wr[ks], e_); }
#define KEEP_WO(ks) { _Pragma("unroll") for (int e_ = 0; e_ < 8; ++e_) { wf[ks][e_] = tr_frag(wr[ks], e_); asm volatile("" : "+v"(wf[ks][e_])); } }
#define BACK_WO(ks) { _Pragma("unroll") for (int e_ = 0; e_ < 8; ++e_) wf[ks][e_] = *(LAS v4u*)(LW + (ks) * 8192 + e_ * 1024 + voff); }
#define SCORES(n, s) { f32x4 sa_[4]; \
    _Pragma("unroll") for (int u = 0; u < 4; ++u) sa_[u] = mfma16(item[n][2 * u], qf[0], f32x4{0.f, 0.f, 0.f, 0.f}); \
    _Pragma("unroll") for (int u = 0; u < 4; ++u) sa_[u] = mfma16(item[n][2 * u + 1], qf[1], sa_[u]); \
    _Pragma("unroll") for (int u = 0; u < 4; ++u) (s)[u] = bsel(hmask, sa_[u][3], sa_[u][0]) + ror8(bsel(hmask, sa_[u][1], sa_[u][2])); \
    asm volatile("" : "+v"((s)[0]), "+v"((s)[1]), "+v"((s)[2]), "+v"((s)[3])); }

  f32x4 oacc[8];
  float sc[NT][4];
  unsigned pk[NT][2];
  v2u epoch0, flags_v;
  unsigned tag_lo = 0, tag_hi = 0;
  v4u snap;
  const unsigned tbase = now32();
  unsigned line0 = 0, line1 = 0, tprev = 0;
#define PV(n) { const v4u pf_ = {__builtin_amdgcn_perm(0u, pk[(n) - NT][0], psel0), __builtin_amdgcn_perm(0u, pk[(n) - NT][0], psel1), \
                                 __builtin_amdgcn_perm(0u, pk[(n) - NT][1], psel0), __builtin_amdgcn_perm(0u, pk[(n) - NT][1], psel1)}; \
    _Pragma("unroll") for (int e = 0; e < 8; ++e) oacc[e] = mfma16(tr_frag(item[n], e), pf_, (n) == NT ? f32x4{0.f, 0.f, 0.f, 0.f} : oacc[e]); \
    asm volatile("" : "+v"(oacc[0]), "+v"(oacc[1]), "+v"(oacc[2]), "+v"(oacc[3]), "+v"(oacc[4]), "+v"(oacc[5]), "+v"(oacc[6]), "+v"(oacc[7])); }
#define CONSUME(n) { if constexpr ((n) < NT) SCORES(n, sc[(n) < NT ? (n) : 0]) else PV((n) < NT ? NT : (n)) }

  static_for<0, NT>([&](auto tc) __attribute__((always_inline)) {
    constexpr int t = decltype(tc)::value;
    CONSUME(2 * t);
    SB();
    if constexpr (t == 0) {
      epoch0 = __builtin_amdgcn_raw_buffer_load_b64(rw0, 0, WS_EPOCH + bp * 256, AUX_SC1);
      const int p_own = ((voff >> 7) & 3) * 8192 + (voff >> 9) * 128 + (voff & 0x70);
      __builtin_amdgcn_raw_buffer_store_b64(v2u{SENT, SENT}, rws, voff >> 1, o_slot + w * 512, AUX_SC1);
      store128_sc1(v4u{SENT, SENT, SENT, SENT}, rws, p_own, p_base + kq * 2048 + w * 256);
      MEMFENCE();
      SB();
    }
    if constexpr (t == NT - 5) flags_v = __builtin_amdgcn_raw_buffer_load_b64(rw0, (voff << 3) & 0xF80, WS_FLAG + bp * 4096, AUX_SC1);
    if constexpr (pace_at(t - 2)) {
      asm volatile("" : "+v"(snap));
      const int dt = (int)(snap[0] - tbase);
      const bool ok = snap[3] == ~(snap[0] ^ snap[1] ^ snap[2]) && dt > -1500 && dt < 12000;
      const int rs = ok ? (int)snap[1] : 0;
      const int nr = __builtin_popcount((unsigned)__builtin_amdgcn_ballot_w64(rs > 0) & 0xFFu);
      const int cm = (int)((float)__builtin_amdgcn_readfirstlane(sum8(rs)) * __builtin_amdgcn_rcpf((float)max(nr, 1)));
      const int cu = cm > PACE_C + PACE_DZ ? min(cm - PACE_S0, PACE_C + 100) : PACE_C;
      const int v = sum8(dt - (int)snap[2] * cu) >> 3;
      const bool all_ok = (__builtin_amdgcn_ballot_w64(ok) & 0xFFull) == 0xFFull;
      if (w == 0) *(LAS v2u*)(L + L_PC) = v2u{tbase + (unsigned)(all_ok ? v : -1000000), (unsigned)cu};
    }
    if constexpr (t >= 4 && next_after(2 * t) >= 0) hold_until(line0 + ((16 * t - 7 + w) * line1 >> 4));
    if constexpr (t >= WO_S0 && t < WO_S0 + 4) LOAD_WO(t - WO_S0, 0);
    if constexpr (pace_at(t - 1)) {
      const int px = (voff >> 4) & 7;
      const int peer = (((bp + px) & 7) * 4 + ((i4 + px) & 3)) * 8 + ((xg + px) & 7);
      snap = __builtin_amdgcn_raw_buffer_load_b128(rw0, peer * 128, WS_PACE, AUX_SC1);
      SB();
    }
    if constexpr (next_after(2 * t) >= 0) LOAD_ITEM(next_after(2 * t));
    SB();
    if constexpr (pace_at(t - 2)) asm volatile("s_waitcnt lgkmcnt(0)" ::: "memory");
    if constexpr (t == NT - 3) {
      if (w == 0) {
        if (__builtin_expect(!__all(flags_v[0] == tag_lo && flags_v[1] == tag_hi), 0)) {
          for (unsigned spins = 0; spins < kSpinCap; ++spins) {
            __builtin_amdgcn_s_sleep(2);
            MEMFENCE();
            const v2u fv = __builtin_amdgcn_raw_buffer_load_b64(rws, (voff << 3) & 0xF80, WS_FLAG + bp * 4096, AUX_SC1);
            if (__all(fv[0] == tag_lo && fv[1] == tag_hi)) break;
          }
        }
        if (h == 0) __builtin_amdgcn_raw_buffer_store_b64(v2u{tag_lo, tag_hi}, rws, (voff >> 1) & 0x78, WS_EPOCH + bp * 256, AUX_SC1);
      }
      MEMFENCE();
      BAR();
      MEMFENCE();
    } else if constexpr (t < NT - 2) BAR();
    if constexpr (pace_at(t - 2)) {
      MEMFENCE();
      const v2u ln = *(LAS v2u*)(L + L_PC);
      line0 = __builtin_amdgcn_readfirstlane(ln[0]); line1 = __builtin_amdgcn_readfirstlane(ln[1]);
    }
    if constexpr (pace_at(t)) {
      const unsigned tv = now32();
      const unsigned rate = t >= 7 ? (tv - tprev - PACE_WO) / (unsigned)(t > 1 ? t - 1 : 1) : 0u;
      if constexpr (t == 1) tprev = tv;
      store128_sc1(v4u{tv, rate, (unsigned)t, ~(tv ^ rate ^ (unsigned)t)}, rw0, voff & 0x70, WS_PACE + wg * 128);
    }
    if constexpr (t == NT - 1) BACK_WO(1);
    SB();
    CONSUME(2 * t + 1);
    SB();
    if constexpr (t == WO_S0 + 1) PARK_WO(0);
    if constexpr (t == WO_S0 + 2) PARK_WO(1);
    if constexpr (t == WO_S0 + 3) KEEP_WO(2);
    if constexpr (t == WO_S0 + 4) KEEP_WO(3);
    SB();
    if constexpr (t >= 3 && t < NT - 1) hold_until(line0 + ((16 * t + 1 + w) * line1 >> 4));
    if constexpr (t >= WO_S0 && t < WO_S0 + 4) { LOAD_WO(t - WO_S0, 4); SB(); }
    if constexpr (next_after(2 * t + 1) >= 0) LOAD_ITEM(next_after(2 * t + 1));
    SB();
    if constexpr (t == NT - 2) { BACK_WO(0); SB(); }
    if constexpr (t == NT / 2 - 1) {
      float m = fmaxf(fmaxf(sc[0][0], sc[0][1]), fmaxf(sc[0][2], sc[0][3]));
#pragma unroll
      for (int k = 1; k < NT; ++k) m = fmaxf(fmaxf(fmaxf(m, sc[k][0]), sc[k][1]), fmaxf(sc[k][2], sc[k][3]));
      m = xg_max(m);
      const float mc = m * CEXP;
      float l0 = 0.f, l1 = 0.f;
#pragma unroll
      for (int k = 0; k < NT; ++k) {
#pragma unroll
        for (int u = 0; u < 4; ++u) sc[k][u] = __builtin_amdgcn_exp2f(__builtin_fmaf(sc[k][u], CEXP, -mc));
        l0 += sc[k][0] + sc[k][1]; l1 += sc[k][2] + sc[k][3];
      }
      const float lsum = xg_sum(l0 + l1);
      LAS unsigned char* ml = L + L_ML + beta * 512 + ((voff >> 4) & 7) * 64;
      if (voff < 256) *(LAS f32x2*)(ml + ws4 * 16 + (voff >> 7) * 8) = f32x2{mc, lsum};
      asm volatile("s_waitcnt vmcnt(0)" ::: "memory");
      LDS_BAR();
      {
        const unsigned long long tag64 = (((unsigned long long)(unsigned)__builtin_amdgcn_readfirstlane(epoch0[1]) << 32) |
                                          (unsigned)__builtin_amdgcn_readfirstlane(epoch0[0])) + 1ull;
        tag_lo = (unsigned)tag64; tag_hi = (unsigned)(tag64 >> 32);
        __builtin_amdgcn_raw_buffer_store_b64(v2u{tag_lo, tag_hi}, rw0, (voff >> 1) & 0x78, WS_FLAG + (bp * 32 + h) * 128, AUX_SC1);
      }
      f32x4 rec[4];
#pragma unroll
      for (int j = 0; j < 4; ++j) rec[j] = *(LAS f32x4*)(ml + j * 16);
      const float M = fmaxf(fmaxf(fmaxf(rec[0][0], rec[0][2]), fmaxf(rec[1][0], rec[1][2])), fmaxf(fmaxf(rec[2][0], rec[2][2]), fmaxf(rec[3][0], rec[3][2])));
      float Ls = 0.f;
#pragma unroll
      for (int j = 0; j < 4; ++j) Ls += __builtin_amdgcn_exp2f(rec[j][0] - M) * rec[j][1] + __builtin_amdgcn_exp2f(rec[j][2] - M) * rec[j][3];
      const float scale = __builtin_amdgcn_exp2f(mc - M) / Ls;
#pragma unroll
      for (int k = 0; k < NT; ++k) {
        pk[k][0] = cvt_pk(sc[k][0] * scale, sc[k][1] * scale); pk[k][1] = cvt_pk(sc[k][2] * scale, sc[k][3] * scale);
        asm volatile("" : "+v"(pk[k][0]), "+v"(pk[k][1]));
      }
      SB();
    }
  });
  const int lane2 = __builtin_amdgcn_mbcnt_hi(~0u, __builtin_amdgcn_mbcnt_lo(~0u, 0u));
  asm volatile("s_waitcnt lgkmcnt(0)" ::: "memory");
#pragma unroll
  for (int e = 0; e < 8; ++e) *(LAS f32x4*)(LW + e * 1024 + lane2 * 16) = oacc[e];
  LDS_BAR();
  {
    const int fl = 32 * (w & 1) + (lane2 >> 1);
    const LAS unsigned char* src = L + ((fl >> 3) & 1) * (4 * L_WAVE) + ((w & ~1) + (lane2 & 1)) * 1024 + (fl & 0x37) * 16;
    const int par = (lane2 & 1) * 128;
    f32x4 pa[8];
#pragma unroll
    for (int j = 0; j < 8; ++j) pa[j] = *(const LAS f32x4*)(src + (j >> 1) * L_WAVE + ((j & 1) ? 128 - par : par));
    const f32x4 a0 = ((pa[0] + pa[1]) + (pa[2] + pa[3])) + ((pa[4] + pa[5]) + (pa[6] + pa[7]));
    v2u ov = {cvt_pk(a0[0], a0[1]), cvt_pk(a0[2], a0[3])};
#pragma unroll
    for (int j = 0; j < 2; ++j) ov[j] = (ov[j] == SENT) ? 0x7FC07FC0u : ov[j];
    __builtin_amdgcn_raw_buffer_store_b64(ov, rws, lane2 * 8, o_slot + w * 512, AUX_SC1);
  }
  SB();
  {
    const int goff = WS_O + (bp * 128 + 4 * (8 * kq + w)) * 1024;
    unsigned spins = 0;
#pragma unroll 1
    for (; spins < kSpinCap; ++spins) {
      MEMFENCE();
      if (__any(__builtin_amdgcn_raw_buffer_load_b32(rws, 0, goff, AUX_SC1) != SENT)) break;
      __builtin_amdgcn_s_sleep(1);
    }
    v4u ob[4];
    for (;; ++spins) {
      MEMFENCE();
#pragma unroll
      for (int ks = 0; ks < 4; ++ks) ob[ks] = __builtin_amdgcn_raw_buffer_load_b128(rws, lane2 * 16, goff + ((ks + w) & 3) * 1024, AUX_SC1);
      if (!__any(max(max(umax4(ob[0]), umax4(ob[1])), max(umax4(ob[2]), umax4(ob[3]))) == SENT) || spins >= kSpinCap) break;
      __builtin_amdgcn_s_sleep(2);
    }
#pragma unroll
    for (int hf = 0; hf < 2; ++hf) {
      f32x4 c[4];
#pragma unroll
      for (int e = 0; e < 4; ++e) c[e] = f32x4{0.f, 0.f, 0.f, 0.f};
#pragma unroll
      for (int ks = 0; ks < 4; ++ks)
#pragma unroll
        for (int e = 0; e < 4; ++e) c[e] = mfma16(wf[ks][4 * hf + e], ob[ks], c[e]);
#pragma unroll
      for (int rho = 0; rho < 4; ++rho)
        *(LAS f32x4*)(LW + L_C + (((lane2 & 15) * 512 + (lane2 >> 4) * 128 + (lane2 & 7) * 16) ^ (rho * 32 + hf * 16))) =
            f32x4{c[0][rho], c[1][rho], c[2][rho], c[3][rho]};
    }
  }
  LDS_BAR();
  {
    const int row = 2 * w + (lane2 >> 5);
    const int so = row * 512 + (((lane2 & 31) * 16) ^ ((row & 7) * 16));
    f32x4 pa[8];
#pragma unroll
    for (int ww = 0; ww < 8; ++ww) pa[ww] = *(const LAS f32x4*)(L + ww * L_WAVE + L_C + so);
    const f32x4 s = ((pa[0] + pa[1]) + (pa[2] + pa[3])) + ((pa[4] + pa[5]) + (pa[6] + pa[7]));
    v4u sv = __builtin_bit_cast(v4u, s);
#pragma unroll
    for (int j = 0; j < 4; ++j) sv[j] = (sv[j] == SENT) ? 0x7FC00000u : sv[j];
    const int p_own2 = ((lane2 >> 3) & 3) * 8192 + (lane2 >> 5) * 128 + (lane2 & 7) * 16;
    store128_sc1(sv, rws, p_own2, p_base + kq * 2048 + w * 256);
  }
  {
    const int poff = (lane2 >> 4) * 2048 + (lane2 & 15) * 16;
    int oo = ((lane2 >> 3) & 1) * (DM * 2) + (lane2 & 7) * 8 + (lane2 >> 5) * 4 + (lane2 & 16) * 0x1000000;
    asm volatile("" : "+v"(oo));
    v4u pv;
    __builtin_amdgcn_s_sleep(8);
    for (unsigned spins = 0;; ++spins) {
      MEMFENCE();
      pv = __builtin_amdgcn_raw_buffer_load_b128(rws, poff, p_base + kq * 8192 + w * 256, AUX_SC1);
      if (!__any(umax4(pv) == SENT) || spins >= kSpinCap) break;
      __builtin_amdgcn_s_sleep(2);
    }
    const float u0 = rs32(__uint_as_float(pv[0]), __uint_as_float(pv[2])), u1 = rs32(__uint_as_float(pv[1]), __uint_as_float(pv[3]));
    const float t0 = rs16(u0, u0), t1 = rs16(u1, u1);
    __builtin_amdgcn_raw_buffer_store_b32(cvt_pk(t0, t1), rout, oo, out_off, 0);
  }
}

}

extern "C" void kernel_launch(void* const* d_in, const int* in_sizes, int n_in, void* d_out, int out_size, void* d_ws,
                              size_t ws_size, hipStream_t stream) {
  (void)in_sizes; (void)n_in; (void)out_size; (void)ws_size;
  (void)hipFuncSetAttribute((const void*)mha_fused, hipFuncAttributeMaxDynamicSharedMemorySize, L_BYTES);
  hipLaunchKernelGGL(mha_fused, dim3(NWG), dim3(NTHR), L_BYTES, stream, (const unsigned short*)d_in[0], (const unsigned short*)d_in[1],
                     (const unsigned short*)d_in[2], (const unsigned short*)d_in[3], (const unsigned short*)d_in[4],
                     (unsigned short*)d_out, (unsigned char*)d_ws);
}
```

```cpp
#include <hip/hip_runtime.h>
#include <hip/hip_bf16.h>

#define LAS __attribute__((address_space(3)))
typedef unsigned v4u __attribute__((ext_vector_type(4)));
typedef unsigned v2u __attribute__((ext_vector_type(2)));
typedef float f32x4 __attribute__((ext_vector_type(4)));
typedef __bf16 bf16x8 __attribute__((ext_vector_type(8)));
typedef __bf16 bf16x2 __attribute__((ext_vector_type(2)));
typedef float f32x2 __attribute__((ext_vector_type(2)));

namespace {

constexpr int NB = 16, SQ = 8, DM = 1024, NH = 32, SK = 2048, DH = 128;
constexpr int NWG = 256, NTHR = 512;
constexpr int NT = 16;
constexpr float CEXP = 0.08838834764831845f * 1.4426950408889634f;
constexpr int AUX_NT = 2, AUX_SC1 = 16;
constexpr int RSRC_W3 = 0x00020000;
constexpr unsigned SENT = 0xFFFFFFFFu;
constexpr unsigned kSpinCap = 1u << 16;

constexpr int PACE_C = 449;
constexpr int PACE_DZ1 = 11, PACE_DZ2 = 9;
constexpr int PACE_S0 = 4;
constexpr int PACE_WO = 20;
constexpr int PACE_MAX = 500;
constexpr int WO_S0 = 2;

constexpr int WS_EPOCH = 0;
constexpr int WS_SPARE = 2048;
constexpr int WS_FLAG = 4096;
constexpr int WS_PACE = WS_FLAG + 32768;
constexpr int WS_O = 131072;
constexpr int WS_P = WS_O + 8 * 128 * 1024;
constexpr int WS_BYTES = WS_P + 8 * 8 * 16 * 2048;

constexpr int L_WAVE = 16384;
constexpr int L_QS = 8 * L_WAVE;
constexpr int QS_STRIDE = 272;
constexpr int L_ML = L_QS + 16 * QS_STRIDE;
constexpr int L_PC = L_ML + 1024;
constexpr int L_BYTES = L_PC + 16;
constexpr int QP_STRIDE = 528;
constexpr int L_X = 9216, X_ODD = 2064;
constexpr int L_C = 8192;

__device__ __forceinline__ f32x4 mfma16(v4u a, v4u b, f32x4 c) {
  return __builtin_amdgcn_mfma_f32_16x16x32_bf16(__builtin_bit_cast(bf16x8, a), __builtin_bit_cast(bf16x8, b), c, 0, 0, 0);
}
__device__ __forceinline__ unsigned pack_lo(unsigned a, unsigned b) { return __builtin_amdgcn_perm(b, a, 0x05040100u); }
__device__ __forceinline__ unsigned pack_hi(unsigned a, unsigned b) { return __builtin_amdgcn_perm(b, a, 0x07060302u); }
__device__ __forceinline__ unsigned cvt_pk(float lo, float hi) {
  f32x2 v = {lo, hi};
  return __builtin_bit_cast(unsigned, __builtin_convertvector(v, bf16x2));
}
__device__ __forceinline__ v4u tr_frag(const v4u (&R)[8], int e) {
  v4u f;
#pragma unroll
  for (int jj = 0; jj < 4; ++jj)
    f[jj] = (e & 1) ? pack_hi(R[2 * jj][e >> 1], R[2 * jj + 1][e >> 1]) : pack_lo(R[2 * jj][e >> 1], R[2 * jj + 1][e >> 1]);
  return f;
}
template <int CTRL> __device__ __forceinline__ int dppi(int x) { return __builtin_amdgcn_mov_dpp(x, CTRL, 0xf, 0xf, true); }
__device__ __forceinline__ int sum8(int v) { v += dppi<0xB1>(v); v += dppi<0x4E>(v); return v + dppi<0x141>(v); }
__device__ __forceinline__ float ror8(float x) {
  return __builtin_bit_cast(float, dppi<0x128>(__builtin_bit_cast(int, x)));
}
__device__ __forceinline__ float bsel(unsigned mask, float a, float b) {
  return __uint_as_float((__float_as_uint(a) & mask) | (__float_as_uint(b) & ~mask));
}
__device__ __forceinline__ float xg_max(float x) {
  auto s = __builtin_amdgcn_permlane16_swap(__float_as_uint(x), __float_as_uint(x), false, false);
  x = fmaxf(__uint_as_float(s[0]), __uint_as_float(s[1]));
  auto t = __builtin_amdgcn_permlane32_swap(__float_as_uint(x), __float_as_uint(x), false, false);
  return fmaxf(__uint_as_float(t[0]), __uint_as_float(t[1]));
}
__device__ __forceinline__ float rs32(float a, float b) {
  auto t = __builtin_amdgcn_permlane32_swap(__float_as_uint(a), __float_as_uint(b), false, false);
  return __uint_as_float(t[0]) + __uint_as_float(t[1]);
}
__device__ __forceinline__ float rs16(float a, float b) {
  auto t = __builtin_amdgcn_permlane16_swap(__float_as_uint(a), __float_as_uint(b), false, false);
  return __uint_as_float(t[0]) + __uint_as_float(t[1]);
}
__device__ __forceinline__ float xg_sum(float x) { x = rs16(x, x); return rs32(x, x); }
__device__ __forceinline__ unsigned umax4(v4u v) { return max(max(v[0], v[1]), max(v[2], v[3])); }
__device__ __forceinline__ unsigned now32() { return (unsigned)__builtin_amdgcn_s_memrealtime(); }
__device__ __forceinline__ void hold_until(unsigned t_abs) {
  const unsigned now = now32();
  int d = min((int)(t_abs - now), PACE_MAX);
  const unsigned lim = now + (unsigned)d;
  while (d > 0) { __builtin_amdgcn_s_sleep(1); d = (int)(lim - now32()); }
}
__device__ __forceinline__ void store128_sc1(v4u v, __amdgpu_buffer_rsrc_t rs, int voffset, int soffset) {
  asm volatile("" : "+v"(v));
  __builtin_amdgcn_raw_buffer_store_b128(v, rs, voffset, soffset, 16);
  __builtin_amdgcn_sched_barrier(0);
  asm volatile("s_nop 2" :: "v"(v));
  __builtin_amdgcn_sched_barrier(0);
}
#define SB() __builtin_amdgcn_sched_barrier(0)
#define BAR() __builtin_amdgcn_s_barrier()
#define MEMFENCE() asm volatile("" ::: "memory")
#define LDS_BAR() { asm volatile("s_waitcnt lgkmcnt(0)" ::: "memory"); BAR(); MEMFENCE(); }

template <int V> struct IC { static constexpr int value = V; };
template <int I, int N, class F> __device__ __forceinline__ void static_for(F&& f) {
  if constexpr (I < N) { f(IC<I>{}); static_for<I + 1, N>(f); }
}
constexpr int next_after(int n) { return n < 2 ? n + 3 : (n == 2 || n + 2 >= 2 * NT) ? -1 : n + 2; }
constexpr bool pace_at(int t) { return t == 1 || t == 4 || t == 7 || t == 10; }
constexpr int vpos(int t) { return 16 * t + (t >= NT / 2 ? 1 : 0); }

__global__ void __launch_bounds__(NTHR, 2)
mha_fused(const unsigned short* __restrict__ x, const unsigned short* __restrict__ kc, const unsigned short* __restrict__ vc,
          const unsigned short* __restrict__ wq, const unsigned short* __restrict__ wo, unsigned short* out, unsigned char* ws) {
  extern __shared__ __attribute__((aligned(16))) unsigned char smem[];
  LAS unsigned char* L = (LAS unsigned char*)smem;
  const int tid = threadIdx.x, lane = tid & 63, w = __builtin_amdgcn_readfirstlane(tid >> 6);
  const int r = lane & 15, g = lane >> 4;
  const int wg = blockIdx.x, idx = wg >> 3, xg = wg & 7, i4 = idx & 3;
  const int h = 4 * xg + i4, bp = idx >> 2;
  const int kq = xg >> 1, ct = 4 * (xg & 1) + ((i4 + 2) & 3);
  const int beta = w >> 2, ws4 = w & 3;
  const int bh = (2 * bp + beta) * NH + h;
  int rot = (4 * h + bp + 8 * beta) & (NT - 1);
  int rotv = rot;
  asm volatile("" : "+s"(rotv));
  LAS unsigned char* LW = L + w * L_WAVE;

  asm volatile("" : "+s"(out));
  const __amdgpu_buffer_rsrc_t rk = __builtin_amdgcn_make_buffer_rsrc((void*)(kc + (size_t)bh * SK * DH), 0, SK * DH * 2, RSRC_W3);
  const __amdgpu_buffer_rsrc_t rv = __builtin_amdgcn_make_buffer_rsrc((void*)(vc + (size_t)bh * SK * DH), 0, SK * DH * 2, RSRC_W3);
  const __amdgpu_buffer_rsrc_t rws = __builtin_amdgcn_make_buffer_rsrc((void*)ws, 0, WS_BYTES, RSRC_W3);
  const __amdgpu_buffer_rsrc_t rw0 = __builtin_amdgcn_make_buffer_rsrc((void*)ws, 0, w == 0 ? WS_BYTES : 0, RSRC_W3);
  const __amdgpu_buffer_rsrc_t rwq = __builtin_amdgcn_make_buffer_rsrc((void*)wq, 0, DM * NH * DH * 2, RSRC_W3);
  const __amdgpu_buffer_rsrc_t rwo = __builtin_amdgcn_make_buffer_rsrc((void*)wo, 0, NH * DH * DM * 2, RSRC_W3);
  const __amdgpu_buffer_rsrc_t rx = __builtin_amdgcn_make_buffer_rsrc((void*)x, 0, NB * SQ * DM * 2, RSRC_W3);
  const __amdgpu_buffer_rsrc_t rout = __builtin_amdgcn_make_buffer_rsrc((void*)out, 0, NB * SQ * DM * 2, RSRC_W3);

  const int koff = (4 * ((r >> 1) & 1) + (r >> 2)) * 256 + (2 * g + (r & 1)) * 16;
  const int voff = lane * 16;
  v4u item[2 * NT][8];
#define LOAD_ITEM(n) { const int so_ = ((((n) + ((n) >= NT ? rotv : rot)) & (NT - 1)) * 4 + ws4) * 8192; _Pragma("unroll") for (int i_ = 0; i_ < 8; ++i_) \
    item[n][i_] = ((n) >= NT) ? __builtin_amdgcn_raw_buffer_load_b128(rv, voff, so_ + i_ * 1024, AUX_NT) \
                            : __builtin_amdgcn_raw_buffer_load_b128(rk, koff, so_ + (i_ >> 1) * 2048 + (i_ & 1) * 128, AUX_NT); }

  const int o_slot = WS_O + (bp * 128 + 4 * h) * 1024;
  const int p_base = WS_P + (bp * 8 + ct) * (16 * 2048);
  const int out_off = ((16 * bp + 2 * w) * DM + 128 * ct + 32 * kq) * 2;

  unsigned tch[5];
  {
    typedef const __attribute__((address_space(4))) unsigned* cptr;
    tch[0] = *(cptr)(kc + (size_t)bh * SK * DH);
    tch[1] = *(cptr)(vc + (size_t)bh * SK * DH);
    tch[2] = *(cptr)(wo + (size_t)(1024 * kq + 128 * w) * DM);
    tch[3] = *(cptr)(ws + WS_SPARE + w * 128);
    tch[4] = *(cptr)(ws + WS_BYTES + w * 128);
    SB();
  }
  {
#define KBLK(s) ((((4 * beta + (s) + bp) & 7) << 2) | ws4)
    const int wqoff = (8 * g) * (NH * DH * 2) + r * 16;
    v4u wa[4][8], xr[4];
#define LOAD_WQ(s) { const int so_ = KBLK(s) * 32 * (NH * DH * 2) + h * (DH * 2); _Pragma("unroll") for (int j_ = 0; j_ < 8; ++j_) \
      wa[s][j_] = __builtin_amdgcn_raw_buffer_load_b128(rwq, wqoff, so_ + j_ * (NH * DH * 2), 0); }
#define LOAD_X() { _Pragma("unroll") for (int i_ = 0; i_ < 4; ++i_) \
      xr[i_] = __builtin_amdgcn_raw_buffer_load_b128(rx, voff, (16 * bp + 2 * w) * (DM * 2) + i_ * 1024, 0); }
#define STAGE_X() { _Pragma("unroll") for (int i_ = 0; i_ < 4; ++i_) \
      *(LAS v4u*)(LW + L_X + w * 32 + (i_ >> 1) * X_ODD + (i_ & 1) * 1024 + voff) = xr[i_]; }
    const LAS unsigned char* xrow = L + (r >> 1) * (L_WAVE + 32) + L_X + (r & 1) * X_ODD + g * 16;
    v4u pfw[4];
#define PF_WO() { const int so_ = (1024 * kq + 128 * w + bp) * (DM * 2) + ct * 256; _Pragma("unroll") for (int j_ = 0; j_ < 4; ++j_) \
      pfw[j_] = __builtin_amdgcn_raw_buffer_load_b128(rwo, (32 * g) * (DM * 2) + r * 16, so_ + 8 * j_ * (DM * 2), 0); }
#define PF_END() asm volatile("" :: "v"(pfw[0]), "v"(pfw[1]), "v"(pfw[2]), "v"(pfw[3]))
    f32x4 qa[8];
#pragma unroll
    for (int e = 0; e < 8; ++e) qa[e] = f32x4{0.f, 0.f, 0.f, 0.f};
#define MMA_WQ(s) { const v4u xf_ = *(const LAS v4u*)(xrow + KBLK(s) * 64); \
      _Pragma("unroll") for (int e_ = 0; e_ < 8; ++e_) qa[e_] = mfma16(tr_frag(wa[s], e_), xf_, qa[e_]); }
#define PUT_Q() { _Pragma("unroll") for (int rho = 0; rho < 4; ++rho) { \
      LAS f32x4* p_ = (LAS f32x4*)(LW + r * QP_STRIDE + (32 * g + 8 * rho) * 4); \
      p_[0] = f32x4{qa[0][rho], qa[1][rho], qa[2][rho], qa[3][rho]}; \
      p_[1] = f32x4{qa[4][rho], qa[5][rho], qa[6][rho], qa[7][rho]}; } }
#define REDUCE_Q() { const int row_ = tid >> 5, n4_ = (tid & 31) * 4; f32x4 pa_[8]; \
      _Pragma("unroll") for (int ww = 0; ww < 8; ++ww) pa_[ww] = *(LAS f32x4*)(L + ww * L_WAVE + row_ * QP_STRIDE + n4_ * 4); \
      const f32x4 s_ = ((pa_[0] + pa_[1]) + (pa_[2] + pa_[3])) + ((pa_[4] + pa_[5]) + (pa_[6] + pa_[7])); \
      *(LAS v2u*)(L + L_QS + row_ * QS_STRIDE + n4_ * 2) = v2u{cvt_pk(s_[0], s_[1]), cvt_pk(s_[2], s_[3])}; }
    if ((bp & 1) == 0) {
      asm volatile("; path A" ::: "memory");
      LOAD_X(); SB(); LOAD_WQ(0); SB(); LOAD_WQ(1); SB(); LOAD_WQ(2); SB(); LOAD_WQ(3); SB();
      PF_WO(); SB();
      STAGE_X(); SB();
      LDS_BAR(); SB();
      LOAD_ITEM(0); SB();
      MMA_WQ(0); MMA_WQ(1); MMA_WQ(2); MMA_WQ(3);
      PUT_Q(); SB();
      PF_END(); SB();
      LOAD_ITEM(1); SB();
      LDS_BAR(); SB();
      REDUCE_Q(); SB();
      LOAD_ITEM(2); SB();
    } else {
      asm volatile("; path B" ::: "memory");
      LOAD_X(); SB(); LOAD_WQ(0); SB();
      PF_WO(); SB();
      LOAD_ITEM(0); SB(); LOAD_ITEM(1); SB();
      STAGE_X(); SB();
      PF_END(); SB();
      LDS_BAR(); SB();
      LOAD_WQ(1); SB(); LOAD_WQ(2); SB();
      MMA_WQ(0); SB();
      LOAD_WQ(3); SB();
      MMA_WQ(1); SB();
      LOAD_ITEM(2); SB();
      MMA_WQ(2); MMA_WQ(3);
      PUT_Q(); SB();
      LDS_BAR(); SB();
      REDUCE_Q(); SB();
    }
  }
  asm volatile("" :: "s"(tch[0]), "s"(tch[1]), "s"(tch[2]), "s"(tch[3]), "s"(tch[4]));
  LDS_BAR();
  asm volatile("" : "+s"(rot));
  v4u qf[2];
#pragma unroll
  for (int hf = 0; hf < 2; ++hf) qf[hf] = *(LAS v4u*)(L + L_QS + (8 * beta + (r & 7)) * QS_STRIDE + (8 * hf + 2 * g + (r >> 3)) * 16);
  unsigned hmask = (r >> 3) ? 0xFFFFFFFFu : 0u;
  asm volatile("" : "+v"(hmask));
  const unsigned psel0 = (hmask & 0x01000C0Cu) | (~hmask & 0x0C0C0100u), psel1 = (hmask & 0x03020C0Cu) | (~hmask & 0x0C0C0302u);

  v4u wr[4][8], wf[4][8];
  const int wooff = (32 * g) * (DM * 2) + r * 16;
  const int wos = (1024 * kq + 128 * w) * (DM * 2) + ct * 256;
#define LOAD_WO(ks, j0) { _Pragma("unroll") for (int j_ = j0; j_ < j0 + 4; ++j_) \
    wr[ks][j_] = __builtin_amdgcn_raw_buffer_load_b128(rwo, wooff, wos + (8 * (j_ & 3) + 2 * (((ks) + w) & 3) + (j_ >> 2)) * (DM * 2), 0); }
#define PARK_WO(ks) { _Pragma("unroll") for (int e_ = 0; e_ < 8; ++e_) *(LAS v4u*)(LW + (ks) * 8192 + e_ * 1024 + voff) = tr_frag(wr[ks], e_); }
#define KEEP_WO(ks) { _Pragma("unroll") for (int e_ = 0; e_ < 8; ++e_) { wf[ks][e_] = tr_frag(wr[ks], e_); asm volatile("" : "+v"(wf[ks][e_])); } }
#define BACK_WO(ks) { _Pragma("unroll") for (int e_ = 0; e_ < 8; ++e_) wf[ks][e_] = *(LAS v4u*)(LW + (ks) * 8192 + e_ * 1024 + voff); }
#define SCORES(n, s) { f32x4 sa_[4]; \
    _Pragma("unroll") for (int u = 0; u < 4; ++u) sa_[u] = mfma16(item[n][2 * u], qf[0], f32x4{0.f, 0.f, 0.f, 0.f}); \
    _Pragma("unroll") for (int u = 0; u < 4; ++u) sa_[u] = mfma16(item[n][2 * u + 1], qf[1], sa_[u]); \
    _Pragma("unroll") for (int u = 0; u < 4; ++u) (s)[u] = bsel(hmask, sa_[u][3], sa_[u][0]) + ror8(bsel(hmask, sa_[u][1], sa_[u][2])); \
    asm volatile("" : "+v"((s)[0]), "+v"((s)[1]), "+v"((s)[2]), "+v"((s)[3])); }

  f32x4 oacc[8];
  float sc[NT][4];
  unsigned pk[NT][2];
  v2u epoch0, flags_v;
  unsigned tag_lo = 0, tag_hi = 0;
  v4u snap;
  const unsigned tbase = now32();
  unsigned line0 = 0, line1 = 0, tprev = 0;
#define PV(n) { const v4u pf_ = {__builtin_amdgcn_perm(0u, pk[(n) - NT][0], psel0), __builtin_amdgcn_perm(0u, pk[(n) - NT][0], psel1), \
                                 __builtin_amdgcn_perm(0u, pk[(n) - NT][1], psel0), __builtin_amdgcn_perm(0u, pk[(n) - NT][1], psel1)}; \
    _Pragma("unroll") for (int e = 0; e < 8; ++e) oacc[e] = mfma16(tr_frag(item[n], e), pf_, (n) == NT ? f32x4{0.f, 0.f, 0.f, 0.f} : oacc[e]); \
    asm volatile("" : "+v"(oacc[0]), "+v"(oacc[1]), "+v"(oacc[2]), "+v"(oacc[3]), "+v"(oacc[4]), "+v"(oacc[5]), "+v"(oacc[6]), "+v"(oacc[7])); }
#define CONSUME(n) { if constexpr ((n) < NT) SCORES(n, sc[(n) < NT ? (n) : 0]) else PV((n) < NT ? NT : (n)) }

  static_for<0, NT>([&](auto tc) __attribute__((always_inline)) {
    constexpr int t = decltype(tc)::value;
    CONSUME(2 * t);
    SB();
    if constexpr (t == 0) {
      epoch0 = __builtin_amdgcn_raw_buffer_load_b64(rw0, 0, WS_EPOCH + bp * 256, AUX_SC1);
      const int p_own = ((voff >> 7) & 3) * 8192 + (voff >> 9) * 128 + (voff & 0x70);
      __builtin_amdgcn_raw_buffer_store_b64(v2u{SENT, SENT}, rws, voff >> 1, o_slot + w * 512, AUX_SC1);
      store128_sc1(v4u{SENT, SENT, SENT, SENT}, rws, p_own, p_base + kq * 2048 + w * 256);
      MEMFENCE();
      SB();
    }
    if constexpr (t == NT - 5) flags_v = __builtin_amdgcn_raw_buffer_load_b64(rw0, (voff << 3) & 0xF80, WS_FLAG + bp * 4096, AUX_SC1);
    if constexpr (pace_at(t - 2)) {
      asm volatile("" : "+v"(snap));
      const int dt = (int)(snap[0] - tbase);
      const bool ok = snap[3] == ~(snap[0] ^ snap[1] ^ snap[2]) && dt > -1500 && dt < 12000;
      const int rs = ok ? (int)snap[1] : 0;
      const int nr = __builtin_popcount((unsigned)__builtin_amdgcn_ballot_w64(rs > 0) & 0xFFu);
      const int cm = (int)((float)__builtin_amdgcn_readfirstlane(sum8(rs)) * __builtin_amdgcn_rcpf((float)max(nr, 1)));
      const int cu = cm > PACE_C + (t - 2 <= 7 ? PACE_DZ1 : PACE_DZ2) ? min(cm - PACE_S0, PACE_C + 100) : PACE_C;
      const int v = sum8(dt - ((int)snap[2] * cu >> 4)) >> 3;
      const bool all_ok = (__builtin_amdgcn_ballot_w64(ok) & 0xFFull) == 0xFFull;
      int l0 = all_ok ? v : -1000000, l1 = cu;
      if constexpr (t > 3) {
        const int old_at = (int)(line0 - tbase) + (vpos(t) * (int)line1 >> 4);
        const int dl = __builtin_amdgcn_readfirstlane(l0) + (vpos(t) * cu >> 4) - old_at;
        if (dl > -200 && dl < 400) { l1 = cu + dl / 2; l0 = old_at - (vpos(t) * l1 >> 4); }
      }
      if (w == 0) *(LAS v2u*)(L + L_PC) = v2u{tbase + (unsigned)l0, (unsigned)l1};
    }
    if constexpr (t >= 4 && next_after(2 * t) >= 0) hold_until(line0 + ((vpos(t) - 7 + w) * line1 >> 4));
    if constexpr (t >= WO_S0 && t < WO_S0 + 4) LOAD_WO(t - WO_S0, 0);
    if constexpr (pace_at(t - 1)) {
      const int px = (voff >> 4) & 7;
      const int peer = (((bp + px) & 7) * 4 + ((i4 + px) & 3)) * 8 + ((xg + px) & 7);
      snap = __builtin_amdgcn_raw_buffer_load_b128(rw0, peer * 128, WS_PACE, AUX_SC1);
      SB();
    }
    if constexpr (next_after(2 * t) >= 0) LOAD_ITEM(next_after(2 * t));
    SB();
    if constexpr (pace_at(t - 2)) asm volatile("s_waitcnt lgkmcnt(0)" ::: "memory");
    if constexpr (t == NT - 3) {
      if (w == 0) {
        if (__builtin_expect(!__all(flags_v[0] == tag_lo && flags_v[1] == tag_hi), 0)) {
          for (unsigned spins = 0; spins < kSpinCap; ++spins) {
            __builtin_amdgcn_s_sleep(2);
            MEMFENCE();
            const v2u fv = __builtin_amdgcn_raw_buffer_load_b64(rws, (voff << 3) & 0xF80, WS_FLAG + bp * 4096, AUX_SC1);
            if (__all(fv[0] == tag_lo && fv[1] == tag_hi)) break;
          }
        }
        if (h == 0) __builtin_amdgcn_raw_buffer_store_b64(v2u{tag_lo, tag_hi}, rws, (voff >> 1) & 0x78, WS_EPOCH + bp * 256, AUX_SC1);
      }
      MEMFENCE();
      BAR();
      MEMFENCE();
    } else if constexpr (t < NT - 2) BAR();
    if constexpr (pace_at(t - 2)) {
      MEMFENCE();
      const v2u ln = *(LAS v2u*)(L + L_PC);
      line0 = __builtin_amdgcn_readfirstlane(ln[0]); line1 = __builtin_amdgcn_readfirstlane(ln[1]);
    }
    if constexpr (pace_at(t)) {
      const unsigned tv = now32();
      const unsigned rate = t >= 7 ? (tv - tprev - PACE_WO) * 16u / (unsigned)(t > 1 ? vpos(t) - 16 : 1) : 0u;
      if constexpr (t == 1) tprev = tv;
      store128_sc1(v4u{tv, rate, (unsigned)vpos(t), ~(tv ^ rate ^ (unsigned)vpos(t))}, rw0, voff & 0x70, WS_PACE + wg * 128);
    }
    if constexpr (t == NT - 1) BACK_WO(1);
    SB();
    CONSUME(2 * t + 1);
    SB();
    if constexpr (t == WO_S0 + 1) PARK_WO(0);
    if constexpr (t == WO_S0 + 2) PARK_WO(1);
    if constexpr (t == WO_S0 + 3) KEEP_WO(2);
    if constexpr (t == WO_S0 + 4) KEEP_WO(3);
    SB();
    if constexpr (t >= 3 && t < NT - 1) hold_until(line0 + ((vpos(t) + 1 + w) * line1 >> 4));
    if constexpr (t >= WO_S0 && t < WO_S0 + 4) { LOAD_WO(t - WO_S0, 4); SB(); }
    if constexpr (next_after(2 * t + 1) >= 0) LOAD_ITEM(next_after(2 * t + 1));
    SB();
    if constexpr (t == NT - 2) { BACK_WO(0); SB(); }
    if constexpr (t == NT / 2 - 1) {
      float m = fmaxf(fmaxf(sc[0][0], sc[0][1]), fmaxf(sc[0][2], sc[0][3]));
#pragma unroll
      for (int k = 1; k < NT; ++k) m = fmaxf(fmaxf(fmaxf(m, sc[k][0]), sc[k][1]), fmaxf(sc[k][2], sc[k][3]));
      m = xg_max(m);
      const float mc = m * CEXP;
      float l0 = 0.f, l1 = 0.f;
#pragma unroll
      for (int k = 0; k < NT; ++k) {
#pragma unroll
        for (int u = 0; u < 4; ++u) sc[k][u] = __builtin_amdgcn_exp2f(__builtin_fmaf(sc[k][u], CEXP, -mc));
        l0 += sc[k][0] + sc[k][1]; l1 += sc[k][2] + sc[k][3];
      }
      const float lsum = xg_sum(l0 + l1);
      LAS unsigned char* ml = L + L_ML + beta * 512 + ((voff >> 4) & 7) * 64;
      if (voff < 256) *(LAS f32x2*)(ml + ws4 * 16 + (voff >> 7) * 8) = f32x2{mc, lsum};
      asm volatile("s_waitcnt vmcnt(0)" ::: "memory");
      LDS_BAR();
      {
        const unsigned long long tag64 = (((unsigned long long)(unsigned)__builtin_amdgcn_readfirstlane(epoch0[1]) << 32) |
                                          (unsigned)__builtin_amdgcn_readfirstlane(epoch0[0])) + 1ull;
        tag_lo = (unsigned)tag64; tag_hi = (unsigned)(tag64 >> 32);
        __builtin_amdgcn_raw_buffer_store_b64(v2u{tag_lo, tag_hi}, rw0, (voff >> 1) & 0x78, WS_FLAG + (bp * 32 + h) * 128, AUX_SC1);
      }
      f32x4 rec[4];
#pragma unroll
      for (int j = 0; j < 4; ++j) rec[j] = *(LAS f32x4*)(ml + j * 16);
      const float M = fmaxf(fmaxf(fmaxf(rec[0][0], rec[0][2]), fmaxf(rec[1][0], rec[1][2])), fmaxf(fmaxf(rec[2][0], rec[2][2]), fmaxf(rec[3][0], rec[3][2])));
      float Ls = 0.f;
#pragma unroll
      for (int j = 0; j < 4; ++j) Ls += __builtin_amdgcn_exp2f(rec[j][0] - M) * rec[j][1] + __builtin_amdgcn_exp2f(rec[j][2] - M) * rec[j][3];
      const float scale = __builtin_amdgcn_exp2f(mc - M) / Ls;
#pragma unroll
      for (int k = 0; k < NT; ++k) {
        pk[k][0] = cvt_pk(sc[k][0] * scale, sc[k][1] * scale); pk[k][1] = cvt_pk(sc[k][2] * scale, sc[k][3] * scale);
        asm volatile("" : "+v"(pk[k][0]), "+v"(pk[k][1]));
      }
      SB();
    }
  });
  const int lane2 = __builtin_amdgcn_mbcnt_hi(~0u, __builtin_amdgcn_mbcnt_lo(~0u, 0u));
  asm volatile("s_waitcnt lgkmcnt(0)" ::: "memory");
#pragma unroll
  for (int e = 0; e < 8; ++e) *(LAS f32x4*)(LW + e * 1024 + lane2 * 16) = oacc[e];
  LDS_BAR();
  {
    const int fl = 32 * (w & 1) + (lane2 >> 1);
    const LAS unsigned char* src = L + ((fl >> 3) & 1) * (4 * L_WAVE) + ((w & ~1) + (lane2 & 1)) * 1024 + (fl & 0x37) * 16;
    const int par = (lane2 & 1) * 128;
    f32x4 pa[8];
#pragma unroll
    for (int j = 0; j < 8; ++j) pa[j] = *(const LAS f32x4*)(src + (j >> 1) * L_WAVE + ((j & 1) ? 128 - par : par));
    const f32x4 a0 = ((pa[0] + pa[1]) + (pa[2] + pa[3])) + ((pa[4] + pa[5]) + (pa[6] + pa[7]));
    v2u ov = {cvt_pk(a0[0], a0[1]), cvt_pk(a0[2], a0[3])};
#pragma unroll
    for (int j = 0; j < 2; ++j) ov[j] = (ov[j] == SENT) ? 0x7FC07FC0u : ov[j];
    __builtin_amdgcn_raw_buffer_store_b64(ov, rws, lane2 * 8, o_slot + w * 512, AUX_SC1);
  }
  SB();
  {
    const int goff = WS_O + (bp * 128 + 4 * (8 * kq + w)) * 1024;
    unsigned spins = 0;
#pragma unroll 1
    for (; spins < kSpinCap; ++spins) {
      MEMFENCE();
      if (__any(__builtin_amdgcn_raw_buffer_load_b32(rws, 0, goff, AUX_SC1) != SENT)) break;
      __builtin_amdgcn_s_sleep(1);
    }
    v4u ob[4];
    for (;; ++spins) {
      MEMFENCE();
#pragma unroll
      for (int ks = 0; ks < 4; ++ks) ob[ks] = __builtin_amdgcn_raw_buffer_load_b128(rws, lane2 * 16, goff + ((ks + w) & 3) * 1024, AUX_SC1);
      if (!__any(max(max(umax4(ob[0]), umax4(ob[1])), max(umax4(ob[2]), umax4(ob[3]))) == SENT) || spins >= kSpinCap) break;
      __builtin_amdgcn_s_sleep(2);
    }
#pragma unroll
    for (int hf = 0; hf < 2; ++hf) {
      f32x4 c[4];
#pragma unroll
      for (int e = 0; e < 4; ++e) c[e] = f32x4{0.f, 0.f, 0.f, 0.f};
#pragma unroll
      for (int ks = 0; ks < 4; ++ks)
#pragma unroll
        for (int e = 0; e < 4; ++e) c[e] = mfma16(wf[ks][4 * hf + e], ob[ks], c[e]);
#pragma unroll
      for (int rho = 0; rho < 4; ++rho)
        *(LAS f32x4*)(LW + L_C + (((lane2 & 15) * 512 + (lane2 >> 4) * 128 + (lane2 & 7) * 16) ^ (rho * 32 + hf * 16))) =
            f32x4{c[0][rho], c[1][rho], c[2][rho], c[3][rho]};
    }
  }
  LDS_BAR();
  {
    const int row = 2 * w + (lane2 >> 5);
    const int so = row * 512 + (((lane2 & 31) * 16) ^ ((row & 7) * 16));
    f32x4 pa[8];
#pragma unroll
    for (int ww = 0; ww < 8; ++ww) pa[ww] = *(const LAS f32x4*)(L + ww * L_WAVE + L_C + so);
    const f32x4 s = ((pa[0] + pa[1]) + (pa[2] + pa[3])) + ((pa[4] + pa[5]) + (pa[6] + pa[7]));
    v4u sv = __builtin_bit_cast(v4u, s);
#pragma unroll
    for (int j = 0; j < 4; ++j) sv[j] = (sv[j] == SENT) ? 0x7FC00000u : sv[j];
    const int p_own2 = ((lane2 >> 3) & 3) * 8192 + (lane2 >> 5) * 128 + (lane2 & 7) * 16;
    store128_sc1(sv, rws, p_own2, p_base + kq * 2048 + w * 256);
  }
  {
    const int poff = (lane2 >> 4) * 2048 + (lane2 & 15) * 16;
    int oo = ((lane2 >> 3) & 1) * (DM * 2) + (lane2 & 7) * 8 + (lane2 >> 5) * 4 + (lane2 & 16) * 0x1000000;
    asm volatile("" : "+v"(oo));
    v4u pv;
    __builtin_amdgcn_s_sleep(8);
    for (unsigned spins = 0;; ++spins) {
      MEMFENCE();
      pv = __builtin_amdgcn_raw_buffer_load_b128(rws, poff, p_base + kq * 8192 + w * 256, AUX_SC1);
      if (!__any(umax4(pv) == SENT) || spins >= kSpinCap) break;
      __builtin_amdgcn_s_sleep(2);
    }
    const float u0 = rs32(__uint_as_float(pv[0]), __uint_as_float(pv[2])), u1 = rs32(__uint_as_float(pv[1]), __uint_as_float(pv[3]));
    const float t0 = rs16(u0, u0), t1 = rs16(u1, u1);
    __builtin_amdgcn_raw_buffer_store_b32(cvt_pk(t0, t1), rout, oo, out_off, 0);
  }
}

}

extern "C" void kernel_launch(void* const* d_in, const int* in_sizes, int n_in, void* d_out, int out_size, void* d_ws,
                              size_t ws_size, hipStream_t stream) {
  (void)in_sizes; (void)n_in; (void)out_size; (void)ws_size;
  (void)hipFuncSetAttribute((const void*)mha_fused, hipFuncAttributeMaxDynamicSharedMemorySize, L_BYTES);
  hipLaunchKernelGGL(mha_fused, dim3(NWG), dim3(NTHR), L_BYTES, stream, (const unsigned short*)d_in[0], (const unsigned short*)d_in[1],
                     (const unsigned short*)d_in[2], (const unsigned short*)d_in[3], (const unsigned short*)d_in[4],
                     (unsigned short*)d_out, (unsigned char*)d_ws);
}
```

```cpp
#include <hip/hip_runtime.h>
#include <hip/hip_bf16.h>

#define LAS __attribute__((address_space(3)))
typedef unsigned v4u __attribute__((ext_vector_type(4)));
typedef unsigned v2u __attribute__((ext_vector_type(2)));
typedef float f32x4 __attribute__((ext_vector_type(4)));
typedef __bf16 bf16x8 __attribute__((ext_vector_type(8)));
typedef __bf16 bf16x2 __attribute__((ext_vector_type(2)));
typedef float f32x2 __attribute__((ext_vector_type(2)));

namespace {

constexpr int NB = 16, SQ = 8, DM = 1024, NH = 32, SK = 2048, DH = 128;
constexpr int NWG = 256, NTHR = 512;
constexpr int NT = 16;
constexpr float CEXP = 0.08838834764831845f * 1.4426950408889634f;
constexpr int AUX_NT = 2, AUX_SC1 = 16;
constexpr int RSRC_W3 = 0x00020000;
constexpr unsigned SENT = 0xFFFFFFFFu;
constexpr unsigned kSpinCap = 1u << 16;

constexpr int PACE_C = 446;
constexpr int PACE_DZ1 = 14, PACE_DZ2 = 12;
constexpr int PACE_S0 = 4;
constexpr int PACE_WO = 20;
constexpr int PACE_MAX = 500;
constexpr int WO_S0 = 2;

constexpr int WS_EPOCH = 0;
constexpr int WS_SPARE = 2048;
constexpr int WS_FLAG = 4096;
constexpr int WS_PACE = WS_FLAG + 32768;
constexpr int WS_O = 131072;
constexpr int WS_P = WS_O + 8 * 128 * 1024;
constexpr int WS_BYTES = WS_P + 8 * 8 * 16 * 2048;

constexpr int L_WAVE = 16384;
constexpr int L_QS = 8 * L_WAVE;
constexpr int QS_STRIDE = 272;
constexpr int L_ML = L_QS + 16 * QS_STRIDE;
constexpr int L_PC = L_ML + 1024;
constexpr int L_BYTES = L_PC + 16;
constexpr int QP_STRIDE = 528;
constexpr int L_X = 9216, X_ODD = 2064;
constexpr int L_C = 8192;

__device__ __forceinline__ f32x4 mfma16(v4u a, v4u b, f32x4 c) {
  return __builtin_amdgcn_mfma_f32_16x16x32_bf16(__builtin_bit_cast(bf16x8, a), __builtin_bit_cast(bf16x8, b), c, 0, 0, 0);
}
__device__ __forceinline__ unsigned pack_lo(unsigned a, unsigned b) { return __builtin_amdgcn_perm(b, a, 0x05040100u); }
__device__ __forceinline__ unsigned pack_hi(unsigned a, unsigned b) { return __builtin_amdgcn_perm(b, a, 0x07060302u); }
__device__ __forceinline__ unsigned cvt_pk(float lo, float hi) {
  f32x2 v = {lo, hi};
  return __builtin_bit_cast(unsigned, __builtin_convertvector(v, bf16x2));
}
__device__ __forceinline__ v4u tr_frag(const v4u (&R)[8], int e) {
  v4u f;
#pragma unroll
  for (int jj = 0; jj < 4; ++jj)
    f[jj] = (e & 1) ? pack_hi(R[2 * jj][e >> 1], R[2 * jj + 1][e >> 1]) : pack_lo(R[2 * jj][e >> 1], R[2 * jj + 1][e >> 1]);
  return f;
}
template <int CTRL> __device__ __forceinline__ int dppi(int x) { return __builtin_amdgcn_mov_dpp(x, CTRL, 0xf, 0xf, true); }
__device__ __forceinline__ int sum8(int v) { v += dppi<0xB1>(v); v += dppi<0x4E>(v); return v + dppi<0x141>(v); }
__device__ __forceinline__ float ror8(float x) {
  return __builtin_bit_cast(float, dppi<0x128>(__builtin_bit_cast(int, x)));
}
__device__ __forceinline__ float bsel(unsigned mask, float a, float b) {
  return __uint_as_float((__float_as_uint(a) & mask) | (__float_as_uint(b) & ~mask));
}
__device__ __forceinline__ float xg_max(float x) {
  auto s = __builtin_amdgcn_permlane16_swap(__float_as_uint(x), __float_as_uint(x), false, false);
  x = fmaxf(__uint_as_float(s[0]), __uint_as_float(s[1]));
  auto t = __builtin_amdgcn_permlane32_swap(__float_as_uint(x), __float_as_uint(x), false, false);
  return fmaxf(__uint_as_float(t[0]), __uint_as_float(t[1]));
}
__device__ __forceinline__ float rs32(float a, float b) {
  auto t = __builtin_amdgcn_permlane32_swap(__float_as_uint(a), __float_as_uint(b), false, false);
  return __uint_as_float(t[0]) + __uint_as_float(t[1]);
}
__device__ __forceinline__ float rs16(float a, float b) {
  auto t = __builtin_amdgcn_permlane16_swap(__float_as_uint(a), __float_as_uint(b), false, false);
  return __uint_as_float(t[0]) + __uint_as_float(t[1]);
}
__device__ __forceinline__ float xg_sum(float x) { x = rs16(x, x); return rs32(x, x); }
__device__ __forceinline__ unsigned umax4(v4u v) { return max(max(v[0], v[1]), max(v[2], v[3])); }
__device__ __forceinline__ unsigned now32() { return (unsigned)__builtin_amdgcn_s_memrealtime(); }
__device__ __forceinline__ void hold_until(unsigned t_abs) {
  const unsigned now = now32();
  int d = min((int)(t_abs - now), PACE_MAX);
  const unsigned lim = now + (unsigned)d;
  while (d > 0) { __builtin_amdgcn_s_sleep(1); d = (int)(lim - now32()); }
}
__device__ __forceinline__ void store128_sc1(v4u v, __amdgpu_buffer_rsrc_t rs, int voffset, int soffset) {
  asm volatile("" : "+v"(v));
  __builtin_amdgcn_raw_buffer_store_b128(v, rs, voffset, soffset, 16);
  __builtin_amdgcn_sched_barrier(0);
  asm volatile("s_nop 2" :: "v"(v));
  __builtin_amdgcn_sched_barrier(0);
}
#define SB() __builtin_amdgcn_sched_barrier(0)
#define BAR() __builtin_amdgcn_s_barrier()
#define MEMFENCE() asm volatile("" ::: "memory")
#define LDS_BAR() { asm volatile("s_waitcnt lgkmcnt(0)" ::: "memory"); BAR(); MEMFENCE(); }

template <int V> struct IC { static constexpr int value = V; };
template <int I, int N, class F> __device__ __forceinline__ void static_for(F&& f) {
  if constexpr (I < N) { f(IC<I>{}); static_for<I + 1, N>(f); }
}
constexpr int next_after(int n) { return n < 2 ? n + 3 : (n == 2 || n + 2 >= 2 * NT) ? -1 : n + 2; }
constexpr bool pace_at(int t) { return t == 1 || t == 4 || t == 7 || t == 10; }

__global__ void __launch_bounds__(NTHR, 2)
mha_fused(const unsigned short* __restrict__ x, const unsigned short* __restrict__ kc, const unsigned short* __restrict__ vc,
          const unsigned short* __restrict__ wq, const unsigned short* __restrict__ wo, unsigned short* out, unsigned char* ws) {
  extern __shared__ __attribute__((aligned(16))) unsigned char smem[];
  LAS unsigned char* L = (LAS unsigned char*)smem;
  const int tid = threadIdx.x, lane = tid & 63, w = __builtin_amdgcn_readfirstlane(tid >> 6);
  const int r = lane & 15, g = lane >> 4;
  const int wg = blockIdx.x, idx = wg >> 3, xg = wg & 7, i4 = idx & 3;
  const int h = 4 * xg + i4, bp = idx >> 2;
  const int kq = xg >> 1, ct = 4 * (xg & 1) + ((i4 + 2) & 3);
  const int beta = w >> 2, ws4 = w & 3;
  const int bh = (2 * bp + beta) * NH + h;
  int rot = (4 * h + bp + 8 * beta) & (NT - 1);
  int rotv = rot;
  asm volatile("" : "+s"(rotv));
  LAS unsigned char* LW = L + w * L_WAVE;

  asm volatile("" : "+s"(out));
  const __amdgpu_buffer_rsrc_t rk = __builtin_amdgcn_make_buffer_rsrc((void*)(kc + (size_t)bh * SK * DH), 0, SK * DH * 2, RSRC_W3);
  const __amdgpu_buffer_rsrc_t rv = __builtin_amdgcn_make_buffer_rsrc((void*)(vc + (size_t)bh * SK * DH), 0, SK * DH * 2, RSRC_W3);
  const __amdgpu_buffer_rsrc_t rws = __builtin_amdgcn_make_buffer_rsrc((void*)ws, 0, WS_BYTES, RSRC_W3);
  const __amdgpu_buffer_rsrc_t rw0 = __builtin_amdgcn_make_buffer_rsrc((void*)ws, 0, w == 0 ? WS_BYTES : 0, RSRC_W3);
  const __amdgpu_buffer_rsrc_t rwq = __builtin_amdgcn_make_buffer_rsrc((void*)wq, 0, DM * NH * DH * 2, RSRC_W3);
  const __amdgpu_buffer_rsrc_t rwo = __builtin_amdgcn_make_buffer_rsrc((void*)wo, 0, NH * DH * DM * 2, RSRC_W3);
  const __amdgpu_buffer_rsrc_t rx = __builtin_amdgcn_make_buffer_rsrc((void*)x, 0, NB * SQ * DM * 2, RSRC_W3);
  const __amdgpu_buffer_rsrc_t rout = __builtin_amdgcn_make_buffer_rsrc((void*)out, 0, NB * SQ * DM * 2, RSRC_W3);

  const int koff = (4 * ((r >> 1) & 1) + (r >> 2)) * 256 + (2 * g + (r & 1)) * 16;
  const int voff = lane * 16;
  v4u item[2 * NT][8];
#define LOAD_ITEM(n) { const int so_ = ((((n) + ((n) >= NT ? rotv : rot)) & (NT - 1)) * 4 + ws4) * 8192; _Pragma("unroll") for (int i_ = 0; i_ < 8; ++i_) \
    item[n][i_] = ((n) >= NT) ? __builtin_amdgcn_raw_buffer_load_b128(rv, voff, so_ + i_ * 1024, AUX_NT) \
                            : __builtin_amdgcn_raw_buffer_load_b128(rk, koff, so_ + (i_ >> 1) * 2048 + (i_ & 1) * 128, AUX_NT); }

  const int o_slot = WS_O + (bp * 128 + 4 * h) * 1024;
  const int p_base = WS_P + (bp * 8 + ct) * (16 * 2048);
  const int out_off = ((16 * bp + 2 * w) * DM + 128 * ct + 32 * kq) * 2;

  unsigned tch[5];
  {
    typedef const __attribute__((address_space(4))) unsigned* cptr;
    tch[0] = *(cptr)(kc + (size_t)bh * SK * DH);
    tch[1] = *(cptr)(vc + (size_t)bh * SK * DH);
    tch[2] = *(cptr)(wo + (size_t)(1024 * kq + 128 * w) * DM);
    tch[3] = *(cptr)(ws + WS_SPARE + w * 128);
    tch[4] = *(cptr)(ws + WS_BYTES + w * 128);
    SB();
  }
  {
#define KBLK(s) ((((4 * beta + (s) + bp) & 7) << 2) | ws4)
    const int wqoff = (8 * g) * (NH * DH * 2) + r * 16;
    v4u wa[4][8], xr[4];
#define LOAD_WQ(s) { const int so_ = KBLK(s) * 32 * (NH * DH * 2) + h * (DH * 2); _Pragma("unroll") for (int j_ = 0; j_ < 8; ++j_) \
      wa[s][j_] = __builtin_amdgcn_raw_buffer_load_b128(rwq, wqoff, so_ + j_ * (NH * DH * 2), 0); }
#define LOAD_X() { _Pragma("unroll") for (int i_ = 0; i_ < 4; ++i_) \
      xr[i_] = __builtin_amdgcn_raw_buffer_load_b128(rx, voff, (16 * bp + 2 * w) * (DM * 2) + i_ * 1024, 0); }
#define STAGE_X() { _Pragma("unroll") for (int i_ = 0; i_ < 4; ++i_) \
      *(LAS v4u*)(LW + L_X + w * 32 + (i_ >> 1) * X_ODD + (i_ & 1) * 1024 + voff) = xr[i_]; }
    const LAS unsigned char* xrow = L + (r >> 1) * (L_WAVE + 32) + L_X + (r & 1) * X_ODD + g * 16;
    v4u pfw[4];
#define PF_WO() { const int so_ = (1024 * kq + 128 * w + bp) * (DM * 2) + ct * 256; _Pragma("unroll") for (int j_ = 0; j_ < 4; ++j_) \
      pfw[j_] = __builtin_amdgcn_raw_buffer_load_b128(rwo, (32 * g) * (DM * 2) + r * 16, so_ + 8 * j_ * (DM * 2), 0); }
#define PF_END() asm volatile("" :: "v"(pfw[0]), "v"(pfw[1]), "v"(pfw[2]), "v"(pfw[3]))
    f32x4 qa[8];
#pragma unroll
    for (int e = 0; e < 8; ++e) qa[e] = f32x4{0.f, 0.f, 0.f, 0.f};
#define MMA_WQ(s) { const v4u xf_ = *(const LAS v4u*)(xrow + KBLK(s) * 64); \
      _Pragma("unroll") for (int e_ = 0; e_ < 8; ++e_) qa[e_] = mfma16(tr_frag(wa[s], e_), xf_, qa[e_]); }
#define PUT_Q() { _Pragma("unroll") for (int rho = 0; rho < 4; ++rho) { \
      LAS f32x4* p_ = (LAS f32x4*)(LW + r * QP_STRIDE + (32 * g + 8 * rho) * 4); \
      p_[0] = f32x4{qa[0][rho], qa[1][rho], qa[2][rho], qa[3][rho]}; \
      p_[1] = f32x4{qa[4][rho], qa[5][rho], qa[6][rho], qa[7][rho]}; } }
#define REDUCE_Q() { const int row_ = tid >> 5, n4_ = (tid & 31) * 4; f32x4 pa_[8]; \
      _Pragma("unroll") for (int ww = 0; ww < 8; ++ww) pa_[ww] = *(LAS f32x4*)(L + ww * L_WAVE + row_ * QP_STRIDE + n4_ * 4); \
      const f32x4 s_ = ((pa_[0] + pa_[1]) + (pa_[2] + pa_[3])) + ((pa_[4] + pa_[5]) + (pa_[6] + pa_[7])); \
      *(LAS v2u*)(L + L_QS + row_ * QS_STRIDE + n4_ * 2) = v2u{cvt_pk(s_[0], s_[1]), cvt_pk(s_[2], s_[3])}; }
    if ((bp & 1) == 0) {
      asm volatile("; path A" ::: "memory");
      LOAD_X(); SB(); LOAD_WQ(0); SB(); LOAD_WQ(1); SB(); LOAD_WQ(2); SB(); LOAD_WQ(3); SB();
      PF_WO(); SB();
      STAGE_X(); SB();
      LDS_BAR(); SB();
      LOAD_ITEM(0); SB();
      MMA_WQ(0); MMA_WQ(1); MMA_WQ(2); MMA_WQ(3);
      PUT_Q(); SB();
      PF_END(); SB();
      LOAD_ITEM(1); SB();
      LDS_BAR(); SB();
      REDUCE_Q(); SB();
      LOAD_ITEM(2); SB();
    } else {
      asm volatile("; path B" ::: "memory");
      LOAD_X(); SB(); LOAD_WQ(0); SB();
      PF_WO(); SB();
      LOAD_ITEM(0); SB(); LOAD_ITEM(1); SB();
      STAGE_X(); SB();
      PF_END(); SB();
      LDS_BAR(); SB();
      LOAD_WQ(1); SB(); LOAD_WQ(2); SB();
      MMA_WQ(0); SB();
      LOAD_WQ(3); SB();
      MMA_WQ(1); SB();
      LOAD_ITEM(2); SB();
      MMA_WQ(2); MMA_WQ(3);
      PUT_Q(); SB();
      LDS_BAR(); SB();
      REDUCE_Q(); SB();
    }
  }
  asm volatile("" :: "s"(tch[0]), "s"(tch[1]), "s"(tch[2]), "s"(tch[3]), "s"(tch[4]));
  LDS_BAR();
  asm volatile("" : "+s"(rot));
  v4u qf[2];
#pragma unroll
  for (int hf = 0; hf < 2; ++hf) qf[hf] = *(LAS v4u*)(L + L_QS + (8 * beta + (r & 7)) * QS_STRIDE + (8 * hf + 2 * g + (r >> 3)) * 16);
  unsigned hmask = (r >> 3) ? 0xFFFFFFFFu : 0u;
  asm volatile("" : "+v"(hmask));
  const unsigned psel0 = (hmask & 0x01000C0Cu) | (~hmask & 0x0C0C0100u), psel1 = (hmask & 0x03020C0Cu) | (~hmask & 0x0C0C0302u);

  v4u wr[4][8], wf[4][8];
  const int wooff = (32 * g) * (DM * 2) + r * 16;
  const int wos = (1024 * kq + 128 * w) * (DM * 2) + ct * 256;
#define LOAD_WO(ks, j0) { _Pragma("unroll") for (int j_ = j0; j_ < j0 + 4; ++j_) \
    wr[ks][j_] = __builtin_amdgcn_raw_buffer_load_b128(rwo, wooff, wos + (8 * (j_ & 3) + 2 * (((ks) + w) & 3) + (j_ >> 2)) * (DM * 2), 0); }
#define PARK_WO(ks) { _Pragma("unroll") for (int e_ = 0; e_ < 8; ++e_) *(LAS v4u*)(LW + (ks) * 8192 + e_ * 1024 + voff) = tr_frag(wr[ks], e_); }
#define KEEP_WO(ks) { _Pragma("unroll") for (int e_ = 0; e_ < 8; ++e_) { wf[ks][e_] = tr_frag(wr[ks], e_); asm volatile("" : "+v"(wf[ks][e_])); } }
#define BACK_WO(ks) { _Pragma("unroll") for (int e_ = 0; e_ < 8; ++e_) wf[ks][e_] = *(LAS v4u*)(LW + (ks) * 8192 + e_ * 1024 + voff); }
#define SCORES(n, s) { f32x4 sa_[4]; \
    _Pragma("unroll") for (int u = 0; u < 4; ++u) sa_[u] = mfma16(item[n][2 * u], qf[0], f32x4{0.f, 0.f, 0.f, 0.f}); \
    _Pragma("unroll") for (int u = 0; u < 4; ++u) sa_[u] = mfma16(item[n][2 * u + 1], qf[1], sa_[u]); \
    _Pragma("unroll") for (int u = 0; u < 4; ++u) (s)[u] = bsel(hmask, sa_[u][3], sa_[u][0]) + ror8(bsel(hmask, sa_[u][1], sa_[u][2])); \
    asm volatile("" : "+v"((s)[0]), "+v"((s)[1]), "+v"((s)[2]), "+v"((s)[3])); }

  f32x4 oacc[8];
  float sc[NT][4];
  unsigned pk[NT][2];
  float mcl, scale;
  v2u epoch0, flags_v;
  unsigned tag_lo = 0, tag_hi = 0;
  v4u snap;
  const unsigned tbase = now32();
  unsigned line0 = 0, line1 = 0, tprev = 0;
#define PV(n) { const v4u pf_ = {__builtin_amdgcn_perm(0u, pk[(n) - NT][0], psel0), __builtin_amdgcn_perm(0u, pk[(n) - NT][0], psel1), \
                                 __builtin_amdgcn_perm(0u, pk[(n) - NT][1], psel0), __builtin_amdgcn_perm(0u, pk[(n) - NT][1], psel1)}; \
    _Pragma("unroll") for (int e = 0; e < 8; ++e) oacc[e] = mfma16(tr_frag(item[n], e), pf_, (n) == NT ? f32x4{0.f, 0.f, 0.f, 0.f} : oacc[e]); \
    asm volatile("" : "+v"(oacc[0]), "+v"(oacc[1]), "+v"(oacc[2]), "+v"(oacc[3]), "+v"(oacc[4]), "+v"(oacc[5]), "+v"(oacc[6]), "+v"(oacc[7])); }
#define CONSUME(n) { if constexpr ((n) < NT) SCORES(n, sc[(n) < NT ? (n) : 0]) else PV((n) < NT ? NT : (n)) }

  static_for<0, NT>([&](auto tc) __attribute__((always_inline)) {
    constexpr int t = decltype(tc)::value;
    CONSUME(2 * t);
    SB();
    if constexpr (t == 0) {
      epoch0 = __builtin_amdgcn_raw_buffer_load_b64(rw0, 0, WS_EPOCH + bp * 256, AUX_SC1);
      const int p_own = ((voff >> 7) & 3) * 8192 + (voff >> 9) * 128 + (voff & 0x70);
      __builtin_amdgcn_raw_buffer_store_b64(v2u{SENT, SENT}, rws, voff >> 1, o_slot + w * 512, AUX_SC1);
      store128_sc1(v4u{SENT, SENT, SENT, SENT}, rws, p_own, p_base + kq * 2048 + w * 256);
      MEMFENCE();
      SB();
    }
    if constexpr (t == NT - 5) flags_v = __builtin_amdgcn_raw_buffer_load_b64(rw0, (voff << 3) & 0xF80, WS_FLAG + bp * 4096, AUX_SC1);
    if constexpr (pace_at(t - 2)) {
      asm volatile("" : "+v"(snap));
      const int dt = (int)(snap[0] - tbase);
      const bool ok = snap[3] == ~(snap[0] ^ snap[1] ^ snap[2]) && dt > -1500 && dt < 12000;
      const int rs = ok ? (int)snap[1] : 0;
      const int nr = __builtin_popcount((unsigned)__builtin_amdgcn_ballot_w64(rs > 0) & 0xFFu);
      const int cm = (int)((float)__builtin_amdgcn_readfirstlane(sum8(rs)) * __builtin_amdgcn_rcpf((float)max(nr, 1)));
      const int cu = cm > PACE_C + (t - 2 <= 7 ? PACE_DZ1 : PACE_DZ2) ? min(cm - PACE_S0, PACE_C + 100) : PACE_C;
      const int v = sum8(dt - (int)snap[2] * cu) >> 3;
      const bool all_ok = (__builtin_amdgcn_ballot_w64(ok) & 0xFFull) == 0xFFull;
      int l0 = all_ok ? v : -1000000, l1 = cu;
      if constexpr (t > 3) {
        const int old_at = (int)(line0 - tbase) + t * (int)line1;
        const int dl = __builtin_amdgcn_readfirstlane(l0) + t * cu - old_at;
        if (dl > -200 && dl < 400) { l1 = cu + dl / 2; l0 = old_at - t * l1; }
      }
      if (w == 0) *(LAS v2u*)(L + L_PC) = v2u{tbase + (unsigned)l0, (unsigned)l1};
    }
    if constexpr (t >= 4 && next_after(2 * t) >= 0) hold_until(line0 + ((16 * t - 7 + w) * line1 >> 4));
    if constexpr (t == NT / 2) asm volatile("s_waitcnt vmcnt(0) lgkmcnt(0)" ::: "memory");
    if constexpr (t >= WO_S0 && t < WO_S0 + 4) LOAD_WO(t - WO_S0, 0);
    if constexpr (pace_at(t - 1)) {
      const int px = (voff >> 4) & 7;
      const int peer = (((bp + px) & 7) * 4 + ((i4 + px) & 3)) * 8 + ((xg + px) & 7);
      snap = __builtin_amdgcn_raw_buffer_load_b128(rw0, peer * 128, WS_PACE, AUX_SC1);
      SB();
    }
    if constexpr (next_after(2 * t) >= 0) LOAD_ITEM(next_after(2 * t));
    SB();
    if constexpr (pace_at(t - 2)) asm volatile("s_waitcnt lgkmcnt(0)" ::: "memory");
    if constexpr (t == NT / 2) {
      const unsigned long long tag64 = (((unsigned long long)(unsigned)__builtin_amdgcn_readfirstlane(epoch0[1]) << 32) |
                                        (unsigned)__builtin_amdgcn_readfirstlane(epoch0[0])) + 1ull;
      tag_lo = (unsigned)tag64; tag_hi = (unsigned)(tag64 >> 32);
      MEMFENCE();
      BAR();
      MEMFENCE();
      __builtin_amdgcn_raw_buffer_store_b64(v2u{tag_lo, tag_hi}, rw0, (voff >> 1) & 0x78, WS_FLAG + (bp * 32 + h) * 128, AUX_SC1);
    } else if constexpr (t == NT - 3) {
      if (w == 0) {
        if (__builtin_expect(!__all(flags_v[0] == tag_lo && flags_v[1] == tag_hi), 0)) {
          for (unsigned spins = 0; spins < kSpinCap; ++spins) {
            __builtin_amdgcn_s_sleep(2);
            MEMFENCE();
            const v2u fv = __builtin_amdgcn_raw_buffer_load_b64(rws, (voff << 3) & 0xF80, WS_FLAG + bp * 4096, AUX_SC1);
            if (__all(fv[0] == tag_lo && fv[1] == tag_hi)) break;
          }
        }
        if (h == 0) __builtin_amdgcn_raw_buffer_store_b64(v2u{tag_lo, tag_hi}, rws, (voff >> 1) & 0x78, WS_EPOCH + bp * 256, AUX_SC1);
      }
      MEMFENCE();
      BAR();
      MEMFENCE();
    } else if constexpr (t < NT - 2) BAR();
    if constexpr (pace_at(t - 2)) {
      MEMFENCE();
      const v2u ln = *(LAS v2u*)(L + L_PC);
      line0 = __builtin_amdgcn_readfirstlane(ln[0]); line1 = __builtin_amdgcn_readfirstlane(ln[1]);
    }
    if constexpr (pace_at(t)) {
      const unsigned tv = now32();
      const unsigned rate = t >= 7 ? (tv - tprev - PACE_WO) / (unsigned)(t > 1 ? t - 1 : 1) : 0u;
      if constexpr (t == 1) tprev = tv;
      store128_sc1(v4u{tv, rate, (unsigned)t, ~(tv ^ rate ^ (unsigned)t)}, rw0, voff & 0x70, WS_PACE + wg * 128);
    }
    if constexpr (t == NT - 1) BACK_WO(1);
    SB();
    CONSUME(2 * t + 1);
    SB();
    if constexpr (t == WO_S0 + 1) PARK_WO(0);
    if constexpr (t == WO_S0 + 2) PARK_WO(1);
    if constexpr (t == WO_S0 + 3) KEEP_WO(2);
    if constexpr (t == WO_S0 + 4) KEEP_WO(3);
    SB();
    if constexpr (t >= 3 && t < NT - 1) hold_until(line0 + ((16 * t + 1 + w) * line1 >> 4));
    if constexpr (t >= WO_S0 && t < WO_S0 + 4) { LOAD_WO(t - WO_S0, 4); SB(); }
    if constexpr (next_after(2 * t + 1) >= 0) LOAD_ITEM(next_after(2 * t + 1));
    SB();
    if constexpr (t == NT - 2) { BACK_WO(0); SB(); }
    if constexpr (t == NT / 2 - 1) {
      float m = fmaxf(fmaxf(sc[0][0], sc[0][1]), fmaxf(sc[0][2], sc[0][3]));
#pragma unroll
      for (int k = 1; k < NT; ++k) m = fmaxf(fmaxf(fmaxf(m, sc[k][0]), sc[k][1]), fmaxf(sc[k][2], sc[k][3]));
      m = xg_max(m);
      const float mc = m * CEXP;
      float l0 = 0.f, l1 = 0.f;
#pragma unroll
      for (int k = 0; k < NT; ++k) {
#pragma unroll
        for (int u = 0; u < 4; ++u) sc[k][u] = __builtin_amdgcn_exp2f(__builtin_fmaf(sc[k][u], CEXP, -mc));
        l0 += sc[k][0] + sc[k][1]; l1 += sc[k][2] + sc[k][3];
      }
      const float lsum = xg_sum(l0 + l1);
      LAS unsigned char* ml = L + L_ML + beta * 512 + ((voff >> 4) & 7) * 64;
      if (voff < 256) *(LAS f32x2*)(ml + ws4 * 16 + (voff >> 7) * 8) = f32x2{mc, lsum};
      mcl = mc;
#pragma unroll
      for (int k = 0; k < NT - 2; ++k) {
        pk[k][0] = cvt_pk(sc[k][0], sc[k][1]); pk[k][1] = cvt_pk(sc[k][2], sc[k][3]);
        asm volatile("" : "+v"(pk[k][0]), "+v"(pk[k][1]));
      }
      asm volatile("" : "+v"(sc[NT - 2][0]), "+v"(sc[NT - 2][1]), "+v"(sc[NT - 2][2]), "+v"(sc[NT - 2][3]),
                        "+v"(sc[NT - 1][0]), "+v"(sc[NT - 1][1]), "+v"(sc[NT - 1][2]), "+v"(sc[NT - 1][3]));
      SB();
    }
    if constexpr (t == NT / 2) {
      LAS unsigned char* ml = L + L_ML + beta * 512 + ((voff >> 4) & 7) * 64;
      f32x4 rec[4];
#pragma unroll
      for (int j = 0; j < 4; ++j) rec[j] = *(LAS f32x4*)(ml + j * 16);
      const float M = fmaxf(fmaxf(fmaxf(rec[0][0], rec[0][2]), fmaxf(rec[1][0], rec[1][2])), fmaxf(fmaxf(rec[2][0], rec[2][2]), fmaxf(rec[3][0], rec[3][2])));
      float Ls = 0.f;
#pragma unroll
      for (int j = 0; j < 4; ++j) Ls += __builtin_amdgcn_exp2f(rec[j][0] - M) * rec[j][1] + __builtin_amdgcn_exp2f(rec[j][2] - M) * rec[j][3];
      scale = __builtin_amdgcn_exp2f(mcl - M) / Ls;
#pragma unroll
      for (int k = NT - 2; k < NT; ++k) {
        pk[k][0] = cvt_pk(sc[k][0] * scale, sc[k][1] * scale); pk[k][1] = cvt_pk(sc[k][2] * scale, sc[k][3] * scale);
        asm volatile("" : "+v"(pk[k][0]), "+v"(pk[k][1]));
      }
      asm volatile("" : "+v"(scale));
      SB();
    }
    if constexpr (t == NT - 2) {
#pragma unroll
      for (int e = 0; e < 8; ++e) oacc[e] *= scale;
      asm volatile("" : "+v"(oacc[0]), "+v"(oacc[1]), "+v"(oacc[2]), "+v"(oacc[3]), "+v"(oacc[4]), "+v"(oacc[5]), "+v"(oacc[6]), "+v"(oacc[7]));
      SB();
    }
  });
  const int lane2 = __builtin_amdgcn_mbcnt_hi(~0u, __builtin_amdgcn_mbcnt_lo(~0u, 0u));
  asm volatile("s_waitcnt lgkmcnt(0)" ::: "memory");
#pragma unroll
  for (int e = 0; e < 8; ++e) *(LAS f32x4*)(LW + e * 1024 + lane2 * 16) = oacc[e];
  LDS_BAR();
  {
    const int fl = 32 * (w & 1) + (lane2 >> 1);
    const LAS unsigned char* src = L + ((fl >> 3) & 1) * (4 * L_WAVE) + ((w & ~1) + (lane2 & 1)) * 1024 + (fl & 0x37) * 16;
    const int par = (lane2 & 1) * 128;
    f32x4 pa[8];
#pragma unroll
    for (int j = 0; j < 8; ++j) pa[j] = *(const LAS f32x4*)(src + (j >> 1) * L_WAVE + ((j & 1) ? 128 - par : par));
    const f32x4 a0 = ((pa[0] + pa[1]) + (pa[2] + pa[3])) + ((pa[4] + pa[5]) + (pa[6] + pa[7]));
    v2u ov = {cvt_pk(a0[0], a0[1]), cvt_pk(a0[2], a0[3])};
#pragma unroll
    for (int j = 0; j < 2; ++j) ov[j] = (ov[j] == SENT) ? 0x7FC07FC0u : ov[j];
    __builtin_amdgcn_raw_buffer_store_b64(ov, rws, lane2 * 8, o_slot + w * 512, AUX_SC1);
  }
  SB();
  {
    const int goff = WS_O + (bp * 128 + 4 * (8 * kq + w)) * 1024;
    unsigned spins = 0;
#pragma unroll 1
    for (; spins < kSpinCap; ++spins) {
      MEMFENCE();
      if (__any(__builtin_amdgcn_raw_buffer_load_b32(rws, 0, goff, AUX_SC1) != SENT)) break;
      __builtin_amdgcn_s_sleep(1);
    }
    v4u ob[4];
    for (;; ++spins) {
      MEMFENCE();
#pragma unroll
      for (int ks = 0; ks < 4; ++ks) ob[ks] = __builtin_amdgcn_raw_buffer_load_b128(rws, lane2 * 16, goff + ((ks + w) & 3) * 1024, AUX_SC1);
      if (!__any(max(max(umax4(ob[0]), umax4(ob[1])), max(umax4(ob[2]), umax4(ob[3]))) == SENT) || spins >= kSpinCap) break;
      __builtin_amdgcn_s_sleep(2);
    }
#pragma unroll
    for (int hf = 0; hf < 2; ++hf) {
      f32x4 c[4];
#pragma unroll
      for (int e = 0; e < 4; ++e) c[e] = f32x4{0.f, 0.f, 0.f, 0.f};
#pragma unroll
      for (int ks = 0; ks < 4; ++ks)
#pragma unroll
        for (int e = 0; e < 4; ++e) c[e] = mfma16(wf[ks][4 * hf + e], ob[ks], c[e]);
#pragma unroll
      for (int rho = 0; rho < 4; ++rho)
        *(LAS f32x4*)(LW + L_C + (((lane2 & 15) * 512 + (lane2 >> 4) * 128 + (lane2 & 7) * 16) ^ (rho * 32 + hf * 16))) =
            f32x4{c[0][rho], c[1][rho], c[2][rho], c[3][rho]};
    }
  }
  LDS_BAR();
  {
    const int row = 2 * w + (lane2 >> 5);
    const int so = row * 512 + (((lane2 & 31) * 16) ^ ((row & 7) * 16));
    f32x4 pa[8];
#pragma unroll
    for (int ww = 0; ww < 8; ++ww) pa[ww] = *(const LAS f32x4*)(L + ww * L_WAVE + L_C + so);
    const f32x4 s = ((pa[0] + pa[1]) + (pa[2] + pa[3])) + ((pa[4] + pa[5]) + (pa[6] + pa[7]));
    v4u sv = __builtin_bit_cast(v4u, s);
#pragma unroll
    for (int j = 0; j < 4; ++j) sv[j] = (sv[j] == SENT) ? 0x7FC00000u : sv[j];
    const int p_own2 = ((lane2 >> 3) & 3) * 8192 + (lane2 >> 5) * 128 + (lane2 & 7) * 16;
    store128_sc1(sv, rws, p_own2, p_base + kq * 2048 + w * 256);
  }
  {
    const int poff = (lane2 >> 4) * 2048 + (lane2 & 15) * 16;
    int oo = ((lane2 >> 3) & 1) * (DM * 2) + (lane2 & 7) * 8 + (lane2 >> 5) * 4 + (lane2 & 16) * 0x1000000;
    asm volatile("" : "+v"(oo));
    v4u pv;
    __builtin_amdgcn_s_sleep(8);
    for (unsigned spins = 0;; ++spins) {
      MEMFENCE();
      pv = __builtin_amdgcn_raw_buffer_load_b128(rws, poff, p_base + kq * 8192 + w * 256, AUX_SC1);
      if (!__any(umax4(pv) == SENT) || spins >= kSpinCap) break;
      __builtin_amdgcn_s_sleep(2);
    }
    const float u0 = rs32(__uint_as_float(pv[0]), __uint_as_float(pv[2])), u1 = rs32(__uint_as_float(pv[1]), __uint_as_float(pv[3]));
    const float t0 = rs16(u0, u0), t1 = rs16(u1, u1);
    __builtin_amdgcn_raw_buffer_store_b32(cvt_pk(t0, t1), rout, oo, out_off, 0);
  }
}

}

extern "C" void kernel_launch(void* const* d_in, const int* in_sizes, int n_in, void* d_out, int out_size, void* d_ws,
                              size_t ws_size, hipStream_t stream) {
  (void)in_sizes; (void)n_in; (void)out_size; (void)ws_size;
  (void)hipFuncSetAttribute((const void*)mha_fused, hipFuncAttributeMaxDynamicSharedMemorySize, L_BYTES);
  hipLaunchKernelGGL(mha_fused, dim3(NWG), dim3(NTHR), L_BYTES, stream, (const unsigned short*)d_in[0], (const unsigned short*)d_in[1],
                     (const unsigned short*)d_in[2], (const unsigned short*)d_in[3], (const unsigned short*)d_in[4],
                     (unsigned short*)d_out, (unsigned char*)d_ws);
}
```
